```python
import torch

def _softmax(x, axis=-1):
    return torch.softmax(x, dim=axis)
def _gelu(x):
    return torch.nn.functional.gelu(x, approximate="tanh")
def _silu(x):
    return torch.nn.functional.silu(x)
def _sigmoid(x):
    return torch.sigmoid(x)
def _layernorm(x, axis=-1, eps=1e-5):
    m = x.mean(dim=axis, keepdim=True)
    v = x.var(dim=axis, keepdim=True, unbiased=False)
    return (x - m) / torch.sqrt(v + eps)
def _scatter_add(tgt, idx, src, axis=0):
    return tgt.clone().index_add_(axis, idx, src)
def _segment_sum(src, seg, n):
    out = torch.zeros((n,) + src.shape[1:], dtype=src.dtype)
    return out.index_add_(0, seg, src)
def _pad(x, pw):
    flat = tuple(v for pair in reversed(pw) for v in pair)
    return torch.nn.functional.pad(x, flat)

kernel_name = 'gated_mix__ssm_conv_h3__compressed_kv_cla__prompt'

def setup_inputs():

    _w = (lambda a: (lambda t: t.to(torch.bfloat16) if t.is_floating_point() else t)(torch.as_tensor(a)))
    def _A_setup():
        B, S, D = 1, 2048, 512
        ks = 4
        return {
            "q": _w((lambda *s: torch.randn(*s, dtype=torch.bfloat16) * 0.05)(B, S, D)),
            "k": _w((lambda *s: torch.randn(*s, dtype=torch.bfloat16) * 0.4)(B, S, D)),
            "v": _w((lambda *s: torch.randn(*s, dtype=torch.bfloat16) * 0.4)(B, S, D)),
            "shift_k": _w((lambda *s: torch.randn(*s, dtype=torch.bfloat16) * 0.4)(D, ks) * 0.5),
            "ssm_k": _w((lambda *s: torch.randn(*s, dtype=torch.bfloat16) * 0.8)(D, S) * (1.0 / S ** 0.5)),
            "ssm_D": _w((lambda *s: torch.randn(*s, dtype=torch.bfloat16) * 0.4)(D)),
        }
    _A_in = _A_setup()

    def _B_setup():
        B, H, Sq, Sk, D, M = 1, 32, 2048, 2048, 128, 512
        Dc = 4 * D
        return {
            "x": _w((lambda *s: torch.randn(*s, dtype=torch.bfloat16) * 0.05)(B, Sq, M)),
            "c_kv": _w((lambda *s: torch.randn(*s, dtype=torch.bfloat16) * 0.4)(B, Sk, Dc)),
            "w_q": _w((lambda *s: torch.randn(*s, dtype=torch.bfloat16) * 6.4)(M, H, D) * (M ** -0.5)),
            "w_uk": _w((lambda *s: torch.randn(*s, dtype=torch.bfloat16) * 6.4)(Dc, H, D) * (Dc ** -0.5)),
            "w_uv": _w((lambda *s: torch.randn(*s, dtype=torch.bfloat16) * 0.4)(Dc, H, D) * (Dc ** -0.5)),
            "w_o": _w((lambda *s: torch.randn(*s, dtype=torch.bfloat16) * 0.4)(H, D, M) * ((H * D) ** -0.5)),
        }
    _B_in = _B_setup()

    return {
        "A_q": _A_in["q"],
        "A_k": _A_in["k"],
        "A_v": _A_in["v"],
        "A_shift_k": _A_in["shift_k"],
        "A_ssm_k": _A_in["ssm_k"],
        "A_ssm_D": _A_in["ssm_D"],
        "B_x": _B_in["x"],
        "B_c_kv": _B_in["c_kv"],
        "B_w_q": _B_in["w_q"],
        "B_w_uk": _B_in["w_uk"],
        "B_w_uv": _B_in["w_uv"],
        "B_w_o": _B_in["w_o"],
        "gate_w": _w((lambda *s: torch.randn(*s, dtype=torch.bfloat16) * 0.05)(512) * 16.0),
    }

def reference(A_q, A_k, A_v, A_shift_k, A_ssm_k, A_ssm_D, B_x, B_c_kv, B_w_q, B_w_uk, B_w_uv, B_w_o, gate_w):
    def _A_ref(q, k, v, shift_k, ssm_k, ssm_D):
        B, S, D = q.shape
        ks = shift_k.shape[-1]
        k_pad = _pad(k, ((0, 0), (ks - 1, 0), (0, 0)))
        k_shift = torch.zeros_like(k)
        for t in range(ks):
            k_shift = k_shift + k_pad[:, t:t + S, :] * shift_k[:, ks - 1 - t][None, None, :]
        kv = k_shift * v
        L = 2 * S
        kv_pad = _pad(kv, ((0, 0), (0, S), (0, 0)))
        kf_pad = _pad(ssm_k, ((0, 0), (0, S)))
        kv_f = (lambda x, axes=(-2,-1): torch.fft.rfft2(x.float(), dim=axes))(kv_pad[:, None, :, :], axes=(1, 2))
        kf_f = (lambda x, axes=(-2,-1): torch.fft.rfft2(x.float(), dim=axes))(kf_pad[:, None, :], axes=(1, 2))
        kf_f = (lambda x, *d: x.permute(*d))(kf_f, 1, 2, 0)
        y_f = kv_f * kf_f[None, :, :, :]
        y = (lambda x, s, axes=(-2,-1): torch.fft.irfft2(x, s=s, dim=axes).to(torch.bfloat16))(y_f, (1, L), axes=(1, 2))[:, 0, :S, :]
        y = y + kv * ssm_D[None, None, :]
        return q * y

    def _B_ref(x, c_kv, w_q, w_uk, w_uv, w_o):
        B, Sq, M = x.shape[0], x.shape[1], x.shape[2]
        Sk = c_kv.shape[1]
        H, D = w_q.shape[1], w_q.shape[2]
        q = torch.einsum("bqm,mhd->bhqd", x, w_q)
        k = torch.einsum("bkc,chd->bhkd", c_kv, w_uk)
        v = torch.einsum("bkc,chd->bhkd", c_kv, w_uv)
        scores = torch.einsum("bhqd,bhkd->bhqk", q, k) * (D ** -0.5)
        off = Sk - Sq
        qi = torch.arange(Sq)[:, None]
        kj = torch.arange(Sk)[None, :]
        cmask = kj <= (qi + off)
        scores = torch.where(cmask[None, None], scores, -1e9)
        p = _softmax(scores, axis=-1)
        ctx = torch.einsum("bhqk,bhkd->bhqd", p, v)
        return torch.einsum("bhqd,hdm->bqm", ctx, w_o)

    x = A_q
    g = _sigmoid((x * gate_w).sum(-1, keepdims=True))
    a = _A_ref(x, A_k, A_v, A_shift_k, A_ssm_k, A_ssm_D)
    b = _B_ref(x, B_c_kv, B_w_q, B_w_uk, B_w_uv, B_w_o)
    return g * a + (1 - g) * b
```

```cpp
#include <hip/hip_runtime.h>
#include <hip/hip_bf16.h>
#include <stdint.h>
#include <utility>

typedef short bf16x8 __attribute__((ext_vector_type(8)));
typedef short s16x4 __attribute__((ext_vector_type(4)));
typedef float f32x2 __attribute__((ext_vector_type(2)));
typedef float f32x4 __attribute__((ext_vector_type(4)));
typedef unsigned u32x2 __attribute__((ext_vector_type(2)));
typedef unsigned u32x4 __attribute__((ext_vector_type(4)));
typedef __bf16 bf16x2 __attribute__((ext_vector_type(2)));
typedef __attribute__((address_space(3))) char* lds_ptr;
typedef __attribute__((address_space(3))) const char* lds_cptr;
typedef short v4i16_t __attribute__((ext_vector_type(4)));

#define DI __device__ __forceinline__
#define MFMA16(a, b, c) __builtin_amdgcn_mfma_f32_16x16x32_bf16(a, b, c, 0, 0, 0)
#define SBAR() __builtin_amdgcn_sched_barrier(0)
#define PIN(x) asm volatile("" : "+v"(x))
#define WAIT_BAR(N) asm volatile("s_waitcnt vmcnt(" #N ") lgkmcnt(0)\n\ts_barrier" ::: "memory")

constexpr int SEQ = 2048, DM = 512, NH = 32, HD = 128;
constexpr float QSCALE = 0.12751743082f;

constexpr size_t WS_KF = 0;
constexpr size_t WS_VF = 16u << 20;
constexpr size_t WS_CF = 32u << 20;
constexpr size_t WS_WO = 48u << 20;
constexpr size_t WS_KVT = 52u << 20;
constexpr size_t WS_QT = 54u << 20;
constexpr size_t WS_AT = 56u << 20;
constexpr size_t WS_G = 58u << 20;
constexpr size_t WS_QS = 60u << 20;
constexpr size_t WS_END = 68u << 20;

DI unsigned cvtpk(float lo, float hi) { f32x2 v = {lo, hi}; bf16x2 b = __builtin_convertvector(v, bf16x2); return __builtin_bit_cast(unsigned, b); }
DI float bfr(float x) { return __uint_as_float(cvtpk(x, 0.f) << 16); }
DI float bflo(unsigned w) { return __uint_as_float(w << 16); }
DI float bfhi(unsigned w) { return __uint_as_float(w & 0xffff0000u); }
DI unsigned rfl(unsigned x) { return __builtin_amdgcn_readfirstlane(x); }
DI __amdgpu_buffer_rsrc_t mk_srd(const void* p, unsigned bytes) { return __builtin_amdgcn_make_buffer_rsrc((void*)p, (short)0, (int)bytes, 0x00020000); }
DI bf16x8 lds_frag(lds_cptr p) { return *(const __attribute__((address_space(3))) bf16x8*)p; }
DI s16x4 lds_tr(lds_cptr p) { return __builtin_bit_cast(s16x4, __builtin_amdgcn_ds_read_tr16_b64_v4i16((__attribute__((address_space(3))) v4i16_t*)p)); }
DI bf16x8 cat8(s16x4 lo, s16x4 hi) { return (bf16x8){lo[0], lo[1], lo[2], lo[3], hi[0], hi[1], hi[2], hi[3]}; }
DI bf16x8 pack8(const f32x4& a, const f32x4& b) { u32x4 w = {cvtpk(a[0], a[1]), cvtpk(a[2], a[3]), cvtpk(b[0], b[1]), cvtpk(b[2], b[3])}; return __builtin_bit_cast(bf16x8, w); }

DI void dma1(unsigned lds, unsigned voff, __amdgpu_buffer_rsrc_t srd, unsigned soff) {
  asm volatile("s_mov_b32 m0, %0\n\ts_nop 0\n\tbuffer_load_dwordx4 %1, %2, %3 offen lds" :: "s"(lds), "v"(voff), "s"(srd), "s"(soff) : "memory");
}
DI void dma4(unsigned lds, unsigned voff, __amdgpu_buffer_rsrc_t srd, unsigned soff) {
  asm volatile("s_mov_b32 m0, %0\n\ts_nop 0\n\tbuffer_load_dwordx4 %1, %2, %3 offen lds\n\tbuffer_load_dwordx4 %1, %2, %3 offen offset:1024 lds\n\t"
               "buffer_load_dwordx4 %1, %2, %3 offen offset:2048 lds\n\tbuffer_load_dwordx4 %1, %2, %3 offen offset:3072 lds"
               :: "s"(lds), "v"(voff), "s"(srd), "s"(soff) : "memory");
}

constexpr unsigned PJ_SLOT = 49152, PJ_W = 32768;
struct Proj {
  unsigned ldsb, wid;
  unsigned va[2], vw;
  lds_cptr ra[2];
  lds_cptr rw[4];
};
DI void proj_init(Proj& P, lds_cptr lds, unsigned lane, unsigned wid) {
  P.ldsb = (unsigned)(size_t)lds; P.wid = wid;
  const unsigned rip = lane >> 3, pos = lane & 7;
#pragma unroll
  for (int par = 0; par < 2; ++par) P.va[par] = rip * 1024 + 16 * (pos ^ ((4 * par + (rip >> 1)) & 7));
  const unsigned sig = ((rip >> 1) & 1) | ((wid & 1) << 1);
  P.vw = rip * 8192 + 16 * (pos ^ (2 * sig));
  const unsigned r = lane & 15, g = lane >> 4;
#pragma unroll
  for (int i = 0; i < 2; ++i) P.ra[i] = lds + (wid * 32 + r) * 128 + 16 * ((4 * i + g) ^ ((r >> 1) & 7));
  const unsigned q = (lane & 15) >> 2, p = lane & 3, sg = ((q >> 1) & 1) | ((g & 1) << 1);
#pragma unroll
  for (int n = 0; n < 4; ++n) P.rw[n] = lds + PJ_W + (8 * g + q) * 128 + 32 * (n ^ sg) + 16 * (p >> 1) + 8 * (p & 1);
}
DI void proj_dma_act(const Proj& P, __amdgpu_buffer_rsrc_t srd, unsigned row0, int c, unsigned slot) {
#pragma unroll
  for (int i = 0; i < 4; ++i) dma1(P.ldsb + slot * PJ_SLOT + P.wid * 4096 + i * 1024, P.va[i & 1], srd, (row0 + 8 * i) * 1024 + c * 128);
}
DI void proj_dma_w(const Proj& P, __amdgpu_buffer_rsrc_t srd, int c, unsigned slot) {
#pragma unroll
  for (int hf = 0; hf < 2; ++hf) dma1(P.ldsb + slot * PJ_SLOT + PJ_W + hf * 8192 + P.wid * 1024, P.vw, srd, (64 * c + 8 * P.wid) * 8192 + hf * 128);
}
DI bf16x8 proj_wfrag(const Proj& P, int slot, int s, int nb) {
  lds_cptr p = P.rw[nb & 3] + slot * PJ_SLOT + (nb >> 2) * 8192 + s * 4096;
  return cat8(lds_tr(p), lds_tr(p + 512));
}
DI bf16x8 proj_afrag(const Proj& P, int slot, int s, int tb) { return lds_frag(P.ra[s] + slot * PJ_SLOT + tb * 2048); }

constexpr int K1_LDS = 3 * PJ_SLOT;
struct K1Ctx { Proj P; __amdgpu_buffer_rsrc_t sC, sK, sV; bf16x8 act[2][16]; f32x4 acc[8][2]; char* Kd; char* Vd; unsigned lane; };

template <int C> DI void k1_chunk(K1Ctx& X) {
  constexpr int slot = C % 3;
  if constexpr (C < 7) WAIT_BAR(6); else if constexpr (C == 7) WAIT_BAR(2); else if constexpr (C == 8 || C == 9) WAIT_BAR(10); else if constexpr (C < 15) WAIT_BAR(2); else WAIT_BAR(0);
  constexpr int N = C + 2;
  if constexpr (N < 8) { proj_dma_act(X.P, X.sC, X.P.wid * 32, N, N % 3); proj_dma_w(X.P, X.sK, N, N % 3); }
  else if constexpr (N < 16) proj_dma_w(X.P, X.sV, N - 8, N % 3);
#pragma unroll
  for (int s = 0; s < 2; ++s) {
    if constexpr (C < 8) { X.act[0][2 * C + s] = proj_afrag(X.P, slot, s, 0); X.act[1][2 * C + s] = proj_afrag(X.P, slot, s, 1); }
#pragma unroll
    for (int nb = 0; nb < 8; ++nb) {
      const bf16x8 w = proj_wfrag(X.P, slot, s, nb);
#pragma unroll
      for (int tb = 0; tb < 2; ++tb) {
        if constexpr (C < 8) X.acc[nb][tb] = MFMA16(w, X.act[tb][2 * C + s], X.acc[nb][tb]);
        else X.acc[nb][tb] = MFMA16(X.act[tb][2 * (C - 8) + s], w, X.acc[nb][tb]);
      }
    }
  }
  if constexpr (C == 7) {
#pragma unroll
    for (int tb = 0; tb < 2; ++tb)
#pragma unroll
      for (int ds = 0; ds < 4; ++ds) *(bf16x8*)(X.Kd + (tb * 4 + ds) * 1024 + X.lane * 16) = pack8(X.acc[2 * ds][tb], X.acc[2 * ds + 1][tb]);
#pragma unroll
    for (int nb = 0; nb < 8; ++nb) { X.acc[nb][0] = f32x4{}; X.acc[nb][1] = f32x4{}; }
    asm volatile("" ::: "memory");
  }
  if constexpr (C == 15) {
#pragma unroll
    for (int nb = 0; nb < 8; ++nb) *(bf16x8*)(X.Vd + nb * 1024 + X.lane * 16) = pack8(X.acc[nb][0], X.acc[nb][1]);
  }
}
template <int... Cs> DI void k1_all(K1Ctx& X, std::integer_sequence<int, Cs...>) { (k1_chunk<Cs>(X), ...); }

__global__ __launch_bounds__(512, 2) void k1_kvproj(const char* __restrict__ ckv, const char* __restrict__ wuk, const char* __restrict__ wuv, char* __restrict__ ws) {
  extern __shared__ __attribute__((aligned(16))) char lds_[];
  const unsigned tid = threadIdx.x, lane = tid & 63, wid = rfl(tid >> 6);
  const unsigned xcd = blockIdx.x & 7, j = blockIdx.x >> 3, h = xcd * 4 + (j & 3), p = j >> 2;
  K1Ctx X;
  proj_init(X.P, (lds_cptr)lds_, lane, wid);
  X.lane = lane;
  X.sC = mk_srd(ckv + (size_t)p * 256 * 1024, 256 * 1024);
  X.sK = mk_srd(wuk + h * 256, 512 * 8192 - h * 256);
  X.sV = mk_srd(wuv + h * 256, 512 * 8192 - h * 256);
  X.Kd = ws + WS_KF + ((size_t)h * 128 + p * 16 + wid * 2) * 4096;
  X.Vd = ws + WS_VF + ((size_t)h * 64 + p * 8 + wid) * 8192;
#pragma unroll
  for (int nb = 0; nb < 8; ++nb) { X.acc[nb][0] = f32x4{}; X.acc[nb][1] = f32x4{}; }
  proj_dma_act(X.P, X.sC, wid * 32, 0, 0); proj_dma_w(X.P, X.sK, 0, 0);
  proj_dma_act(X.P, X.sC, wid * 32, 1, 1); proj_dma_w(X.P, X.sK, 1, 1);
  k1_all(X, std::make_integer_sequence<int, 16>{});
}

__global__ __launch_bounds__(256) void k0_prep(const unsigned short* __restrict__ aq, const unsigned short* __restrict__ ak, const unsigned short* __restrict__ av,
                                               const unsigned short* __restrict__ shk, const unsigned short* __restrict__ gw, const unsigned short* __restrict__ wo,
                                               char* __restrict__ ws) {
  __shared__ __attribute__((aligned(16))) unsigned short tile[2][64][72];
  __shared__ __attribute__((aligned(16))) char wbuf[32 * 528];
  const unsigned tid = threadIdx.x, lane = tid & 63, wid = tid >> 6, bid = blockIdx.x;
  const int t0 = (bid >> 3) * 64, d0 = (bid & 7) * 64;
  {
    const int tr = tid >> 3, dc = (tid & 7) * 8;
    u32x4 wk[4];
#pragma unroll
    for (int i = 0; i < 4; ++i) wk[i] = *(const u32x4*)(shk + (d0 + dc) * 4 + i * 8);
#pragma unroll
    for (int rep = 0; rep < 2; ++rep) {
      const int tl = tr + rep * 32, t = t0 + tl;
      u32x4 kr[4];
#pragma unroll
      for (int i = 0; i < 4; ++i) { const int ts = t - 3 + i; kr[i] = ts >= 0 ? *(const u32x4*)(ak + (size_t)ts * DM + d0 + dc) : u32x4{0u, 0u, 0u, 0u}; }
      const u32x4 vv = *(const u32x4*)(av + (size_t)t * DM + d0 + dc), qq = *(const u32x4*)(aq + (size_t)t * DM + d0 + dc);
#pragma unroll
      for (int e = 0; e < 8; ++e) {
        const unsigned w01 = wk[e >> 1][(e & 1) * 2], w23 = wk[e >> 1][(e & 1) * 2 + 1];
        const float w[4] = {bflo(w01), bfhi(w01), bflo(w23), bfhi(w23)};
        float s = 0.f;
#pragma unroll
        for (int i = 0; i < 4; ++i) { const float kx = (e & 1) ? bfhi(kr[i][e >> 1]) : bflo(kr[i][e >> 1]); s = bfr(s + bfr(kx * w[3 - i])); }
        const float vx = (e & 1) ? bfhi(vv[e >> 1]) : bflo(vv[e >> 1]);
        tile[0][dc + e][tl] = (unsigned short)(cvtpk(s * vx, 0.f) & 0xffffu);
        tile[1][dc + e][tl] = (unsigned short)((e & 1) ? (qq[e >> 1] >> 16) : (qq[e >> 1] & 0xffffu));
      }
    }
  }
  {
    const unsigned hds = bid >> 1, mh = bid & 1;
    const unsigned short* src = wo + (size_t)hds * 32 * DM + mh * 256;
#pragma unroll
    for (int i = 0; i < 4; ++i) { const unsigned idx = tid + i * 256, row = idx >> 5, ch = idx & 31; *(u32x4*)(wbuf + row * 528 + ch * 16) = *(const u32x4*)(src + (size_t)row * DM + ch * 8); }
  }
  {
#pragma unroll
    for (int rep = 0; rep < 2; ++rep) {
      const int t = bid * 8 + wid * 2 + rep;
      const u32x4 xv = *(const u32x4*)(aq + (size_t)t * DM + lane * 8), gv = *(const u32x4*)(gw + lane * 8);
      float s = 0.f;
#pragma unroll
      for (int e = 0; e < 4; ++e) { s += bfr(bflo(xv[e]) * bflo(gv[e])); s += bfr(bfhi(xv[e]) * bfhi(gv[e])); }
#pragma unroll
      for (int o = 32; o >= 1; o >>= 1) s += __shfl_xor(s, o);
      if (lane == 0) { const float z = bfr(s), g = bfr(1.f / (1.f + __expf(-z))); float* G = (float*)(ws + WS_G); G[t] = g; G[SEQ + t] = bfr(1.f - g); }
    }
  }
  __syncthreads();
  {
    const int dr = tid >> 3, tc = (tid & 7) * 8;
#pragma unroll
    for (int rep = 0; rep < 2; ++rep) {
      const int dl = dr + rep * 32;
      *(u32x4*)(ws + WS_KVT + ((size_t)(d0 + dl) * SEQ + t0 + tc) * 2) = *(const u32x4*)&tile[0][dl][tc];
      *(u32x4*)(ws + WS_QT + ((size_t)(d0 + dl) * SEQ + t0 + tc) * 2) = *(const u32x4*)&tile[1][dl][tc];
    }
  }
  {
    const unsigned hds = bid >> 1, mh = bid & 1, g = lane >> 4, q = (lane & 15) >> 2, p = lane & 3;
#pragma unroll
    for (int i = 0; i < 4; ++i) {
      const unsigned mb = wid * 4 + i;
      lds_cptr a = (lds_cptr)wbuf + (4 * g + q) * 528 + mb * 32 + p * 8;
      const bf16x8 f = cat8(lds_tr(a), lds_tr(a + 16 * 528));
      *(bf16x8*)(ws + WS_WO + ((size_t)hds * 32 + mh * 16 + mb) * 1024 + lane * 16) = f;
    }
  }
}

constexpr unsigned CV_HR = 4608, CV_CS = 4160, CV_CH = CV_HR + 8 * CV_CS;
constexpr int CONV_LDS = 2 * CV_CH;
__global__ __launch_bounds__(512, 2) void k_conv(const unsigned short* __restrict__ ssk, const unsigned short* __restrict__ ssd, char* __restrict__ ws) {
  extern __shared__ __attribute__((aligned(16))) char lds_[];
  const unsigned tid = threadIdx.x, lane = tid & 63, wid = rfl(tid >> 6);
  const unsigned ch0 = blockIdx.x * 2;
  {
    const unsigned cl = tid >> 8, i = tid & 255;
    char* base = lds_ + cl * CV_CH;
    const u32x4 hv = *(const u32x4*)(ssk + (size_t)(ch0 + cl) * SEQ + i * 8);
    u32x4 rv;
#pragma unroll
    for (int e = 0; e < 4; ++e) rv[e] = (hv[3 - e] >> 16) | (hv[3 - e] << 16);
    *(u32x4*)(base + (255 - i) * 16) = rv;
    if (i < 32) *(u32x4*)(base + 4096 + i * 16) = u32x4{0u, 0u, 0u, 0u};
    const unsigned short* u = (const unsigned short*)(ws + WS_KVT) + (size_t)(ch0 + cl) * SEQ;
    const u32x4 a = *(const u32x4*)(u + i * 8);
    const u32x4 b = i < 255 ? *(const u32x4*)(u + i * 8 + 8) : u32x4{0u, 0u, 0u, 0u};
    const unsigned w[8] = {a[0], a[1], a[2], a[3], b[0], b[1], b[2], b[3]};
    char* cp = base + CV_HR;
#pragma unroll
    for (int c = 0; c < 8; ++c) {
      u32x4 o;
#pragma unroll
      for (int e = 0; e < 4; ++e) o[e] = (c & 1) ? __builtin_amdgcn_alignbit(w[(c >> 1) + e + 1], w[(c >> 1) + e], 16) : w[(c >> 1) + e];
      *(u32x4*)(cp + c * CV_CS + (i + 2) * 16) = o;
    }
    if (i < 2) {
      const u32x4 f = *(const u32x4*)u;
      const unsigned z[12] = {0u, 0u, 0u, 0u, f[0], f[1], f[2], f[3], 0u, 0u, 0u, 0u};
#pragma unroll
      for (int c = 0; c < 8; ++c) {
        u32x4 o = {0u, 0u, 0u, 0u};
        if (i == 1) {
#pragma unroll
          for (int e = 0; e < 4; ++e) o[e] = (c & 1) ? __builtin_amdgcn_alignbit(z[(c >> 1) + e + 1], z[(c >> 1) + e], 16) : z[(c >> 1) + e];
        }
        *(u32x4*)(cp + c * CV_CS + i * 16) = o;
      }
    }
  }
  __syncthreads();
  const unsigned cl = wid >> 2, rr = wid & 3, r = lane & 15, g = lane >> 4;
  lds_cptr hb = (lds_cptr)lds_ + cl * CV_CH + (2032 - 16 * r + 8 * g) * 2;
  lds_cptr ub = (lds_cptr)lds_ + cl * CV_CH + CV_HR + ((r + 1) & 7) * CV_CS + (8 * g + ((r + 1) & ~7u)) * 2;
  f32x4 acc[8];
#pragma unroll
  for (int R = 0; R < 8; ++R) acc[R] = f32x4{};
#pragma unroll
  for (int e = 0; e < 2; ++e) {
    const unsigned rho = rr * 2 + e;
    bf16x8 A[8], B[8];
#pragma unroll
    for (int k = 0; k < 8; ++k) { A[k] = lds_frag(hb + (int)rho * 64 - k * 512); B[k] = lds_frag(ub + rho * 64 + k * 512); }
#pragma unroll
    for (int R = 0; R < 8; ++R)
#pragma unroll
      for (int m = 0; m <= R; ++m) acc[R] = MFMA16(B[m], A[R - m], acc[R]);
  }
  __syncthreads();
  float* red = (float*)lds_;
#pragma unroll
  for (int R = 0; R < 8; ++R) *(f32x4*)(red + (cl * 4 + rr) * 2048 + 256 * R + 16 * r + 4 * g) = acc[R];
  __syncthreads();
  {
    const unsigned c2 = tid >> 8, t = (tid & 255) * 8, ch = ch0 + c2;
    float y[8];
#pragma unroll
    for (int e = 0; e < 8; ++e) y[e] = 0.f;
#pragma unroll
    for (int w = 0; w < 4; ++w) {
      const f32x4 p0 = *(const f32x4*)(red + (c2 * 4 + w) * 2048 + t), p1 = *(const f32x4*)(red + (c2 * 4 + w) * 2048 + t + 4);
#pragma unroll
      for (int e = 0; e < 4; ++e) { y[e] += p0[e]; y[4 + e] += p1[e]; }
    }
    const u32x4 kv = *(const u32x4*)(ws + WS_KVT + ((size_t)ch * SEQ + t) * 2), qv = *(const u32x4*)(ws + WS_QT + ((size_t)ch * SEQ + t) * 2);
    const float* G = (const float*)(ws + WS_G);
    const f32x4 g0 = *(const f32x4*)(G + t), g1 = *(const f32x4*)(G + t + 4);
    const float Dv = __uint_as_float((unsigned)ssd[ch] << 16);
    float o[8];
#pragma unroll
    for (int e = 0; e < 8; ++e) {
      const float kx = (e & 1) ? bfhi(kv[e >> 1]) : bflo(kv[e >> 1]), qx = (e & 1) ? bfhi(qv[e >> 1]) : bflo(qv[e >> 1]);
      const float y2 = bfr(bfr(y[e]) + bfr(kx * Dv));
      o[e] = (e < 4 ? g0[e & 3] : g1[e & 3]) * bfr(qx * y2);
    }
    *(u32x4*)(ws + WS_AT + ((size_t)ch * SEQ + t) * 2) = u32x4{cvtpk(o[0], o[1]), cvtpk(o[2], o[3]), cvtpk(o[4], o[5]), cvtpk(o[6], o[7])};
  }
}

constexpr unsigned A_K0 = 98304, A_K1 = 0, A_V = 32768, A_SP = 131072;
constexpr int K2_LDS = 163840;
DI unsigned kslot(unsigned gs) { return (gs & 1) ? A_K1 : A_K0; }
DI unsigned vslot(unsigned gs) { return A_V + (gs & 1) * 32768; }

struct Att {
  lds_cptr lds; unsigned ldsb, lane, wid, rg, half, vlin;
  __amdgpu_buffer_rsrc_t sK, sV;
  unsigned n0;
  bf16x8 Q[2][4];
  f32x4 O[8][2], L[2], NM[2];
  bf16x8 Pf[2][2];
  bf16x8 ones;
  float m[2];
};
DI void att_dma_k(const Att& A, unsigned gs) { const unsigned ki = gs < A.n0 ? gs : gs - A.n0; dma4(A.ldsb + kslot(gs) + A.wid * 4096, A.vlin, A.sK, ki * 32768 + A.wid * 4096); }
DI void att_dma_v(const Att& A, unsigned gs) { const unsigned ki = gs < A.n0 ? gs : gs - A.n0; dma4(A.ldsb + vslot(gs) + A.wid * 4096, A.vlin, A.sV, ki * 32768 + A.wid * 4096); }
DI void att_b1(const Att& A, unsigned gs) {
  if (gs + 1 < 17) { WAIT_BAR(4); } else { WAIT_BAR(0); }
  if (gs + 2 < 17) att_dma_k(A, gs + 2);
}
DI void att_b2(const Att& A, unsigned gs) {
  if (gs + 2 < 17) { WAIT_BAR(4); } else { WAIT_BAR(0); }
  if (gs + 1 < 17) att_dma_v(A, gs + 1);
}
DI void att_mask(const Att& A, f32x4 (&S)[4][2]) {
  const int row = A.rg * 32 + (A.lane & 15), key = A.half * 64 + (A.lane >> 4) * 4;
#pragma unroll
  for (int kb = 0; kb < 4; ++kb)
#pragma unroll
    for (int qb = 0; qb < 2; ++qb)
#pragma unroll
      for (int r = 0; r < 4; ++r) if (key + 16 * kb + r > row + 16 * qb) S[kb][qb][r] = -__builtin_inff();
}
DI float ex2(float x) { return __builtin_amdgcn_exp2f(x); }

template <bool MASK> DI void att_step(Att& A, f32x4 (&C)[4][2], f32x4 (&P)[4][2], unsigned gs) {
  lds_cptr kp = A.lds + kslot(gs) + A.half * 16384 + A.lane * 16;
  lds_cptr vp = A.lds + vslot(gs - 1) + A.half * 16384 + A.lane * 16;
  u32x4 pw[2][2];
  bf16x8 kf[16], vf[16];
  kf[0] = lds_frag(kp + 0 * 4096 + 0); kf[1] = lds_frag(kp + 1 * 4096 + 0); kf[2] = lds_frag(kp + 2 * 4096 + 0);
  SBAR();
#pragma unroll
  for (int f = 0; f < 16; ++f) {
    const int ds = f >> 2, kb = f & 3;
    if (f + 3 < 16) kf[f + 3] = lds_frag(kp + ((f + 3) & 3) * 4096 + ((f + 3) >> 2) * 1024);
    else vf[f + 3 - 16] = lds_frag(vp + (f + 3 - 16) * 1024);
    C[kb][0] = MFMA16(kf[f], A.Q[0][ds], ds == 0 ? A.NM[0] : C[kb][0]);
    C[kb][1] = MFMA16(kf[f], A.Q[1][ds], ds == 0 ? A.NM[1] : C[kb][1]);
    { const int qb = f >> 3, k2 = 2 + ((f >> 2) & 1), r = f & 3; P[k2][qb][r] = ex2(P[k2][qb][r]); PIN(P[k2][qb]); }
    if (f < 8) { const int qb = f >> 2, w = f & 3; pw[0][qb][w] = cvtpk(P[w >> 1][qb][2 * (w & 1)], P[w >> 1][qb][2 * (w & 1) + 1]); PIN(pw[0][qb]); }
    else if (f < 12) { const int w = f & 3; pw[1][0][w] = cvtpk(P[2 + (w >> 1)][0][2 * (w & 1)], P[2 + (w >> 1)][0][2 * (w & 1) + 1]); PIN(pw[1][0]); }
    SBAR();
  }
  att_b1(A, gs);
  if constexpr (MASK) att_mask(A, C);
#pragma unroll
  for (int f = 0; f < 16; ++f) {
    const int ks = f >> 3, db = f & 7;
    if (f + 3 < 16) vf[f + 3] = lds_frag(vp + (((f + 3) >> 3) * 8 + ((f + 3) & 7)) * 1024);
    const bf16x8 p0 = __builtin_bit_cast(bf16x8, pw[ks][0]), p1 = __builtin_bit_cast(bf16x8, pw[ks][1]);
    A.O[db][0] = MFMA16(vf[f], p0, A.O[db][0]);
    A.O[db][1] = MFMA16(vf[f], p1, A.O[db][1]);
    if (db == 0) { A.L[0] = MFMA16(A.ones, p0, A.L[0]); A.L[1] = MFMA16(A.ones, p1, A.L[1]); }
    if (f < 4) { const int w = f; pw[1][1][w] = cvtpk(P[2 + (w >> 1)][1][2 * (w & 1)], P[2 + (w >> 1)][1][2 * (w & 1) + 1]); PIN(pw[1][1]); }
    { const int k2 = (f >> 2) & 1, qb = f >> 3, r = f & 3; C[k2][qb][r] = ex2(C[k2][qb][r]); PIN(C[k2][qb]); }
    SBAR();
  }
  att_b2(A, gs);
}

DI void att_first(Att& A, f32x4 (&C)[4][2], unsigned gs, bool mask) {
  lds_cptr kp = A.lds + kslot(gs) + A.half * 16384 + A.lane * 16;
#pragma unroll
  for (int f = 0; f < 16; ++f) {
    const int ds = f >> 2, kb = f & 3;
    const bf16x8 k = lds_frag(kp + kb * 4096 + ds * 1024);
    C[kb][0] = MFMA16(k, A.Q[0][ds], ds == 0 ? f32x4{} : C[kb][0]);
    C[kb][1] = MFMA16(k, A.Q[1][ds], ds == 0 ? f32x4{} : C[kb][1]);
  }
  att_b1(A, gs);
  if (mask) att_mask(A, C);
#pragma unroll
  for (int qb = 0; qb < 2; ++qb) {
    float mx = C[0][qb][0];
#pragma unroll
    for (int kb = 0; kb < 4; ++kb)
#pragma unroll
      for (int r = 0; r < 4; ++r) mx = fmaxf(mx, C[kb][qb][r]);
    mx = fmaxf(mx, __shfl_xor(mx, 16)); mx = fmaxf(mx, __shfl_xor(mx, 32));
    if (mx == -__builtin_inff()) mx = 0.f;
    A.m[qb] = mx; A.NM[qb] = f32x4{-mx, -mx, -mx, -mx};
#pragma unroll
    for (int kb = 0; kb < 4; ++kb)
#pragma unroll
      for (int r = 0; r < 4; ++r) { C[kb][qb][r] -= mx; if (kb < 2) C[kb][qb][r] = ex2(C[kb][qb][r]); }
    A.L[qb] = f32x4{};
#pragma unroll
    for (int db = 0; db < 8; ++db) A.O[db][qb] = f32x4{};
  }
  att_b2(A, gs);
}

DI void att_drain(Att& A, f32x4 (&P)[4][2], unsigned gs_last) {
  lds_cptr vp = A.lds + vslot(gs_last) + A.half * 16384 + A.lane * 16;
#pragma unroll
  for (int qb = 0; qb < 2; ++qb) {
#pragma unroll
    for (int kb = 2; kb < 4; ++kb)
#pragma unroll
      for (int r = 0; r < 4; ++r) P[kb][qb][r] = ex2(P[kb][qb][r]);
    A.Pf[0][qb] = pack8(P[0][qb], P[1][qb]); A.Pf[1][qb] = pack8(P[2][qb], P[3][qb]);
  }
#pragma unroll
  for (int f = 0; f < 16; ++f) {
    const int ks = f >> 3, db = f & 7;
    const bf16x8 v = lds_frag(vp + f * 1024);
    A.O[db][0] = MFMA16(v, A.Pf[ks][0], A.O[db][0]);
    A.O[db][1] = MFMA16(v, A.Pf[ks][1], A.O[db][1]);
    if (db == 0) { A.L[0] = MFMA16(A.ones, A.Pf[ks][0], A.L[0]); A.L[1] = MFMA16(A.ones, A.Pf[ks][1], A.L[1]); }
  }
}

DI void att_finish(Att& A, char* ctx) {
  float* sp = (float*)((char*)A.lds + A_SP);
  const unsigned partner = A.wid ^ 4;
  *(f32x4*)(sp + A.wid * 256 + A.lane * 4) = f32x4{A.m[0], A.L[0][0], A.m[1], A.L[1][0]};
  asm volatile("s_waitcnt lgkmcnt(0)\n\ts_barrier" ::: "memory");
  const f32x4 o = *(const f32x4*)(sp + partner * 256 + A.lane * 4);
  float fac[2];
#pragma unroll
  for (int qb = 0; qb < 2; ++qb) {
    const float m1 = A.m[qb], l1 = A.L[qb][0], m2 = o[2 * qb], l2 = o[2 * qb + 1];
    const float e1 = l1 == 0.f ? -__builtin_inff() : m1, e2 = l2 == 0.f ? -__builtin_inff() : m2;
    const float mm = fmaxf(e1, e2);
    const float f1 = ex2(e1 - mm), f2 = ex2(e2 - mm);
    fac[qb] = f1 / (l1 * f1 + l2 * f2);
  }
  asm volatile("s_waitcnt lgkmcnt(0)\n\ts_barrier" ::: "memory");
  const int keep = A.half * 4, give = 4 - keep;
#pragma unroll
  for (int rnd = 0; rnd < 2; ++rnd) {
#pragma unroll
    for (int i = 0; i < 2; ++i)
#pragma unroll
      for (int qb = 0; qb < 2; ++qb) {
        const f32x4 v = A.half ? A.O[2 * rnd + i][qb] : A.O[4 + 2 * rnd + i][qb];
        *(f32x4*)(sp + A.wid * 1024 + (i * 2 + qb) * 256 + A.lane * 4) = v * fac[qb];
      }
    asm volatile("s_waitcnt lgkmcnt(0)\n\ts_barrier" ::: "memory");
    f32x4 s[2][2];
#pragma unroll
    for (int i = 0; i < 2; ++i)
#pragma unroll
      for (int qb = 0; qb < 2; ++qb) {
        const f32x4 mine = A.half ? A.O[4 + 2 * rnd + i][qb] : A.O[2 * rnd + i][qb];
        s[i][qb] = mine * fac[qb] + *(const f32x4*)(sp + partner * 1024 + (i * 2 + qb) * 256 + A.lane * 4);
      }
#pragma unroll
    for (int qb = 0; qb < 2; ++qb) *(bf16x8*)(ctx + (size_t)qb * (NH * 4096) + (A.half * 2 + rnd) * 1024 + A.lane * 16) = pack8(s[0][qb], s[1][qb]);
    asm volatile("s_waitcnt lgkmcnt(0)\n\ts_barrier" ::: "memory");
  }
  (void)give;
}

struct QCtx { Proj P; __amdgpu_buffer_rsrc_t sX, sW; unsigned row0; f32x4 acc[8][2]; };
template <int C> DI void q_chunk(QCtx& X, const Att& A) {
  constexpr int slot = C % 3;
  if constexpr (C < 6) WAIT_BAR(6); else if constexpr (C == 6) WAIT_BAR(6); else WAIT_BAR(4);
  constexpr int N = C + 2;
  if constexpr (N < 8) { proj_dma_act(X.P, X.sX, X.row0, N, N % 3); proj_dma_w(X.P, X.sW, N, N % 3); }
  if constexpr (C == 6) att_dma_k(A, 0);
#pragma unroll
  for (int s = 0; s < 2; ++s) {
    const bf16x8 a0 = proj_afrag(X.P, slot, s, 0), a1 = proj_afrag(X.P, slot, s, 1);
#pragma unroll
    for (int nb = 0; nb < 8; ++nb) {
      const bf16x8 w = proj_wfrag(X.P, slot, s, nb);
      X.acc[nb][0] = MFMA16(w, a0, X.acc[nb][0]);
      X.acc[nb][1] = MFMA16(w, a1, X.acc[nb][1]);
    }
  }
}
template <int... Cs> DI void q_all(QCtx& X, const Att& A, std::integer_sequence<int, Cs...>) { (q_chunk<Cs>(X, A), ...); }

__global__ __launch_bounds__(512, 2) void k2_attn(const char* __restrict__ xin, const char* __restrict__ wq, char* __restrict__ ws) {
  extern __shared__ __attribute__((aligned(16))) char lds_[];
  const unsigned tid = threadIdx.x, lane = tid & 63, wid = rfl(tid >> 6);
  const unsigned xcd = blockIdx.x & 7, j = blockIdx.x >> 3, h = xcd * 4 + (j & 3), x = j >> 2;
  Att A;
  A.lds = (lds_cptr)lds_; A.ldsb = (unsigned)(size_t)A.lds; A.lane = lane; A.wid = wid; A.rg = wid & 3; A.half = wid >> 2; A.vlin = lane * 16;
  A.sK = mk_srd(ws + WS_KF + (size_t)h * (128 * 4096), 128 * 4096);
  A.sV = mk_srd(ws + WS_VF + (size_t)h * (64 * 8192), 64 * 8192);
  A.n0 = 16 - x;
  A.ones = (bf16x8){0x3f80, 0x3f80, 0x3f80, 0x3f80, 0x3f80, 0x3f80, 0x3f80, 0x3f80};
  char* qs = ws + WS_QS + (size_t)blockIdx.x * 32768;
  {
    QCtx X;
    proj_init(X.P, A.lds, lane, wid);
    X.sX = mk_srd(xin, SEQ * 1024);
    X.sW = mk_srd(wq + h * 256, 512 * 8192 - h * 256);
    X.row0 = (A.half ? x : 15 - x) * 128 + A.rg * 32;
#pragma unroll
    for (int nb = 0; nb < 8; ++nb) { X.acc[nb][0] = f32x4{}; X.acc[nb][1] = f32x4{}; }
    proj_dma_act(X.P, X.sX, X.row0, 0, 0); proj_dma_w(X.P, X.sW, 0, 0);
    proj_dma_act(X.P, X.sX, X.row0, 1, 1); proj_dma_w(X.P, X.sW, 1, 1);
    q_all(X, A, std::make_integer_sequence<int, 8>{});
#pragma unroll
    for (int tb = 0; tb < 2; ++tb)
#pragma unroll
      for (int ds = 0; ds < 4; ++ds) {
        const bf16x8 f = pack8(X.acc[2 * ds][tb] * QSCALE, X.acc[2 * ds + 1][tb] * QSCALE);
        if (A.half) *(bf16x8*)(qs + A.rg * 8192 + (tb * 4 + ds) * 1024 + lane * 16) = f;
        else *(bf16x8*)(lds_ + A_SP + A.rg * 8192 + (tb * 4 + ds) * 1024 + lane * 16) = f;
      }
  }
  asm volatile("s_waitcnt vmcnt(0) lgkmcnt(0)\n\ts_barrier" ::: "memory");
  att_dma_k(A, 1); att_dma_v(A, 0);
#pragma unroll
  for (int qb = 0; qb < 2; ++qb)
#pragma unroll
    for (int ds = 0; ds < 4; ++ds) A.Q[qb][ds] = lds_frag(A.lds + A_SP + A.rg * 8192 + (qb * 4 + ds) * 1024 + lane * 16);
  asm volatile("s_waitcnt lgkmcnt(0)\n\ts_barrier" ::: "memory");
  unsigned gs = 0;
  f32x4 SA[4][2], SB[4][2];
  for (int job = 0; job < 2; ++job) {
    const unsigned blk = job ? x : 15 - x, n = blk + 1;
    if (job) {
#pragma unroll
      for (int qb = 0; qb < 2; ++qb)
#pragma unroll
        for (int ds = 0; ds < 4; ++ds) A.Q[qb][ds] = *(const bf16x8*)(qs + A.rg * 8192 + (qb * 4 + ds) * 1024 + lane * 16);
    }
    att_first(A, SA, gs, n == 1); ++gs;
    unsigned i = 1;
    if (n >= 3 && (n & 1)) {
      att_step<false>(A, SB, SA, gs); ++gs; ++i;
#pragma unroll
      for (int kb = 0; kb < 4; ++kb) { SA[kb][0] = SB[kb][0]; SA[kb][1] = SB[kb][1]; }
    }
    for (; i + 2 < n; i += 2) { att_step<false>(A, SB, SA, gs); att_step<false>(A, SA, SB, gs + 1); gs += 2; }
    if (n >= 2) {
      att_step<true>(A, SB, SA, gs); ++gs;
#pragma unroll
      for (int kb = 0; kb < 4; ++kb) { SA[kb][0] = SB[kb][0]; SA[kb][1] = SB[kb][1]; }
    }
    att_drain(A, SA, gs - 1);
    att_finish(A, ws + WS_CF + ((size_t)(blk * 8 + A.rg * 2) * NH + h) * 4096);
  }
}

constexpr int K3_LDS = 8 * 64 * 272;
__global__ __launch_bounds__(512, 2) void k3_out(char* __restrict__ ws, unsigned short* __restrict__ out) {
  extern __shared__ __attribute__((aligned(16))) char lds_[];
  const unsigned tid = threadIdx.x, lane = tid & 63, wid = rfl(tid >> 6);
  const unsigned xcd = blockIdx.x & 7, j = blockIdx.x >> 3, tb64 = xcd * 4 + (j & 3), mb64 = j >> 2;
  const char* cf = ws + WS_CF + ((size_t)tb64 * 4 * NH + wid * 4) * 4096 + lane * 16;
  const char* wf = ws + WS_WO + ((size_t)wid * 16 * 32 + mb64 * 4) * 1024 + lane * 16;
  const unsigned et = tid >> 3, em = (tid & 7) * 8;
  unsigned short av[8];
#pragma unroll
  for (int e = 0; e < 8; ++e) av[e] = *(const unsigned short*)(ws + WS_AT + ((size_t)(mb64 * 64 + em + e) * SEQ + tb64 * 64 + et) * 2);
  const float omg = ((const float*)(ws + WS_G))[SEQ + tb64 * 64 + et];
  f32x4 acc[4][4];
#pragma unroll
  for (int a = 0; a < 4; ++a)
#pragma unroll
    for (int b = 0; b < 4; ++b) acc[a][b] = f32x4{};
  constexpr int PD = 4;
  bf16x8 cb[PD][4], wb[PD][4];
#pragma unroll
  for (int k = 0; k < PD; ++k)
#pragma unroll
    for (int i = 0; i < 4; ++i) { cb[k][i] = *(const bf16x8*)(cf + (size_t)i * NH * 4096 + k * 1024); wb[k][i] = *(const bf16x8*)(wf + (size_t)k * 32768 + i * 1024); }
#pragma unroll
  for (int k = 0; k < 16; ++k) {
    bf16x8 c[4], w[4];
#pragma unroll
    for (int i = 0; i < 4; ++i) { c[i] = cb[k % PD][i]; w[i] = wb[k % PD][i]; }
    if (k + PD < 16) {
#pragma unroll
      for (int i = 0; i < 4; ++i) { cb[k % PD][i] = *(const bf16x8*)(cf + (size_t)i * NH * 4096 + (k + PD) * 1024); wb[k % PD][i] = *(const bf16x8*)(wf + (size_t)(k + PD) * 32768 + i * 1024); }
    }
#pragma unroll
    for (int mb = 0; mb < 4; ++mb)
#pragma unroll
      for (int tb = 0; tb < 4; ++tb) acc[mb][tb] = MFMA16(w[mb], c[tb], acc[mb][tb]);
  }
  {
    const unsigned t = lane & 15, g = lane >> 4;
#pragma unroll
    for (int mb = 0; mb < 4; ++mb)
#pragma unroll
      for (int tb = 0; tb < 4; ++tb) *(f32x4*)(lds_ + wid * (64 * 272) + (16 * tb + t) * 272 + (16 * mb + 4 * g) * 4) = acc[mb][tb];
  }
  __syncthreads();
  float b[8];
#pragma unroll
  for (int e = 0; e < 8; ++e) b[e] = 0.f;
#pragma unroll
  for (int w = 0; w < 8; ++w) {
    const f32x4 p0 = *(const f32x4*)(lds_ + w * (64 * 272) + et * 272 + em * 4), p1 = *(const f32x4*)(lds_ + w * (64 * 272) + et * 272 + em * 4 + 16);
#pragma unroll
    for (int e = 0; e < 4; ++e) { b[e] += p0[e]; b[4 + e] += p1[e]; }
  }
  float o[8];
#pragma unroll
  for (int e = 0; e < 8; ++e) o[e] = __uint_as_float((unsigned)av[e] << 16) + bfr(omg * bfr(b[e]));
  *(u32x4*)(out + (size_t)(tb64 * 64 + et) * DM + mb64 * 64 + em) = u32x4{cvtpk(o[0], o[1]), cvtpk(o[2], o[3]), cvtpk(o[4], o[5]), cvtpk(o[6], o[7])};
}

extern "C" void kernel_launch(void* const* d_in, const int* in_sizes, int n_in, void* d_out, int out_size, void* d_ws, size_t ws_size, hipStream_t stream) {
  (void)in_sizes; (void)n_in; (void)out_size; (void)ws_size;
  const unsigned short* aq = (const unsigned short*)d_in[0];
  const unsigned short* ak = (const unsigned short*)d_in[1];
  const unsigned short* av = (const unsigned short*)d_in[2];
  const unsigned short* shk = (const unsigned short*)d_in[3];
  const unsigned short* ssk = (const unsigned short*)d_in[4];
  const unsigned short* ssd = (const unsigned short*)d_in[5];
  const char* ckv = (const char*)d_in[7];
  const char* wq = (const char*)d_in[8];
  const char* wuk = (const char*)d_in[9];
  const char* wuv = (const char*)d_in[10];
  const unsigned short* wo = (const unsigned short*)d_in[11];
  const unsigned short* gw = (const unsigned short*)d_in[12];
  char* ws = (char*)d_ws;
  (void)hipFuncSetAttribute((const void*)k1_kvproj, hipFuncAttributeMaxDynamicSharedMemorySize, K1_LDS);
  (void)hipFuncSetAttribute((const void*)k_conv, hipFuncAttributeMaxDynamicSharedMemorySize, CONV_LDS);
  (void)hipFuncSetAttribute((const void*)k2_attn, hipFuncAttributeMaxDynamicSharedMemorySize, K2_LDS);
  (void)hipFuncSetAttribute((const void*)k3_out, hipFuncAttributeMaxDynamicSharedMemorySize, K3_LDS);
  k0_prep<<<256, 256, 0, stream>>>(aq, ak, av, shk, gw, wo, ws);
  k1_kvproj<<<256, 512, K1_LDS, stream>>>(ckv, wuk, wuv, ws);
  k_conv<<<256, 512, CONV_LDS, stream>>>(ssk, ssd, ws);
  k2_attn<<<256, 512, K2_LDS, stream>>>((const char*)aq, wq, ws);
  k3_out<<<256, 512, K3_LDS, stream>>>(ws, (unsigned short*)d_out);
}
```

```cpp
#include <hip/hip_runtime.h>
#include <hip/hip_bf16.h>
#include <stdint.h>

typedef short bf16x8 __attribute__((ext_vector_type(8)));
typedef short s16x4 __attribute__((ext_vector_type(4)));
typedef float f32x2 __attribute__((ext_vector_type(2)));
typedef float f32x4 __attribute__((ext_vector_type(4)));
typedef unsigned u32x2 __attribute__((ext_vector_type(2)));
typedef unsigned u32x4 __attribute__((ext_vector_type(4)));
typedef __bf16 bf16x2 __attribute__((ext_vector_type(2)));
typedef __attribute__((address_space(3))) const char* lds_cptr;
typedef short v4i16_t __attribute__((ext_vector_type(4)));

#define DI __device__ __forceinline__
#define MFMA16(a, b, c) __builtin_amdgcn_mfma_f32_16x16x32_bf16(a, b, c, 0, 0, 0)
#define SBAR() __builtin_amdgcn_sched_barrier(0)

constexpr int SEQ = 2048, DM = 512, NH = 32;
constexpr float QSCALE = 0.12751743082f;

constexpr size_t WS_KF = 0;
constexpr size_t WS_VF = 16u << 20;
constexpr size_t WS_CF = 32u << 20;
constexpr size_t WS_WO = 48u << 20;
constexpr size_t WS_KVT = 52u << 20;
constexpr size_t WS_QT = 54u << 20;
constexpr size_t WS_AT = 56u << 20;
constexpr size_t WS_G = 58u << 20;
constexpr size_t WS_CNT = 59u << 20;
constexpr size_t WS_END = 60u << 20;

#define WS_OFFSET(n) ((((n) - WS_END) >> 21) << 21)

DI unsigned cvtpk(float lo, float hi) { f32x2 v = {lo, hi}; bf16x2 b = __builtin_convertvector(v, bf16x2); return __builtin_bit_cast(unsigned, b); }
DI float bfr(float x) { return __uint_as_float(cvtpk(x, 0.f) << 16); }
DI float bflo(unsigned w) { return __uint_as_float(w << 16); }
DI float bfhi(unsigned w) { return __uint_as_float(w & 0xffff0000u); }
DI unsigned rfl(unsigned x) { return __builtin_amdgcn_readfirstlane(x); }
DI s16x4 lds_tr(lds_cptr p) { return __builtin_bit_cast(s16x4, __builtin_amdgcn_ds_read_tr16_b64_v4i16((__attribute__((address_space(3))) v4i16_t*)p)); }
DI bf16x8 cat8(s16x4 lo, s16x4 hi) { return (bf16x8){lo[0], lo[1], lo[2], lo[3], hi[0], hi[1], hi[2], hi[3]}; }
DI bf16x8 pack8(const f32x4& a, const f32x4& b) { u32x4 w = {cvtpk(a[0], a[1]), cvtpk(a[2], a[3]), cvtpk(b[0], b[1]), cvtpk(b[2], b[3])}; return __builtin_bit_cast(bf16x8, w); }

DI unsigned lane_id() { unsigned l; asm volatile("v_mbcnt_lo_u32_b32 %0, -1, 0\n\tv_mbcnt_hi_u32_b32 %0, -1, %0" : "=v"(l)); return l; }
#define CODE_ADDR(dst, anchor, target) do { uint64_t pc_; unsigned d_; asm volatile("s_getpc_b64 %0\n" #anchor ":" : "=s"(pc_)); \
    asm volatile("s_mov_b32 %0, " #target "-" #anchor : "=s"(d_)); dst = (const char*)(pc_ + (int)d_); } while (0)
typedef float f32x8 __attribute__((ext_vector_type(8)));
typedef float f32x16 __attribute__((ext_vector_type(16)));
typedef float f32x32 __attribute__((ext_vector_type(32)));
typedef unsigned u32x8 __attribute__((ext_vector_type(8)));
typedef unsigned u32x32 __attribute__((ext_vector_type(32)));
DI u32x4 srd4(const void* p, unsigned bytes) {
  const uint64_t a = (uint64_t)p;
  return u32x4{rfl((unsigned)a), rfl((unsigned)(a >> 32) & 0xffffu), rfl(bytes), 0x00020000u};
}
#define LBAR() asm volatile("s_waitcnt lgkmcnt(0)\n\ts_barrier" ::: "memory")

struct ProjAddr { u32x8 rw; u32x4 ra; unsigned va0, va1, vw; };
DI ProjAddr proj_addr(unsigned ldsb, unsigned lane, unsigned wid, unsigned abase) {
  ProjAddr P;
  const unsigned rip = lane >> 3, pos = lane & 7;
  P.va0 = rip * 1024 + 16 * (pos ^ ((rip >> 1) & 7));
  P.va1 = rip * 1024 + 16 * (pos ^ ((4 + (rip >> 1)) & 7));
  const unsigned sig = ((rip >> 1) & 1) | ((wid & 1) << 1);
  P.vw = rip * 8192 + 16 * (pos ^ (2 * sig));
  const unsigned r = lane & 15, g = lane >> 4;
  const unsigned a0 = ldsb + abase + (wid * 32 + r) * 128 + 16 * ((0 + g) ^ ((r >> 1) & 7));
  const unsigned a1 = ldsb + abase + (wid * 32 + r) * 128 + 16 * ((4 + g) ^ ((r >> 1) & 7));
  P.ra = u32x4{a0, a1, a0 + 65536, a1 + 65536};
  const unsigned q = (lane & 15) >> 2, p = lane & 3, sg = ((q >> 1) & 1) | ((g & 1) << 1);
  const unsigned b = ldsb + (8 * g + q) * 128 + 16 * (p >> 1) + 8 * (p & 1);
#pragma unroll
  for (int n = 0; n < 4; ++n) { P.rw[n] = b + 32 * (n ^ sg); P.rw[4 + n] = b + 32 * (n ^ sg) + 65536; }
  return P;
}

constexpr unsigned PR_K = 0, PR_V = 9216, PR_Q = 17408, PR_W = 32768, PR_G = 49152, PR_S = 57344, PR_GW = 58368, PR_X = 59392, PR_T = 65536;
template <int CTRL, int RM> DI float dpp_add(float x) { return x + __int_as_float(__builtin_amdgcn_update_dpp(0, __float_as_int(x), CTRL, RM, 0xf, true)); }
DI void prep(char* lds, char* __restrict__ ws, unsigned wid) {
  const unsigned lane = lane_id(), tid = wid * 64 + lane, bid = blockIdx.x;
  char* tile = lds + PR_T;
  const int t0 = (bid >> 3) * 64, d0 = (bid & 7) * 64;
  const unsigned tl = tid >> 3, dq = tid & 7;
  const unsigned hds = bid >> 1, mh = bid & 1;
  const int tg = bid * 8 + wid;
  if (bid < 24 && tid == 0) ((unsigned*)(ws + WS_CNT))[bid * 32] = 0u;
  u32x4 wk[4], kr[4];
#pragma unroll
  for (int i = 0; i < 4; ++i) kr[i] = *(const u32x4*)(lds + PR_K + (tl + 5 + i) * 128 + dq * 16);
  const u32x4 vv = *(const u32x4*)(lds + PR_V + tl * 128 + dq * 16), qq = *(const u32x4*)(lds + PR_Q + tl * 128 + dq * 16);
#pragma unroll
  for (int i = 0; i < 4; ++i) wk[i] = *(const u32x4*)(lds + PR_S + dq * 64 + i * 16);
  const u32x4 xv = *(const u32x4*)(lds + PR_G + wid * 1024 + lane * 16), gv = *(const u32x4*)(lds + PR_GW + lane * 16);
  float kvv[8];
#pragma unroll
  for (int e = 0; e < 8; ++e) {
    const unsigned w01 = wk[e >> 1][(e & 1) * 2], w23 = wk[e >> 1][(e & 1) * 2 + 1];
    const float w[4] = {bflo(w01), bfhi(w01), bflo(w23), bfhi(w23)};
    float s = 0.f;
#pragma unroll
    for (int i = 0; i < 4; ++i) { const float kx = (e & 1) ? bfhi(kr[i][e >> 1]) : bflo(kr[i][e >> 1]); s = __builtin_fmaf(kx, w[3 - i], s); }
    kvv[e] = bfr(s) * ((e & 1) ? bfhi(vv[e >> 1]) : bflo(vv[e >> 1]));
  }
  const unsigned woff = tl * 128 + ((dq ^ ((tl >> 1) & 7)) << 4);
  *(u32x4*)(tile + woff) = u32x4{cvtpk(kvv[0], kvv[1]), cvtpk(kvv[2], kvv[3]), cvtpk(kvv[4], kvv[5]), cvtpk(kvv[6], kvv[7])};
  *(u32x4*)(tile + 8192 + woff) = qq;
  const unsigned g = lane >> 4, q = (lane & 15) >> 2, p = lane & 3;
#pragma unroll
  for (int i = 0; i < 2; ++i) {
    const unsigned mb = wid * 2 + i;
    lds_cptr a = (lds_cptr)lds + PR_W + (4 * g + q) * 512 + mb * 32 + p * 8;
    *(bf16x8*)(ws + WS_WO + ((size_t)hds * 32 + mh * 16 + mb) * 1024 + lane * 16) = cat8(lds_tr(a), lds_tr(a + 16 * 512));
  }
  {
    float s = 0.f;
#pragma unroll
    for (int e = 0; e < 4; ++e) { s += bfr(bflo(xv[e]) * bflo(gv[e])); s += bfr(bfhi(xv[e]) * bfhi(gv[e])); }
    s = dpp_add<0x111, 0xf>(s); s = dpp_add<0x112, 0xf>(s); s = dpp_add<0x114, 0xf>(s); s = dpp_add<0x118, 0xf>(s);
    s = dpp_add<0x142, 0xa>(s); s = dpp_add<0x143, 0xc>(s);
    if (lane == 63) { const float z = bfr(s), gt = bfr(1.f / (1.f + __expf(-z))); float* G = (float*)(ws + WS_G); G[tg] = gt; G[SEQ + tg] = bfr(1.f - gt); }
  }
  LBAR();
  {
    const unsigned cb = wid & 3, th = wid >> 2;
    const unsigned r0 = th * 32 + g * 8 + q, ch = cb * 2 + (p >> 1);
    lds_cptr a0 = (lds_cptr)tile + r0 * 128 + ((ch ^ ((r0 >> 1) & 7)) << 4) + (p & 1) * 8;
    lds_cptr a1 = (lds_cptr)tile + (r0 + 4) * 128 + ((ch ^ (((r0 + 4) >> 1) & 7)) << 4) + (p & 1) * 8;
    const size_t o = ((size_t)(d0 + cb * 16 + (lane & 15)) * SEQ + t0 + th * 32 + g * 8) * 2;
    *(bf16x8*)(ws + WS_KVT + o) = cat8(lds_tr(a0), lds_tr(a1));
    *(bf16x8*)(ws + WS_QT + o) = cat8(lds_tr(a0 + 8192), lds_tr(a1 + 8192));
  }
}

constexpr int K1_LDS = 163840;
__global__ __launch_bounds__(512, 2) void k1_kvproj(const char* __restrict__ ckv, const char* __restrict__ wuk, const char* __restrict__ wuv,
                                                    const char* __restrict__ aq, const char* __restrict__ ak, const char* __restrict__ av,
                                                    const char* __restrict__ shk, const char* __restrict__ gw, const char* __restrict__ wo,
                                                    const char* __restrict__ wq, const char* __restrict__ ssk, char* __restrict__ ws) {
  extern __shared__ __attribute__((aligned(16))) char lds_[];
  const unsigned wid = rfl(threadIdx.x >> 6);
  const unsigned xcd = blockIdx.x & 7, j = blockIdx.x >> 3, h = xcd * 4 + (j & 3), p = j >> 2;
  const unsigned ldsb = (unsigned)(size_t)(lds_cptr)lds_;
  asm volatile("" :: "s"(ckv), "s"(wuk), "s"(wuv), "s"(aq), "s"(ak), "s"(av), "s"(shk), "s"(gw), "s"(wo), "s"(wq), "s"(ws));
  const u32x4 sC = srd4(ckv + (size_t)p * 256 * 1024, 256 * 1024);
  const u32x4 sK = srd4(wuk + h * 256, 512 * 8192 - h * 256), sV = srd4(wuv + h * 256, 512 * 8192 - h * 256);
  const unsigned s60 = rfl(ldsb + wid * 4096), s61 = rfl(ldsb + wid * 1024), s62 = rfl(wid * 32768), s63 = rfl(wid * 65536);
  {
    const unsigned lane = lane_id(), rip = lane >> 3, pos = lane & 7;
    const unsigned va0 = rip * 1024 + 16 * (pos ^ ((rip >> 1) & 7)), va1 = rip * 1024 + 16 * (pos ^ ((4 + (rip >> 1)) & 7));
    const unsigned vw = rip * 8192 + 16 * (pos ^ (2 * (((rip >> 1) & 1) | ((wid & 1) << 1))));
    SBAR();
    asm volatile(
    "s_nop 7\n"
    "s_add_u32 m0, s60, 65536\n"
    "s_add_u32 s64, s62, 0\n"
    "buffer_load_dwordx4 v232, s[40:43], s64 offen lds\n"
    "s_add_u32 m0, s60, 66560\n"
    "s_add_u32 s64, s62, 8192\n"
    "buffer_load_dwordx4 v233, s[40:43], s64 offen lds\n"
    "s_add_u32 m0, s60, 67584\n"
    "s_add_u32 s64, s62, 16384\n"
    "buffer_load_dwordx4 v232, s[40:43], s64 offen lds\n"
    "s_add_u32 m0, s60, 68608\n"
    "s_add_u32 s64, s62, 24576\n"
    "buffer_load_dwordx4 v233, s[40:43], s64 offen lds\n"
    "s_add_u32 m0, s61, 0\n"
    "s_add_u32 s65, s63, 0\n"
    "buffer_load_dwordx4 v234, s[44:47], s65 offen lds\n"
    "s_add_u32 m0, s61, 8192\n"
    "s_add_u32 s65, s63, 128\n"
    "buffer_load_dwordx4 v234, s[44:47], s65 offen lds\n"
    "s_add_u32 m0, s60, 98304\n"
    "s_add_u32 s64, s62, 128\n"
    "buffer_load_dwordx4 v232, s[40:43], s64 offen lds\n"
    "s_add_u32 m0, s60, 99328\n"
    "s_add_u32 s64, s62, 8320\n"
    "buffer_load_dwordx4 v233, s[40:43], s64 offen lds\n"
    "s_add_u32 m0, s60, 100352\n"
    "s_add_u32 s64, s62, 16512\n"
    "buffer_load_dwordx4 v232, s[40:43], s64 offen lds\n"
    "s_add_u32 m0, s60, 101376\n"
    "s_add_u32 s64, s62, 24704\n"
    "buffer_load_dwordx4 v233, s[40:43], s64 offen lds\n"
    "s_add_u32 m0, s61, 16384\n"
    "s_add_u32 s65, s63, 524288\n"
    "buffer_load_dwordx4 v234, s[44:47], s65 offen lds\n"
    "s_add_u32 m0, s61, 24576\n"
    "s_add_u32 s65, s63, 524416\n"
    "buffer_load_dwordx4 v234, s[44:47], s65 offen lds\n"
    "s_add_u32 m0, s60, 131072\n"
    "s_add_u32 s64, s62, 256\n"
    "buffer_load_dwordx4 v232, s[40:43], s64 offen lds\n"
    "s_add_u32 m0, s60, 132096\n"
    "s_add_u32 s64, s62, 8448\n"
    "buffer_load_dwordx4 v233, s[40:43], s64 offen lds\n"
    "s_add_u32 m0, s60, 133120\n"
    "s_add_u32 s64, s62, 16640\n"
    "buffer_load_dwordx4 v232, s[40:43], s64 offen lds\n"
    "s_add_u32 m0, s60, 134144\n"
    "s_add_u32 s64, s62, 24832\n"
    "buffer_load_dwordx4 v233, s[40:43], s64 offen lds\n"
    "s_add_u32 m0, s61, 32768\n"
    "s_add_u32 s65, s63, 1048576\n"
    "buffer_load_dwordx4 v234, s[44:47], s65 offen lds\n"
    "s_add_u32 m0, s61, 40960\n"
    "s_add_u32 s65, s63, 1048704\n"
    "buffer_load_dwordx4 v234, s[44:47], s65 offen lds\n"
      :
      : "{v232}"(va0), "{v233}"(va1), "{v234}"(vw), "{s[40:43]}"(sC), "{s[44:47]}"(sK), "{s60}"(s60), "{s61}"(s61), "{s62}"(s62), "{s63}"(s63)
      : "s64", "s65", "scc", "memory");
    SBAR();
  }
  {
    const unsigned lane = lane_id();
    const ProjAddr P = proj_addr(ldsb, lane, wid, 65536);
    const u32x4 dK = srd4(ws + WS_KF + ((size_t)h * 128 + p * 16 + wid * 2) * 4096, 8192), dV = srd4(ws + WS_VF + ((size_t)h * 64 + p * 8 + wid) * 8192, 8192);
    u32x4 rw = {P.rw[0], P.rw[1], P.rw[2], P.rw[3]};
    const unsigned bid = blockIdx.x, t0 = (bid >> 3) * 64, d0 = (bid & 7) * 64;
    const u32x4 pk = srd4(ak + d0 * 2, SEQ * 1024 - d0 * 2), pv = srd4(av + d0 * 2, SEQ * 1024 - d0 * 2), pq = srd4(aq, SEQ * 1024), pw = srd4(wo, 4096 * 1024);
    const unsigned v8 = (lane >> 3) * 1024 + (lane & 7) * 16;
    const char* xb = ak; unsigned xn = 0, xl = PR_X, xv = lane * 16;
    if (wid == 0) { xb = ak + d0 * 2 + (t0 ? (t0 - 8) * 1024 : 0); xn = t0 ? 8 * 1024 : 0; xl = PR_K; xv = v8; }
    else if (wid == 1) { xb = shk + d0 * 8; xn = 512; xl = PR_S; }
    else if (wid == 2) { xb = gw; xn = 1024; xl = PR_GW; }
    u32x4 pq2 = pq; pq2[2] = rfl(SEQ * 1024);
    const char* k2c;
    CODE_ADDR(k2c, gm_anchor1, gm_k2_code);
    const char* tb_ = wid < 2 ? wq + ((size_t)p * 64 + wid * 32) * 8192 + h * 256 : k2c - 1280 + ((wid - 2) & 3) * 6656;
    SBAR();
    asm volatile(
    "s_nop 7\n"
    "s_movk_i32 s98, 0x1000\n"
    "s_waitcnt vmcnt(12)\n"
    "s_barrier\n"
    "ds_read_b128 v[0:3], v228 offset:0\n"
    "ds_read_b128 v[64:67], v228 offset:2048\n"
    "ds_read_b64_tr_b16 v[192:193], v224 offset:0\n"
    "ds_read_b64_tr_b16 v[194:195], v224 offset:512\n"
    "ds_read_b64_tr_b16 v[196:197], v225 offset:0\n"
    "ds_read_b64_tr_b16 v[198:199], v225 offset:512\n"
    "ds_read_b64_tr_b16 v[200:201], v226 offset:0\n"
    "ds_read_b64_tr_b16 v[202:203], v226 offset:512\n"
    "ds_read_b64_tr_b16 v[204:205], v227 offset:0\n"
    "ds_read_b64_tr_b16 v[206:207], v227 offset:512\n"
    "ds_read_b64_tr_b16 v[208:209], v224 offset:8192\n"
    "ds_read_b64_tr_b16 v[210:211], v224 offset:8704\n"
    "ds_read_b64_tr_b16 v[212:213], v225 offset:8192\n"
    "ds_read_b64_tr_b16 v[214:215], v225 offset:8704\n"
    "s_waitcnt lgkmcnt(10)\n"
    "v_mfma_f32_16x16x32_bf16 v[128:131], v[192:195], v[0:3], 0\n"
    "s_waitcnt lgkmcnt(8)\n"
    "ds_read_b64_tr_b16 v[216:217], v226 offset:8192\n"
    "v_mfma_f32_16x16x32_bf16 v[132:135], v[192:195], v[64:67], 0\n"
    "ds_read_b64_tr_b16 v[218:219], v226 offset:8704\n"
    "v_mfma_f32_16x16x32_bf16 v[136:139], v[196:199], v[0:3], 0\n"
    "s_waitcnt lgkmcnt(8)\n"
    "ds_read_b64_tr_b16 v[192:193], v227 offset:8192\n"
    "v_mfma_f32_16x16x32_bf16 v[140:143], v[196:199], v[64:67], 0\n"
    "ds_read_b64_tr_b16 v[194:195], v227 offset:8704\n"
    "v_mfma_f32_16x16x32_bf16 v[144:147], v[200:203], v[0:3], 0\n"
    "s_waitcnt lgkmcnt(8)\n"
    "ds_read_b64_tr_b16 v[196:197], v224 offset:4096\n"
    "ds_read_b128 v[4:7], v229 offset:0\n"
    "v_mfma_f32_16x16x32_bf16 v[148:151], v[200:203], v[64:67], 0\n"
    "ds_read_b64_tr_b16 v[198:199], v224 offset:4608\n"
    "v_mfma_f32_16x16x32_bf16 v[152:155], v[204:207], v[0:3], 0\n"
    "s_waitcnt lgkmcnt(9)\n"
    "ds_read_b64_tr_b16 v[200:201], v225 offset:4096\n"
    "ds_read_b128 v[68:71], v229 offset:2048\n"
    "v_mfma_f32_16x16x32_bf16 v[156:159], v[204:207], v[64:67], 0\n"
    "ds_read_b64_tr_b16 v[202:203], v225 offset:4608\n"
    "v_mfma_f32_16x16x32_bf16 v[160:163], v[208:211], v[0:3], 0\n"
    "s_waitcnt lgkmcnt(10)\n"
    "ds_read_b64_tr_b16 v[204:205], v226 offset:4096\n"
    "v_mfma_f32_16x16x32_bf16 v[164:167], v[208:211], v[64:67], 0\n"
    "ds_read_b64_tr_b16 v[206:207], v226 offset:4608\n"
    "v_mfma_f32_16x16x32_bf16 v[168:171], v[212:215], v[0:3], 0\n"
    "s_waitcnt lgkmcnt(10)\n"
    "ds_read_b64_tr_b16 v[208:209], v227 offset:4096\n"
    "v_mfma_f32_16x16x32_bf16 v[172:175], v[212:215], v[64:67], 0\n"
    "ds_read_b64_tr_b16 v[210:211], v227 offset:4608\n"
    "v_mfma_f32_16x16x32_bf16 v[176:179], v[216:219], v[0:3], 0\n"
    "s_waitcnt lgkmcnt(10)\n"
    "ds_read_b64_tr_b16 v[212:213], v224 offset:12288\n"
    "v_mfma_f32_16x16x32_bf16 v[180:183], v[216:219], v[64:67], 0\n"
    "ds_read_b64_tr_b16 v[214:215], v224 offset:12800\n"
    "v_mfma_f32_16x16x32_bf16 v[184:187], v[192:195], v[0:3], 0\n"
    "s_waitcnt lgkmcnt(7)\n"
    "ds_read_b64_tr_b16 v[216:217], v225 offset:12288\n"
    "v_mfma_f32_16x16x32_bf16 v[188:191], v[192:195], v[64:67], 0\n"
    "ds_read_b64_tr_b16 v[218:219], v225 offset:12800\n"
    "v_mfma_f32_16x16x32_bf16 v[128:131], v[196:199], v[4:7], v[128:131]\n"
    "s_waitcnt lgkmcnt(8)\n"
    "ds_read_b64_tr_b16 v[192:193], v226 offset:12288\n"
    "v_mfma_f32_16x16x32_bf16 v[132:135], v[196:199], v[68:71], v[132:135]\n"
    "ds_read_b64_tr_b16 v[194:195], v226 offset:12800\n"
    "v_mfma_f32_16x16x32_bf16 v[136:139], v[200:203], v[4:7], v[136:139]\n"
    "s_waitcnt lgkmcnt(8)\n"
    "ds_read_b64_tr_b16 v[196:197], v227 offset:12288\n"
    "v_mfma_f32_16x16x32_bf16 v[140:143], v[200:203], v[68:71], v[140:143]\n"
    "ds_read_b64_tr_b16 v[198:199], v227 offset:12800\n"
    "v_mfma_f32_16x16x32_bf16 v[144:147], v[204:207], v[4:7], v[144:147]\n"
    "s_waitcnt vmcnt(6)\n"
    "s_barrier\n"
    "s_waitcnt lgkmcnt(8)\n"
    "ds_read_b64_tr_b16 v[200:201], v224 offset:16384\n"
    "ds_read_b128 v[8:11], v228 offset:32768\n"
    "v_mfma_f32_16x16x32_bf16 v[148:151], v[204:207], v[68:71], v[148:151]\n"
    "ds_read_b64_tr_b16 v[202:203], v224 offset:16896\n"
    "v_mfma_f32_16x16x32_bf16 v[152:155], v[208:211], v[4:7], v[152:155]\n"
    "s_waitcnt lgkmcnt(9)\n"
    "ds_read_b64_tr_b16 v[204:205], v225 offset:16384\n"
    "ds_read_b128 v[72:75], v228 offset:34816\n"
    "v_mfma_f32_16x16x32_bf16 v[156:159], v[208:211], v[68:71], v[156:159]\n"
    "ds_read_b64_tr_b16 v[206:207], v225 offset:16896\n"
    "v_mfma_f32_16x16x32_bf16 v[160:163], v[212:215], v[4:7], v[160:163]\n"
    "s_waitcnt lgkmcnt(10)\n"
    "ds_read_b64_tr_b16 v[208:209], v226 offset:16384\n"
    "s_add_u32 m0, s61, 49152\n"
    "s_add_u32 s65, s63, 1572864\n"
    "buffer_load_dwordx4 v234, s[44:47], s65 offen lds\n"
    "v_mfma_f32_16x16x32_bf16 v[164:167], v[212:215], v[68:71], v[164:167]\n"
    "ds_read_b64_tr_b16 v[210:211], v226 offset:16896\n"
    "v_mfma_f32_16x16x32_bf16 v[168:171], v[216:219], v[4:7], v[168:171]\n"
    "s_waitcnt lgkmcnt(10)\n"
    "ds_read_b64_tr_b16 v[212:213], v227 offset:16384\n"
    "s_add_u32 m0, s61, 57344\n"
    "s_add_u32 s65, s63, 1572992\n"
    "buffer_load_dwordx4 v234, s[44:47], s65 offen lds\n"
    "v_mfma_f32_16x16x32_bf16 v[172:175], v[216:219], v[68:71], v[172:175]\n"
    "ds_read_b64_tr_b16 v[214:215], v227 offset:16896\n"
    "v_mfma_f32_16x16x32_bf16 v[176:179], v[192:195], v[4:7], v[176:179]\n"
    "s_waitcnt lgkmcnt(10)\n"
    "ds_read_b64_tr_b16 v[216:217], v224 offset:24576\n"
    "s_add_u32 m0, s60, 65536\n"
    "s_add_u32 s64, s62, 384\n"
    "buffer_load_dwordx4 v232, s[40:43], s64 offen lds\n"
    "v_mfma_f32_16x16x32_bf16 v[180:183], v[192:195], v[68:71], v[180:183]\n"
    "ds_read_b64_tr_b16 v[218:219], v224 offset:25088\n"
    "v_mfma_f32_16x16x32_bf16 v[184:187], v[196:199], v[4:7], v[184:187]\n"
    "s_waitcnt lgkmcnt(7)\n"
    "ds_read_b64_tr_b16 v[192:193], v225 offset:24576\n"
    "s_add_u32 m0, s60, 66560\n"
    "s_add_u32 s64, s62, 8576\n"
    "buffer_load_dwordx4 v233, s[40:43], s64 offen lds\n"
    "v_mfma_f32_16x16x32_bf16 v[188:191], v[196:199], v[68:71], v[188:191]\n"
    "ds_read_b64_tr_b16 v[194:195], v225 offset:25088\n"
    "v_mfma_f32_16x16x32_bf16 v[128:131], v[200:203], v[8:11], v[128:131]\n"
    "s_waitcnt lgkmcnt(8)\n"
    "ds_read_b64_tr_b16 v[196:197], v226 offset:24576\n"
    "s_add_u32 m0, s60, 67584\n"
    "s_add_u32 s64, s62, 16768\n"
    "buffer_load_dwordx4 v232, s[40:43], s64 offen lds\n"
    "v_mfma_f32_16x16x32_bf16 v[132:135], v[200:203], v[72:75], v[132:135]\n"
    "ds_read_b64_tr_b16 v[198:199], v226 offset:25088\n"
    "v_mfma_f32_16x16x32_bf16 v[136:139], v[204:207], v[8:11], v[136:139]\n"
    "s_waitcnt lgkmcnt(8)\n"
    "ds_read_b64_tr_b16 v[200:201], v227 offset:24576\n"
    "s_add_u32 m0, s60, 68608\n"
    "s_add_u32 s64, s62, 24960\n"
    "buffer_load_dwordx4 v233, s[40:43], s64 offen lds\n"
    "v_mfma_f32_16x16x32_bf16 v[140:143], v[204:207], v[72:75], v[140:143]\n"
    "ds_read_b64_tr_b16 v[202:203], v227 offset:25088\n"
    "v_mfma_f32_16x16x32_bf16 v[144:147], v[208:211], v[8:11], v[144:147]\n"
    "s_waitcnt lgkmcnt(8)\n"
    "ds_read_b64_tr_b16 v[204:205], v224 offset:20480\n"
    "ds_read_b128 v[12:15], v229 offset:32768\n"
    "v_mfma_f32_16x16x32_bf16 v[148:151], v[208:211], v[72:75], v[148:151]\n"
    "ds_read_b64_tr_b16 v[206:207], v224 offset:20992\n"
    "v_mfma_f32_16x16x32_bf16 v[152:155], v[212:215], v[8:11], v[152:155]\n"
    "s_waitcnt lgkmcnt(9)\n"
    "ds_read_b64_tr_b16 v[208:209], v225 offset:20480\n"
    "ds_read_b128 v[76:79], v229 offset:34816\n"
    "v_mfma_f32_16x16x32_bf16 v[156:159], v[212:215], v[72:75], v[156:159]\n"
    "ds_read_b64_tr_b16 v[210:211], v225 offset:20992\n"
    "v_mfma_f32_16x16x32_bf16 v[160:163], v[216:219], v[8:11], v[160:163]\n"
    "s_waitcnt lgkmcnt(10)\n"
    "ds_read_b64_tr_b16 v[212:213], v226 offset:20480\n"
    "v_mfma_f32_16x16x32_bf16 v[164:167], v[216:219], v[72:75], v[164:167]\n"
    "ds_read_b64_tr_b16 v[214:215], v226 offset:20992\n"
    "v_mfma_f32_16x16x32_bf16 v[168:171], v[192:195], v[8:11], v[168:171]\n"
    "s_waitcnt lgkmcnt(10)\n"
    "ds_read_b64_tr_b16 v[216:217], v227 offset:20480\n"
    "v_mfma_f32_16x16x32_bf16 v[172:175], v[192:195], v[72:75], v[172:175]\n"
    "ds_read_b64_tr_b16 v[218:219], v227 offset:20992\n"
    "v_mfma_f32_16x16x32_bf16 v[176:179], v[196:199], v[8:11], v[176:179]\n"
    "s_waitcnt lgkmcnt(10)\n"
    "ds_read_b64_tr_b16 v[192:193], v224 offset:28672\n"
    "v_mfma_f32_16x16x32_bf16 v[180:183], v[196:199], v[72:75], v[180:183]\n"
    "ds_read_b64_tr_b16 v[194:195], v224 offset:29184\n"
    "v_mfma_f32_16x16x32_bf16 v[184:187], v[200:203], v[8:11], v[184:187]\n"
    "s_waitcnt lgkmcnt(7)\n"
    "ds_read_b64_tr_b16 v[196:197], v225 offset:28672\n"
    "v_mfma_f32_16x16x32_bf16 v[188:191], v[200:203], v[72:75], v[188:191]\n"
    "ds_read_b64_tr_b16 v[198:199], v225 offset:29184\n"
    "v_mfma_f32_16x16x32_bf16 v[128:131], v[204:207], v[12:15], v[128:131]\n"
    "s_waitcnt lgkmcnt(8)\n"
    "ds_read_b64_tr_b16 v[200:201], v226 offset:28672\n"
    "v_mfma_f32_16x16x32_bf16 v[132:135], v[204:207], v[76:79], v[132:135]\n"
    "ds_read_b64_tr_b16 v[202:203], v226 offset:29184\n"
    "v_mfma_f32_16x16x32_bf16 v[136:139], v[208:211], v[12:15], v[136:139]\n"
    "s_waitcnt lgkmcnt(8)\n"
    "ds_read_b64_tr_b16 v[204:205], v227 offset:28672\n"
    "v_mfma_f32_16x16x32_bf16 v[140:143], v[208:211], v[76:79], v[140:143]\n"
    "ds_read_b64_tr_b16 v[206:207], v227 offset:29184\n"
    "v_mfma_f32_16x16x32_bf16 v[144:147], v[212:215], v[12:15], v[144:147]\n"
    "s_waitcnt vmcnt(6)\n"
    "s_barrier\n"
    "s_waitcnt lgkmcnt(8)\n"
    "ds_read_b64_tr_b16 v[208:209], v224 offset:32768\n"
    "ds_read_b128 v[16:19], v230 offset:0\n"
    "v_mfma_f32_16x16x32_bf16 v[148:151], v[212:215], v[76:79], v[148:151]\n"
    "ds_read_b64_tr_b16 v[210:211], v224 offset:33280\n"
    "v_mfma_f32_16x16x32_bf16 v[152:155], v[216:219], v[12:15], v[152:155]\n"
    "s_waitcnt lgkmcnt(9)\n"
    "ds_read_b64_tr_b16 v[212:213], v225 offset:32768\n"
    "ds_read_b128 v[80:83], v230 offset:2048\n"
    "v_mfma_f32_16x16x32_bf16 v[156:159], v[216:219], v[76:79], v[156:159]\n"
    "ds_read_b64_tr_b16 v[214:215], v225 offset:33280\n"
    "v_mfma_f32_16x16x32_bf16 v[160:163], v[192:195], v[12:15], v[160:163]\n"
    "s_waitcnt lgkmcnt(10)\n"
    "ds_read_b64_tr_b16 v[216:217], v226 offset:32768\n"
    "s_add_u32 m0, s61, 0\n"
    "s_add_u32 s65, s63, 2097152\n"
    "buffer_load_dwordx4 v234, s[44:47], s65 offen lds\n"
    "v_mfma_f32_16x16x32_bf16 v[164:167], v[192:195], v[76:79], v[164:167]\n"
    "ds_read_b64_tr_b16 v[218:219], v226 offset:33280\n"
    "v_mfma_f32_16x16x32_bf16 v[168:171], v[196:199], v[12:15], v[168:171]\n"
    "s_waitcnt lgkmcnt(10)\n"
    "ds_read_b64_tr_b16 v[192:193], v227 offset:32768\n"
    "s_add_u32 m0, s61, 8192\n"
    "s_add_u32 s65, s63, 2097280\n"
    "buffer_load_dwordx4 v234, s[44:47], s65 offen lds\n"
    "v_mfma_f32_16x16x32_bf16 v[172:175], v[196:199], v[76:79], v[172:175]\n"
    "ds_read_b64_tr_b16 v[194:195], v227 offset:33280\n"
    "v_mfma_f32_16x16x32_bf16 v[176:179], v[200:203], v[12:15], v[176:179]\n"
    "s_waitcnt lgkmcnt(10)\n"
    "ds_read_b64_tr_b16 v[196:197], v224 offset:40960\n"
    "s_add_u32 m0, s60, 98304\n"
    "s_add_u32 s64, s62, 512\n"
    "buffer_load_dwordx4 v232, s[40:43], s64 offen lds\n"
    "v_mfma_f32_16x16x32_bf16 v[180:183], v[200:203], v[76:79], v[180:183]\n"
    "ds_read_b64_tr_b16 v[198:199], v224 offset:41472\n"
    "v_mfma_f32_16x16x32_bf16 v[184:187], v[204:207], v[12:15], v[184:187]\n"
    "s_waitcnt lgkmcnt(7)\n"
    "ds_read_b64_tr_b16 v[200:201], v225 offset:40960\n"
    "s_add_u32 m0, s60, 99328\n"
    "s_add_u32 s64, s62, 8704\n"
    "buffer_load_dwordx4 v233, s[40:43], s64 offen lds\n"
    "v_mfma_f32_16x16x32_bf16 v[188:191], v[204:207], v[76:79], v[188:191]\n"
    "ds_read_b64_tr_b16 v[202:203], v225 offset:41472\n"
    "v_mfma_f32_16x16x32_bf16 v[128:131], v[208:211], v[16:19], v[128:131]\n"
    "s_waitcnt lgkmcnt(8)\n"
    "ds_read_b64_tr_b16 v[204:205], v226 offset:40960\n"
    "s_add_u32 m0, s60, 100352\n"
    "s_add_u32 s64, s62, 16896\n"
    "buffer_load_dwordx4 v232, s[40:43], s64 offen lds\n"
    "v_mfma_f32_16x16x32_bf16 v[132:135], v[208:211], v[80:83], v[132:135]\n"
    "ds_read_b64_tr_b16 v[206:207], v226 offset:41472\n"
    "v_mfma_f32_16x16x32_bf16 v[136:139], v[212:215], v[16:19], v[136:139]\n"
    "s_waitcnt lgkmcnt(8)\n"
    "ds_read_b64_tr_b16 v[208:209], v227 offset:40960\n"
    "s_add_u32 m0, s60, 101376\n"
    "s_add_u32 s64, s62, 25088\n"
    "buffer_load_dwordx4 v233, s[40:43], s64 offen lds\n"
    "v_mfma_f32_16x16x32_bf16 v[140:143], v[212:215], v[80:83], v[140:143]\n"
    "ds_read_b64_tr_b16 v[210:211], v227 offset:41472\n"
    "v_mfma_f32_16x16x32_bf16 v[144:147], v[216:219], v[16:19], v[144:147]\n"
    "s_waitcnt lgkmcnt(8)\n"
    "ds_read_b64_tr_b16 v[212:213], v224 offset:36864\n"
    "ds_read_b128 v[20:23], v231 offset:0\n"
    "v_mfma_f32_16x16x32_bf16 v[148:151], v[216:219], v[80:83], v[148:151]\n"
    "ds_read_b64_tr_b16 v[214:215], v224 offset:37376\n"
    "v_mfma_f32_16x16x32_bf16 v[152:155], v[192:195], v[16:19], v[152:155]\n"
    "s_waitcnt lgkmcnt(9)\n"
    "ds_read_b64_tr_b16 v[216:217], v225 offset:36864\n"
    "ds_read_b128 v[84:87], v231 offset:2048\n"
    "v_mfma_f32_16x16x32_bf16 v[156:159], v[192:195], v[80:83], v[156:159]\n"
    "ds_read_b64_tr_b16 v[218:219], v225 offset:37376\n"
    "v_mfma_f32_16x16x32_bf16 v[160:163], v[196:199], v[16:19], v[160:163]\n"
    "s_waitcnt lgkmcnt(10)\n"
    "ds_read_b64_tr_b16 v[192:193], v226 offset:36864\n"
    "v_mfma_f32_16x16x32_bf16 v[164:167], v[196:199], v[80:83], v[164:167]\n"
    "ds_read_b64_tr_b16 v[194:195], v226 offset:37376\n"
    "v_mfma_f32_16x16x32_bf16 v[168:171], v[200:203], v[16:19], v[168:171]\n"
    "s_waitcnt lgkmcnt(10)\n"
    "ds_read_b64_tr_b16 v[196:197], v227 offset:36864\n"
    "v_mfma_f32_16x16x32_bf16 v[172:175], v[200:203], v[80:83], v[172:175]\n"
    "ds_read_b64_tr_b16 v[198:199], v227 offset:37376\n"
    "v_mfma_f32_16x16x32_bf16 v[176:179], v[204:207], v[16:19], v[176:179]\n"
    "s_waitcnt lgkmcnt(10)\n"
    "ds_read_b64_tr_b16 v[200:201], v224 offset:45056\n"
    "v_mfma_f32_16x16x32_bf16 v[180:183], v[204:207], v[80:83], v[180:183]\n"
    "ds_read_b64_tr_b16 v[202:203], v224 offset:45568\n"
    "v_mfma_f32_16x16x32_bf16 v[184:187], v[208:211], v[16:19], v[184:187]\n"
    "s_waitcnt lgkmcnt(7)\n"
    "ds_read_b64_tr_b16 v[204:205], v225 offset:45056\n"
    "v_mfma_f32_16x16x32_bf16 v[188:191], v[208:211], v[80:83], v[188:191]\n"
    "ds_read_b64_tr_b16 v[206:207], v225 offset:45568\n"
    "v_mfma_f32_16x16x32_bf16 v[128:131], v[212:215], v[20:23], v[128:131]\n"
    "s_waitcnt lgkmcnt(8)\n"
    "ds_read_b64_tr_b16 v[208:209], v226 offset:45056\n"
    "v_mfma_f32_16x16x32_bf16 v[132:135], v[212:215], v[84:87], v[132:135]\n"
    "ds_read_b64_tr_b16 v[210:211], v226 offset:45568\n"
    "v_mfma_f32_16x16x32_bf16 v[136:139], v[216:219], v[20:23], v[136:139]\n"
    "s_waitcnt lgkmcnt(8)\n"
    "ds_read_b64_tr_b16 v[212:213], v227 offset:45056\n"
    "v_mfma_f32_16x16x32_bf16 v[140:143], v[216:219], v[84:87], v[140:143]\n"
    "ds_read_b64_tr_b16 v[214:215], v227 offset:45568\n"
    "v_mfma_f32_16x16x32_bf16 v[144:147], v[192:195], v[20:23], v[144:147]\n"
    "s_waitcnt vmcnt(6)\n"
    "s_barrier\n"
    "s_waitcnt lgkmcnt(8)\n"
    "ds_read_b64_tr_b16 v[216:217], v224 offset:49152\n"
    "ds_read_b128 v[24:27], v228 offset:0\n"
    "v_mfma_f32_16x16x32_bf16 v[148:151], v[192:195], v[84:87], v[148:151]\n"
    "ds_read_b64_tr_b16 v[218:219], v224 offset:49664\n"
    "v_mfma_f32_16x16x32_bf16 v[152:155], v[196:199], v[20:23], v[152:155]\n"
    "s_waitcnt lgkmcnt(9)\n"
    "ds_read_b64_tr_b16 v[192:193], v225 offset:49152\n"
    "ds_read_b128 v[88:91], v228 offset:2048\n"
    "v_mfma_f32_16x16x32_bf16 v[156:159], v[196:199], v[84:87], v[156:159]\n"
    "ds_read_b64_tr_b16 v[194:195], v225 offset:49664\n"
    "v_mfma_f32_16x16x32_bf16 v[160:163], v[200:203], v[20:23], v[160:163]\n"
    "s_waitcnt lgkmcnt(10)\n"
    "ds_read_b64_tr_b16 v[196:197], v226 offset:49152\n"
    "s_add_u32 m0, s61, 16384\n"
    "s_add_u32 s65, s63, 2621440\n"
    "buffer_load_dwordx4 v234, s[44:47], s65 offen lds\n"
    "v_mfma_f32_16x16x32_bf16 v[164:167], v[200:203], v[84:87], v[164:167]\n"
    "ds_read_b64_tr_b16 v[198:199], v226 offset:49664\n"
    "v_mfma_f32_16x16x32_bf16 v[168:171], v[204:207], v[20:23], v[168:171]\n"
    "s_waitcnt lgkmcnt(10)\n"
    "ds_read_b64_tr_b16 v[200:201], v227 offset:49152\n"
    "s_add_u32 m0, s61, 24576\n"
    "s_add_u32 s65, s63, 2621568\n"
    "buffer_load_dwordx4 v234, s[44:47], s65 offen lds\n"
    "v_mfma_f32_16x16x32_bf16 v[172:175], v[204:207], v[84:87], v[172:175]\n"
    "ds_read_b64_tr_b16 v[202:203], v227 offset:49664\n"
    "v_mfma_f32_16x16x32_bf16 v[176:179], v[208:211], v[20:23], v[176:179]\n"
    "s_waitcnt lgkmcnt(10)\n"
    "ds_read_b64_tr_b16 v[204:205], v224 offset:57344\n"
    "s_add_u32 m0, s60, 131072\n"
    "s_add_u32 s64, s62, 640\n"
    "buffer_load_dwordx4 v232, s[40:43], s64 offen lds\n"
    "v_mfma_f32_16x16x32_bf16 v[180:183], v[208:211], v[84:87], v[180:183]\n"
    "ds_read_b64_tr_b16 v[206:207], v224 offset:57856\n"
    "v_mfma_f32_16x16x32_bf16 v[184:187], v[212:215], v[20:23], v[184:187]\n"
    "s_waitcnt lgkmcnt(7)\n"
    "ds_read_b64_tr_b16 v[208:209], v225 offset:57344\n"
    "s_add_u32 m0, s60, 132096\n"
    "s_add_u32 s64, s62, 8832\n"
    "buffer_load_dwordx4 v233, s[40:43], s64 offen lds\n"
    "v_mfma_f32_16x16x32_bf16 v[188:191], v[212:215], v[84:87], v[188:191]\n"
    "ds_read_b64_tr_b16 v[210:211], v225 offset:57856\n"
    "v_mfma_f32_16x16x32_bf16 v[128:131], v[216:219], v[24:27], v[128:131]\n"
    "s_waitcnt lgkmcnt(8)\n"
    "ds_read_b64_tr_b16 v[212:213], v226 offset:57344\n"
    "s_add_u32 m0, s60, 133120\n"
    "s_add_u32 s64, s62, 17024\n"
    "buffer_load_dwordx4 v232, s[40:43], s64 offen lds\n"
    "v_mfma_f32_16x16x32_bf16 v[132:135], v[216:219], v[88:91], v[132:135]\n"
    "ds_read_b64_tr_b16 v[214:215], v226 offset:57856\n"
    "v_mfma_f32_16x16x32_bf16 v[136:139], v[192:195], v[24:27], v[136:139]\n"
    "s_waitcnt lgkmcnt(8)\n"
    "ds_read_b64_tr_b16 v[216:217], v227 offset:57344\n"
    "s_add_u32 m0, s60, 134144\n"
    "s_add_u32 s64, s62, 25216\n"
    "buffer_load_dwordx4 v233, s[40:43], s64 offen lds\n"
    "v_mfma_f32_16x16x32_bf16 v[140:143], v[192:195], v[88:91], v[140:143]\n"
    "ds_read_b64_tr_b16 v[218:219], v227 offset:57856\n"
    "v_mfma_f32_16x16x32_bf16 v[144:147], v[196:199], v[24:27], v[144:147]\n"
    "s_waitcnt lgkmcnt(8)\n"
    "ds_read_b64_tr_b16 v[192:193], v224 offset:53248\n"
    "ds_read_b128 v[28:31], v229 offset:0\n"
    "v_mfma_f32_16x16x32_bf16 v[148:151], v[196:199], v[88:91], v[148:151]\n"
    "ds_read_b64_tr_b16 v[194:195], v224 offset:53760\n"
    "v_mfma_f32_16x16x32_bf16 v[152:155], v[200:203], v[24:27], v[152:155]\n"
    "s_waitcnt lgkmcnt(9)\n"
    "ds_read_b64_tr_b16 v[196:197], v225 offset:53248\n"
    "ds_read_b128 v[92:95], v229 offset:2048\n"
    "v_mfma_f32_16x16x32_bf16 v[156:159], v[200:203], v[88:91], v[156:159]\n"
    "ds_read_b64_tr_b16 v[198:199], v225 offset:53760\n"
    "v_mfma_f32_16x16x32_bf16 v[160:163], v[204:207], v[24:27], v[160:163]\n"
    "s_waitcnt lgkmcnt(10)\n"
    "ds_read_b64_tr_b16 v[200:201], v226 offset:53248\n"
    "v_mfma_f32_16x16x32_bf16 v[164:167], v[204:207], v[88:91], v[164:167]\n"
    "ds_read_b64_tr_b16 v[202:203], v226 offset:53760\n"
    "v_mfma_f32_16x16x32_bf16 v[168:171], v[208:211], v[24:27], v[168:171]\n"
    "s_waitcnt lgkmcnt(10)\n"
    "ds_read_b64_tr_b16 v[204:205], v227 offset:53248\n"
    "v_mfma_f32_16x16x32_bf16 v[172:175], v[208:211], v[88:91], v[172:175]\n"
    "ds_read_b64_tr_b16 v[206:207], v227 offset:53760\n"
    "v_mfma_f32_16x16x32_bf16 v[176:179], v[212:215], v[24:27], v[176:179]\n"
    "s_waitcnt lgkmcnt(10)\n"
    "ds_read_b64_tr_b16 v[208:209], v224 offset:61440\n"
    "v_mfma_f32_16x16x32_bf16 v[180:183], v[212:215], v[88:91], v[180:183]\n"
    "ds_read_b64_tr_b16 v[210:211], v224 offset:61952\n"
    "v_mfma_f32_16x16x32_bf16 v[184:187], v[216:219], v[24:27], v[184:187]\n"
    "s_waitcnt lgkmcnt(7)\n"
    "ds_read_b64_tr_b16 v[212:213], v225 offset:61440\n"
    "v_mfma_f32_16x16x32_bf16 v[188:191], v[216:219], v[88:91], v[188:191]\n"
    "ds_read_b64_tr_b16 v[214:215], v225 offset:61952\n"
    "v_mfma_f32_16x16x32_bf16 v[128:131], v[192:195], v[28:31], v[128:131]\n"
    "s_waitcnt lgkmcnt(8)\n"
    "ds_read_b64_tr_b16 v[216:217], v226 offset:61440\n"
    "v_mfma_f32_16x16x32_bf16 v[132:135], v[192:195], v[92:95], v[132:135]\n"
    "ds_read_b64_tr_b16 v[218:219], v226 offset:61952\n"
    "v_mfma_f32_16x16x32_bf16 v[136:139], v[196:199], v[28:31], v[136:139]\n"
    "s_waitcnt lgkmcnt(8)\n"
    "ds_read_b64_tr_b16 v[192:193], v227 offset:61440\n"
    "v_mfma_f32_16x16x32_bf16 v[140:143], v[196:199], v[92:95], v[140:143]\n"
    "ds_read_b64_tr_b16 v[194:195], v227 offset:61952\n"
    "v_mfma_f32_16x16x32_bf16 v[144:147], v[200:203], v[28:31], v[144:147]\n"
    "s_waitcnt vmcnt(6)\n"
    "s_barrier\n"
    "s_waitcnt lgkmcnt(8)\n"
    "ds_read_b64_tr_b16 v[196:197], v224 offset:0\n"
    "ds_read_b128 v[32:35], v228 offset:32768\n"
    "v_mfma_f32_16x16x32_bf16 v[148:151], v[200:203], v[92:95], v[148:151]\n"
    "ds_read_b64_tr_b16 v[198:199], v224 offset:512\n"
    "v_mfma_f32_16x16x32_bf16 v[152:155], v[204:207], v[28:31], v[152:155]\n"
    "s_waitcnt lgkmcnt(9)\n"
    "ds_read_b64_tr_b16 v[200:201], v225 offset:0\n"
    "ds_read_b128 v[96:99], v228 offset:34816\n"
    "v_mfma_f32_16x16x32_bf16 v[156:159], v[204:207], v[92:95], v[156:159]\n"
    "ds_read_b64_tr_b16 v[202:203], v225 offset:512\n"
    "v_mfma_f32_16x16x32_bf16 v[160:163], v[208:211], v[28:31], v[160:163]\n"
    "s_waitcnt lgkmcnt(10)\n"
    "ds_read_b64_tr_b16 v[204:205], v226 offset:0\n"
    "s_add_u32 m0, s61, 32768\n"
    "s_add_u32 s65, s63, 3145728\n"
    "buffer_load_dwordx4 v234, s[44:47], s65 offen lds\n"
    "v_mfma_f32_16x16x32_bf16 v[164:167], v[208:211], v[92:95], v[164:167]\n"
    "ds_read_b64_tr_b16 v[206:207], v226 offset:512\n"
    "v_mfma_f32_16x16x32_bf16 v[168:171], v[212:215], v[28:31], v[168:171]\n"
    "s_waitcnt lgkmcnt(10)\n"
    "ds_read_b64_tr_b16 v[208:209], v227 offset:0\n"
    "s_add_u32 m0, s61, 40960\n"
    "s_add_u32 s65, s63, 3145856\n"
    "buffer_load_dwordx4 v234, s[44:47], s65 offen lds\n"
    "v_mfma_f32_16x16x32_bf16 v[172:175], v[212:215], v[92:95], v[172:175]\n"
    "ds_read_b64_tr_b16 v[210:211], v227 offset:512\n"
    "v_mfma_f32_16x16x32_bf16 v[176:179], v[216:219], v[28:31], v[176:179]\n"
    "s_waitcnt lgkmcnt(10)\n"
    "ds_read_b64_tr_b16 v[212:213], v224 offset:8192\n"
    "s_add_u32 m0, s60, 65536\n"
    "s_add_u32 s64, s62, 768\n"
    "buffer_load_dwordx4 v232, s[40:43], s64 offen lds\n"
    "v_mfma_f32_16x16x32_bf16 v[180:183], v[216:219], v[92:95], v[180:183]\n"
    "ds_read_b64_tr_b16 v[214:215], v224 offset:8704\n"
    "v_mfma_f32_16x16x32_bf16 v[184:187], v[192:195], v[28:31], v[184:187]\n"
    "s_waitcnt lgkmcnt(7)\n"
    "ds_read_b64_tr_b16 v[216:217], v225 offset:8192\n"
    "s_add_u32 m0, s60, 66560\n"
    "s_add_u32 s64, s62, 8960\n"
    "buffer_load_dwordx4 v233, s[40:43], s64 offen lds\n"
    "v_mfma_f32_16x16x32_bf16 v[188:191], v[192:195], v[92:95], v[188:191]\n"
    "ds_read_b64_tr_b16 v[218:219], v225 offset:8704\n"
    "v_mfma_f32_16x16x32_bf16 v[128:131], v[196:199], v[32:35], v[128:131]\n"
    "s_waitcnt lgkmcnt(8)\n"
    "ds_read_b64_tr_b16 v[192:193], v226 offset:8192\n"
    "s_add_u32 m0, s60, 67584\n"
    "s_add_u32 s64, s62, 17152\n"
    "buffer_load_dwordx4 v232, s[40:43], s64 offen lds\n"
    "v_mfma_f32_16x16x32_bf16 v[132:135], v[196:199], v[96:99], v[132:135]\n"
    "ds_read_b64_tr_b16 v[194:195], v226 offset:8704\n"
    "v_mfma_f32_16x16x32_bf16 v[136:139], v[200:203], v[32:35], v[136:139]\n"
    "s_waitcnt lgkmcnt(8)\n"
    "ds_read_b64_tr_b16 v[196:197], v227 offset:8192\n"
    "s_add_u32 m0, s60, 68608\n"
    "s_add_u32 s64, s62, 25344\n"
    "buffer_load_dwordx4 v233, s[40:43], s64 offen lds\n"
    "v_mfma_f32_16x16x32_bf16 v[140:143], v[200:203], v[96:99], v[140:143]\n"
    "ds_read_b64_tr_b16 v[198:199], v227 offset:8704\n"
    "v_mfma_f32_16x16x32_bf16 v[144:147], v[204:207], v[32:35], v[144:147]\n"
    "s_waitcnt lgkmcnt(8)\n"
    "ds_read_b64_tr_b16 v[200:201], v224 offset:4096\n"
    "ds_read_b128 v[36:39], v229 offset:32768\n"
    "v_mfma_f32_16x16x32_bf16 v[148:151], v[204:207], v[96:99], v[148:151]\n"
    "ds_read_b64_tr_b16 v[202:203], v224 offset:4608\n"
    "v_mfma_f32_16x16x32_bf16 v[152:155], v[208:211], v[32:35], v[152:155]\n"
    "s_waitcnt lgkmcnt(9)\n"
    "ds_read_b64_tr_b16 v[204:205], v225 offset:4096\n"
    "ds_read_b128 v[100:103], v229 offset:34816\n"
    "v_mfma_f32_16x16x32_bf16 v[156:159], v[208:211], v[96:99], v[156:159]\n"
    "ds_read_b64_tr_b16 v[206:207], v225 offset:4608\n"
    "v_mfma_f32_16x16x32_bf16 v[160:163], v[212:215], v[32:35], v[160:163]\n"
    "s_waitcnt lgkmcnt(10)\n"
    "ds_read_b64_tr_b16 v[208:209], v226 offset:4096\n"
    "v_mfma_f32_16x16x32_bf16 v[164:167], v[212:215], v[96:99], v[164:167]\n"
    "ds_read_b64_tr_b16 v[210:211], v226 offset:4608\n"
    "v_mfma_f32_16x16x32_bf16 v[168:171], v[216:219], v[32:35], v[168:171]\n"
    "s_waitcnt lgkmcnt(10)\n"
    "ds_read_b64_tr_b16 v[212:213], v227 offset:4096\n"
    "v_mfma_f32_16x16x32_bf16 v[172:175], v[216:219], v[96:99], v[172:175]\n"
    "ds_read_b64_tr_b16 v[214:215], v227 offset:4608\n"
    "v_mfma_f32_16x16x32_bf16 v[176:179], v[192:195], v[32:35], v[176:179]\n"
    "s_waitcnt lgkmcnt(10)\n"
    "ds_read_b64_tr_b16 v[216:217], v224 offset:12288\n"
    "v_mfma_f32_16x16x32_bf16 v[180:183], v[192:195], v[96:99], v[180:183]\n"
    "ds_read_b64_tr_b16 v[218:219], v224 offset:12800\n"
    "v_mfma_f32_16x16x32_bf16 v[184:187], v[196:199], v[32:35], v[184:187]\n"
    "s_waitcnt lgkmcnt(7)\n"
    "ds_read_b64_tr_b16 v[192:193], v225 offset:12288\n"
    "v_mfma_f32_16x16x32_bf16 v[188:191], v[196:199], v[96:99], v[188:191]\n"
    "ds_read_b64_tr_b16 v[194:195], v225 offset:12800\n"
    "v_mfma_f32_16x16x32_bf16 v[128:131], v[200:203], v[36:39], v[128:131]\n"
    "s_waitcnt lgkmcnt(8)\n"
    "ds_read_b64_tr_b16 v[196:197], v226 offset:12288\n"
    "v_mfma_f32_16x16x32_bf16 v[132:135], v[200:203], v[100:103], v[132:135]\n"
    "ds_read_b64_tr_b16 v[198:199], v226 offset:12800\n"
    "v_mfma_f32_16x16x32_bf16 v[136:139], v[204:207], v[36:39], v[136:139]\n"
    "s_waitcnt lgkmcnt(8)\n"
    "ds_read_b64_tr_b16 v[200:201], v227 offset:12288\n"
    "v_mfma_f32_16x16x32_bf16 v[140:143], v[204:207], v[100:103], v[140:143]\n"
    "ds_read_b64_tr_b16 v[202:203], v227 offset:12800\n"
    "v_mfma_f32_16x16x32_bf16 v[144:147], v[208:211], v[36:39], v[144:147]\n"
    "s_waitcnt vmcnt(6)\n"
    "s_barrier\n"
    "s_waitcnt lgkmcnt(8)\n"
    "ds_read_b64_tr_b16 v[204:205], v224 offset:16384\n"
    "ds_read_b128 v[40:43], v230 offset:0\n"
    "v_mfma_f32_16x16x32_bf16 v[148:151], v[208:211], v[100:103], v[148:151]\n"
    "ds_read_b64_tr_b16 v[206:207], v224 offset:16896\n"
    "v_mfma_f32_16x16x32_bf16 v[152:155], v[212:215], v[36:39], v[152:155]\n"
    "s_waitcnt lgkmcnt(9)\n"
    "ds_read_b64_tr_b16 v[208:209], v225 offset:16384\n"
    "ds_read_b128 v[104:107], v230 offset:2048\n"
    "v_mfma_f32_16x16x32_bf16 v[156:159], v[212:215], v[100:103], v[156:159]\n"
    "ds_read_b64_tr_b16 v[210:211], v225 offset:16896\n"
    "v_mfma_f32_16x16x32_bf16 v[160:163], v[216:219], v[36:39], v[160:163]\n"
    "s_waitcnt lgkmcnt(10)\n"
    "ds_read_b64_tr_b16 v[212:213], v226 offset:16384\n"
    "s_add_u32 m0, s61, 49152\n"
    "s_add_u32 s65, s63, 3670016\n"
    "buffer_load_dwordx4 v234, s[44:47], s65 offen lds\n"
    "v_mfma_f32_16x16x32_bf16 v[164:167], v[216:219], v[100:103], v[164:167]\n"
    "ds_read_b64_tr_b16 v[214:215], v226 offset:16896\n"
    "v_mfma_f32_16x16x32_bf16 v[168:171], v[192:195], v[36:39], v[168:171]\n"
    "s_waitcnt lgkmcnt(10)\n"
    "ds_read_b64_tr_b16 v[216:217], v227 offset:16384\n"
    "s_add_u32 m0, s61, 57344\n"
    "s_add_u32 s65, s63, 3670144\n"
    "buffer_load_dwordx4 v234, s[44:47], s65 offen lds\n"
    "v_mfma_f32_16x16x32_bf16 v[172:175], v[192:195], v[100:103], v[172:175]\n"
    "ds_read_b64_tr_b16 v[218:219], v227 offset:16896\n"
    "v_mfma_f32_16x16x32_bf16 v[176:179], v[196:199], v[36:39], v[176:179]\n"
    "s_waitcnt lgkmcnt(10)\n"
    "ds_read_b64_tr_b16 v[192:193], v224 offset:24576\n"
    "s_add_u32 m0, s60, 98304\n"
    "s_add_u32 s64, s62, 896\n"
    "buffer_load_dwordx4 v232, s[40:43], s64 offen lds\n"
    "v_mfma_f32_16x16x32_bf16 v[180:183], v[196:199], v[100:103], v[180:183]\n"
    "ds_read_b64_tr_b16 v[194:195], v224 offset:25088\n"
    "v_mfma_f32_16x16x32_bf16 v[184:187], v[200:203], v[36:39], v[184:187]\n"
    "s_waitcnt lgkmcnt(7)\n"
    "ds_read_b64_tr_b16 v[196:197], v225 offset:24576\n"
    "s_add_u32 m0, s60, 99328\n"
    "s_add_u32 s64, s62, 9088\n"
    "buffer_load_dwordx4 v233, s[40:43], s64 offen lds\n"
    "v_mfma_f32_16x16x32_bf16 v[188:191], v[200:203], v[100:103], v[188:191]\n"
    "ds_read_b64_tr_b16 v[198:199], v225 offset:25088\n"
    "v_mfma_f32_16x16x32_bf16 v[128:131], v[204:207], v[40:43], v[128:131]\n"
    "s_waitcnt lgkmcnt(8)\n"
    "ds_read_b64_tr_b16 v[200:201], v226 offset:24576\n"
    "s_add_u32 m0, s60, 100352\n"
    "s_add_u32 s64, s62, 17280\n"
    "buffer_load_dwordx4 v232, s[40:43], s64 offen lds\n"
    "v_mfma_f32_16x16x32_bf16 v[132:135], v[204:207], v[104:107], v[132:135]\n"
    "ds_read_b64_tr_b16 v[202:203], v226 offset:25088\n"
    "v_mfma_f32_16x16x32_bf16 v[136:139], v[208:211], v[40:43], v[136:139]\n"
    "s_waitcnt lgkmcnt(8)\n"
    "ds_read_b64_tr_b16 v[204:205], v227 offset:24576\n"
    "s_add_u32 m0, s60, 101376\n"
    "s_add_u32 s64, s62, 25472\n"
    "buffer_load_dwordx4 v233, s[40:43], s64 offen lds\n"
    "v_mfma_f32_16x16x32_bf16 v[140:143], v[208:211], v[104:107], v[140:143]\n"
    "ds_read_b64_tr_b16 v[206:207], v227 offset:25088\n"
    "v_mfma_f32_16x16x32_bf16 v[144:147], v[212:215], v[40:43], v[144:147]\n"
    "s_waitcnt lgkmcnt(8)\n"
    "ds_read_b64_tr_b16 v[208:209], v224 offset:20480\n"
    "ds_read_b128 v[44:47], v231 offset:0\n"
    "v_mfma_f32_16x16x32_bf16 v[148:151], v[212:215], v[104:107], v[148:151]\n"
    "ds_read_b64_tr_b16 v[210:211], v224 offset:20992\n"
    "v_mfma_f32_16x16x32_bf16 v[152:155], v[216:219], v[40:43], v[152:155]\n"
    "s_waitcnt lgkmcnt(9)\n"
    "ds_read_b64_tr_b16 v[212:213], v225 offset:20480\n"
    "ds_read_b128 v[108:111], v231 offset:2048\n"
    "v_mfma_f32_16x16x32_bf16 v[156:159], v[216:219], v[104:107], v[156:159]\n"
    "ds_read_b64_tr_b16 v[214:215], v225 offset:20992\n"
    "v_mfma_f32_16x16x32_bf16 v[160:163], v[192:195], v[40:43], v[160:163]\n"
    "s_waitcnt lgkmcnt(10)\n"
    "ds_read_b64_tr_b16 v[216:217], v226 offset:20480\n"
    "v_mfma_f32_16x16x32_bf16 v[164:167], v[192:195], v[104:107], v[164:167]\n"
    "ds_read_b64_tr_b16 v[218:219], v226 offset:20992\n"
    "v_mfma_f32_16x16x32_bf16 v[168:171], v[196:199], v[40:43], v[168:171]\n"
    "s_waitcnt lgkmcnt(10)\n"
    "ds_read_b64_tr_b16 v[192:193], v227 offset:20480\n"
    "v_mfma_f32_16x16x32_bf16 v[172:175], v[196:199], v[104:107], v[172:175]\n"
    "ds_read_b64_tr_b16 v[194:195], v227 offset:20992\n"
    "v_mfma_f32_16x16x32_bf16 v[176:179], v[200:203], v[40:43], v[176:179]\n"
    "s_waitcnt lgkmcnt(10)\n"
    "ds_read_b64_tr_b16 v[196:197], v224 offset:28672\n"
    "v_mfma_f32_16x16x32_bf16 v[180:183], v[200:203], v[104:107], v[180:183]\n"
    "ds_read_b64_tr_b16 v[198:199], v224 offset:29184\n"
    "v_mfma_f32_16x16x32_bf16 v[184:187], v[204:207], v[40:43], v[184:187]\n"
    "s_waitcnt lgkmcnt(7)\n"
    "ds_read_b64_tr_b16 v[200:201], v225 offset:28672\n"
    "v_mfma_f32_16x16x32_bf16 v[188:191], v[204:207], v[104:107], v[188:191]\n"
    "ds_read_b64_tr_b16 v[202:203], v225 offset:29184\n"
    "v_mfma_f32_16x16x32_bf16 v[128:131], v[208:211], v[44:47], v[128:131]\n"
    "s_waitcnt lgkmcnt(8)\n"
    "ds_read_b64_tr_b16 v[204:205], v226 offset:28672\n"
    "v_mfma_f32_16x16x32_bf16 v[132:135], v[208:211], v[108:111], v[132:135]\n"
    "ds_read_b64_tr_b16 v[206:207], v226 offset:29184\n"
    "v_mfma_f32_16x16x32_bf16 v[136:139], v[212:215], v[44:47], v[136:139]\n"
    "s_waitcnt lgkmcnt(8)\n"
    "ds_read_b64_tr_b16 v[208:209], v227 offset:28672\n"
    "v_mfma_f32_16x16x32_bf16 v[140:143], v[212:215], v[108:111], v[140:143]\n"
    "ds_read_b64_tr_b16 v[210:211], v227 offset:29184\n"
    "v_mfma_f32_16x16x32_bf16 v[144:147], v[216:219], v[44:47], v[144:147]\n"
    "s_waitcnt vmcnt(6)\n"
    "s_barrier\n"
    "s_waitcnt lgkmcnt(8)\n"
    "ds_read_b64_tr_b16 v[212:213], v224 offset:32768\n"
    "ds_read_b128 v[48:51], v228 offset:0\n"
    "v_mfma_f32_16x16x32_bf16 v[148:151], v[216:219], v[108:111], v[148:151]\n"
    "ds_read_b64_tr_b16 v[214:215], v224 offset:33280\n"
    "v_mfma_f32_16x16x32_bf16 v[152:155], v[192:195], v[44:47], v[152:155]\n"
    "s_waitcnt lgkmcnt(9)\n"
    "ds_read_b64_tr_b16 v[216:217], v225 offset:32768\n"
    "ds_read_b128 v[112:115], v228 offset:2048\n"
    "v_mfma_f32_16x16x32_bf16 v[156:159], v[192:195], v[108:111], v[156:159]\n"
    "ds_read_b64_tr_b16 v[218:219], v225 offset:33280\n"
    "v_mfma_f32_16x16x32_bf16 v[160:163], v[196:199], v[44:47], v[160:163]\n"
    "s_waitcnt lgkmcnt(10)\n"
    "ds_read_b64_tr_b16 v[192:193], v226 offset:32768\n"
    "s_add_u32 m0, s61, 0\n"
    "s_add_u32 s65, s63, 0\n"
    "buffer_load_dwordx4 v234, s[48:51], s65 offen lds\n"
    "v_mfma_f32_16x16x32_bf16 v[164:167], v[196:199], v[108:111], v[164:167]\n"
    "ds_read_b64_tr_b16 v[194:195], v226 offset:33280\n"
    "v_mfma_f32_16x16x32_bf16 v[168:171], v[200:203], v[44:47], v[168:171]\n"
    "s_waitcnt lgkmcnt(10)\n"
    "ds_read_b64_tr_b16 v[196:197], v227 offset:32768\n"
    "s_add_u32 m0, s61, 8192\n"
    "s_add_u32 s65, s63, 128\n"
    "buffer_load_dwordx4 v234, s[48:51], s65 offen lds\n"
    "v_mfma_f32_16x16x32_bf16 v[172:175], v[200:203], v[108:111], v[172:175]\n"
    "ds_read_b64_tr_b16 v[198:199], v227 offset:33280\n"
    "v_mfma_f32_16x16x32_bf16 v[176:179], v[204:207], v[44:47], v[176:179]\n"
    "s_waitcnt lgkmcnt(10)\n"
    "ds_read_b64_tr_b16 v[200:201], v224 offset:40960\n"
    "v_mfma_f32_16x16x32_bf16 v[180:183], v[204:207], v[108:111], v[180:183]\n"
    "ds_read_b64_tr_b16 v[202:203], v224 offset:41472\n"
    "v_mfma_f32_16x16x32_bf16 v[184:187], v[208:211], v[44:47], v[184:187]\n"
    "s_waitcnt lgkmcnt(7)\n"
    "ds_read_b64_tr_b16 v[204:205], v225 offset:40960\n"
    "v_mfma_f32_16x16x32_bf16 v[188:191], v[208:211], v[108:111], v[188:191]\n"
    "ds_read_b64_tr_b16 v[206:207], v225 offset:41472\n"
    "v_mfma_f32_16x16x32_bf16 v[128:131], v[212:215], v[48:51], v[128:131]\n"
    "s_waitcnt lgkmcnt(8)\n"
    "ds_read_b64_tr_b16 v[208:209], v226 offset:40960\n"
    "v_mfma_f32_16x16x32_bf16 v[132:135], v[212:215], v[112:115], v[132:135]\n"
    "ds_read_b64_tr_b16 v[210:211], v226 offset:41472\n"
    "v_mfma_f32_16x16x32_bf16 v[136:139], v[216:219], v[48:51], v[136:139]\n"
    "s_waitcnt lgkmcnt(8)\n"
    "ds_read_b64_tr_b16 v[212:213], v227 offset:40960\n"
    "v_mfma_f32_16x16x32_bf16 v[140:143], v[216:219], v[112:115], v[140:143]\n"
    "ds_read_b64_tr_b16 v[214:215], v227 offset:41472\n"
    "v_mfma_f32_16x16x32_bf16 v[144:147], v[192:195], v[48:51], v[144:147]\n"
    "s_waitcnt lgkmcnt(8)\n"
    "ds_read_b64_tr_b16 v[216:217], v224 offset:36864\n"
    "ds_read_b128 v[52:55], v229 offset:0\n"
    "v_mfma_f32_16x16x32_bf16 v[148:151], v[192:195], v[112:115], v[148:151]\n"
    "ds_read_b64_tr_b16 v[218:219], v224 offset:37376\n"
    "v_mfma_f32_16x16x32_bf16 v[152:155], v[196:199], v[48:51], v[152:155]\n"
    "s_waitcnt lgkmcnt(9)\n"
    "ds_read_b64_tr_b16 v[192:193], v225 offset:36864\n"
    "ds_read_b128 v[116:119], v229 offset:2048\n"
    "v_mfma_f32_16x16x32_bf16 v[156:159], v[196:199], v[112:115], v[156:159]\n"
    "ds_read_b64_tr_b16 v[194:195], v225 offset:37376\n"
    "v_mfma_f32_16x16x32_bf16 v[160:163], v[200:203], v[48:51], v[160:163]\n"
    "s_waitcnt lgkmcnt(10)\n"
    "ds_read_b64_tr_b16 v[196:197], v226 offset:36864\n"
    "v_mfma_f32_16x16x32_bf16 v[164:167], v[200:203], v[112:115], v[164:167]\n"
    "ds_read_b64_tr_b16 v[198:199], v226 offset:37376\n"
    "v_mfma_f32_16x16x32_bf16 v[168:171], v[204:207], v[48:51], v[168:171]\n"
    "s_waitcnt lgkmcnt(10)\n"
    "ds_read_b64_tr_b16 v[200:201], v227 offset:36864\n"
    "v_mfma_f32_16x16x32_bf16 v[172:175], v[204:207], v[112:115], v[172:175]\n"
    "ds_read_b64_tr_b16 v[202:203], v227 offset:37376\n"
    "v_mfma_f32_16x16x32_bf16 v[176:179], v[208:211], v[48:51], v[176:179]\n"
    "s_waitcnt lgkmcnt(10)\n"
    "ds_read_b64_tr_b16 v[204:205], v224 offset:45056\n"
    "v_mfma_f32_16x16x32_bf16 v[180:183], v[208:211], v[112:115], v[180:183]\n"
    "ds_read_b64_tr_b16 v[206:207], v224 offset:45568\n"
    "v_mfma_f32_16x16x32_bf16 v[184:187], v[212:215], v[48:51], v[184:187]\n"
    "s_waitcnt lgkmcnt(7)\n"
    "ds_read_b64_tr_b16 v[208:209], v225 offset:45056\n"
    "v_mfma_f32_16x16x32_bf16 v[188:191], v[212:215], v[112:115], v[188:191]\n"
    "ds_read_b64_tr_b16 v[210:211], v225 offset:45568\n"
    "v_mfma_f32_16x16x32_bf16 v[128:131], v[216:219], v[52:55], v[128:131]\n"
    "s_waitcnt lgkmcnt(8)\n"
    "ds_read_b64_tr_b16 v[212:213], v226 offset:45056\n"
    "v_mfma_f32_16x16x32_bf16 v[132:135], v[216:219], v[116:119], v[132:135]\n"
    "ds_read_b64_tr_b16 v[214:215], v226 offset:45568\n"
    "v_mfma_f32_16x16x32_bf16 v[136:139], v[192:195], v[52:55], v[136:139]\n"
    "s_waitcnt lgkmcnt(8)\n"
    "ds_read_b64_tr_b16 v[216:217], v227 offset:45056\n"
    "v_mfma_f32_16x16x32_bf16 v[140:143], v[192:195], v[116:119], v[140:143]\n"
    "ds_read_b64_tr_b16 v[218:219], v227 offset:45568\n"
    "v_mfma_f32_16x16x32_bf16 v[144:147], v[196:199], v[52:55], v[144:147]\n"
    "s_waitcnt vmcnt(2)\n"
    "s_barrier\n"
    "s_waitcnt lgkmcnt(8)\n"
    "ds_read_b64_tr_b16 v[192:193], v224 offset:49152\n"
    "ds_read_b128 v[56:59], v228 offset:32768\n"
    "v_mfma_f32_16x16x32_bf16 v[148:151], v[196:199], v[116:119], v[148:151]\n"
    "ds_read_b64_tr_b16 v[194:195], v224 offset:49664\n"
    "v_mfma_f32_16x16x32_bf16 v[152:155], v[200:203], v[52:55], v[152:155]\n"
    "s_waitcnt lgkmcnt(9)\n"
    "ds_read_b64_tr_b16 v[196:197], v225 offset:49152\n"
    "ds_read_b128 v[120:123], v228 offset:34816\n"
    "v_mfma_f32_16x16x32_bf16 v[156:159], v[200:203], v[116:119], v[156:159]\n"
    "ds_read_b64_tr_b16 v[198:199], v225 offset:49664\n"
    "v_mfma_f32_16x16x32_bf16 v[160:163], v[204:207], v[52:55], v[160:163]\n"
    "s_waitcnt lgkmcnt(10)\n"
    "ds_read_b64_tr_b16 v[200:201], v226 offset:49152\n"
    "s_add_u32 m0, s61, 16384\n"
    "s_add_u32 s65, s63, 524288\n"
    "buffer_load_dwordx4 v234, s[48:51], s65 offen lds\n"
    "v_mfma_f32_16x16x32_bf16 v[164:167], v[204:207], v[116:119], v[164:167]\n"
    "ds_read_b64_tr_b16 v[202:203], v226 offset:49664\n"
    "v_mfma_f32_16x16x32_bf16 v[168:171], v[208:211], v[52:55], v[168:171]\n"
    "s_waitcnt lgkmcnt(10)\n"
    "ds_read_b64_tr_b16 v[204:205], v227 offset:49152\n"
    "s_add_u32 m0, s61, 24576\n"
    "s_add_u32 s65, s63, 524416\n"
    "buffer_load_dwordx4 v234, s[48:51], s65 offen lds\n"
    "v_mfma_f32_16x16x32_bf16 v[172:175], v[208:211], v[116:119], v[172:175]\n"
    "ds_read_b64_tr_b16 v[206:207], v227 offset:49664\n"
    "v_mfma_f32_16x16x32_bf16 v[176:179], v[212:215], v[52:55], v[176:179]\n"
    "s_waitcnt lgkmcnt(10)\n"
    "ds_read_b64_tr_b16 v[208:209], v224 offset:57344\n"
    "v_mfma_f32_16x16x32_bf16 v[180:183], v[212:215], v[116:119], v[180:183]\n"
    "ds_read_b64_tr_b16 v[210:211], v224 offset:57856\n"
    "v_mfma_f32_16x16x32_bf16 v[184:187], v[216:219], v[52:55], v[184:187]\n"
    "s_waitcnt lgkmcnt(7)\n"
    "ds_read_b64_tr_b16 v[212:213], v225 offset:57344\n"
    "v_mfma_f32_16x16x32_bf16 v[188:191], v[216:219], v[116:119], v[188:191]\n"
    "ds_read_b64_tr_b16 v[214:215], v225 offset:57856\n"
    "v_mfma_f32_16x16x32_bf16 v[128:131], v[192:195], v[56:59], v[128:131]\n"
    "s_waitcnt lgkmcnt(8)\n"
    "ds_read_b64_tr_b16 v[216:217], v226 offset:57344\n"
    "v_mfma_f32_16x16x32_bf16 v[132:135], v[192:195], v[120:123], v[132:135]\n"
    "ds_read_b64_tr_b16 v[218:219], v226 offset:57856\n"
    "v_mfma_f32_16x16x32_bf16 v[136:139], v[196:199], v[56:59], v[136:139]\n"
    "s_waitcnt lgkmcnt(8)\n"
    "ds_read_b64_tr_b16 v[192:193], v227 offset:57344\n"
    "v_mfma_f32_16x16x32_bf16 v[140:143], v[196:199], v[120:123], v[140:143]\n"
    "ds_read_b64_tr_b16 v[194:195], v227 offset:57856\n"
    "v_mfma_f32_16x16x32_bf16 v[144:147], v[200:203], v[56:59], v[144:147]\n"
    "s_waitcnt lgkmcnt(8)\n"
    "ds_read_b64_tr_b16 v[196:197], v224 offset:53248\n"
    "ds_read_b128 v[60:63], v229 offset:32768\n"
    "v_mfma_f32_16x16x32_bf16 v[148:151], v[200:203], v[120:123], v[148:151]\n"
    "ds_read_b64_tr_b16 v[198:199], v224 offset:53760\n"
    "v_mfma_f32_16x16x32_bf16 v[152:155], v[204:207], v[56:59], v[152:155]\n"
    "s_waitcnt lgkmcnt(9)\n"
    "ds_read_b64_tr_b16 v[200:201], v225 offset:53248\n"
    "ds_read_b128 v[124:127], v229 offset:34816\n"
    "v_mfma_f32_16x16x32_bf16 v[156:159], v[204:207], v[120:123], v[156:159]\n"
    "ds_read_b64_tr_b16 v[202:203], v225 offset:53760\n"
    "v_mfma_f32_16x16x32_bf16 v[160:163], v[208:211], v[56:59], v[160:163]\n"
    "s_waitcnt lgkmcnt(10)\n"
    "ds_read_b64_tr_b16 v[204:205], v226 offset:53248\n"
    "v_mfma_f32_16x16x32_bf16 v[164:167], v[208:211], v[120:123], v[164:167]\n"
    "ds_read_b64_tr_b16 v[206:207], v226 offset:53760\n"
    "v_mfma_f32_16x16x32_bf16 v[168:171], v[212:215], v[56:59], v[168:171]\n"
    "s_waitcnt lgkmcnt(10)\n"
    "ds_read_b64_tr_b16 v[208:209], v227 offset:53248\n"
    "v_mfma_f32_16x16x32_bf16 v[172:175], v[212:215], v[120:123], v[172:175]\n"
    "ds_read_b64_tr_b16 v[210:211], v227 offset:53760\n"
    "v_mfma_f32_16x16x32_bf16 v[176:179], v[216:219], v[56:59], v[176:179]\n"
    "s_waitcnt lgkmcnt(10)\n"
    "ds_read_b64_tr_b16 v[212:213], v224 offset:61440\n"
    "v_mfma_f32_16x16x32_bf16 v[180:183], v[216:219], v[120:123], v[180:183]\n"
    "ds_read_b64_tr_b16 v[214:215], v224 offset:61952\n"
    "v_mfma_f32_16x16x32_bf16 v[184:187], v[192:195], v[56:59], v[184:187]\n"
    "s_waitcnt lgkmcnt(7)\n"
    "ds_read_b64_tr_b16 v[216:217], v225 offset:61440\n"
    "v_mfma_f32_16x16x32_bf16 v[188:191], v[192:195], v[120:123], v[188:191]\n"
    "ds_read_b64_tr_b16 v[218:219], v225 offset:61952\n"
    "v_mfma_f32_16x16x32_bf16 v[128:131], v[196:199], v[60:63], v[128:131]\n"
    "s_waitcnt lgkmcnt(8)\n"
    "ds_read_b64_tr_b16 v[192:193], v226 offset:61440\n"
    "v_mfma_f32_16x16x32_bf16 v[132:135], v[196:199], v[124:127], v[132:135]\n"
    "ds_read_b64_tr_b16 v[194:195], v226 offset:61952\n"
    "v_mfma_f32_16x16x32_bf16 v[136:139], v[200:203], v[60:63], v[136:139]\n"
    "s_waitcnt lgkmcnt(8)\n"
    "ds_read_b64_tr_b16 v[196:197], v227 offset:61440\n"
    "v_mfma_f32_16x16x32_bf16 v[140:143], v[200:203], v[124:127], v[140:143]\n"
    "ds_read_b64_tr_b16 v[198:199], v227 offset:61952\n"
    "v_mfma_f32_16x16x32_bf16 v[144:147], v[204:207], v[60:63], v[144:147]\n"
    "s_waitcnt vmcnt(2)\n"
    "s_barrier\n"
    "s_waitcnt lgkmcnt(8)\n"
    "ds_read_b64_tr_b16 v[200:201], v224 offset:0\n"
    "s_add_u32 m0, s61, 65536\n"
    "s_add_u32 s65, s63, 1048576\n"
    "buffer_load_dwordx4 v234, s[48:51], s65 offen lds\n"
    "v_mfma_f32_16x16x32_bf16 v[148:151], v[204:207], v[124:127], v[148:151]\n"
    "ds_read_b64_tr_b16 v[202:203], v224 offset:512\n"
    "v_mfma_f32_16x16x32_bf16 v[152:155], v[208:211], v[60:63], v[152:155]\n"
    "s_waitcnt lgkmcnt(8)\n"
    "ds_read_b64_tr_b16 v[204:205], v224 offset:4096\n"
    "v_mfma_f32_16x16x32_bf16 v[156:159], v[208:211], v[124:127], v[156:159]\n"
    "ds_read_b64_tr_b16 v[206:207], v224 offset:4608\n"
    "v_mfma_f32_16x16x32_bf16 v[160:163], v[212:215], v[60:63], v[160:163]\n"
    "s_waitcnt vmcnt(1)\n"
    "s_barrier\n"
    "s_waitcnt lgkmcnt(8)\n"
    "ds_read_b64_tr_b16 v[208:209], v224 offset:16384\n"
    "v_mfma_f32_16x16x32_bf16 v[164:167], v[212:215], v[124:127], v[164:167]\n"
    "ds_read_b64_tr_b16 v[210:211], v224 offset:16896\n"
    "v_mfma_f32_16x16x32_bf16 v[168:171], v[216:219], v[60:63], v[168:171]\n"
    "s_waitcnt lgkmcnt(8)\n"
    "ds_read_b64_tr_b16 v[212:213], v224 offset:20480\n"
    "v_mfma_f32_16x16x32_bf16 v[172:175], v[216:219], v[124:127], v[172:175]\n"
    "ds_read_b64_tr_b16 v[214:215], v224 offset:20992\n"
    "s_add_u32 m0, s96, 32768\n"
    "s_add_u32 s65, s94, 0\n"
    "buffer_load_dwordx4 v253, s[84:87], s65 offen nt lds\n"
    "v_mfma_f32_16x16x32_bf16 v[176:179], v[192:195], v[60:63], v[176:179]\n"
    "s_waitcnt lgkmcnt(8)\n"
    "ds_read_b64_tr_b16 v[216:217], v225 offset:0\n"
    "s_add_u32 m0, s61, 73728\n"
    "s_add_u32 s65, s63, 1048704\n"
    "buffer_load_dwordx4 v234, s[48:51], s65 offen lds\n"
    "v_mfma_f32_16x16x32_bf16 v[180:183], v[192:195], v[124:127], v[180:183]\n"
    "ds_read_b64_tr_b16 v[218:219], v225 offset:512\n"
    "v_mfma_f32_16x16x32_bf16 v[184:187], v[196:199], v[60:63], v[184:187]\n"
    "s_waitcnt lgkmcnt(8)\n"
    "ds_read_b64_tr_b16 v[192:193], v225 offset:4096\n"
    "v_mfma_f32_16x16x32_bf16 v[188:191], v[196:199], v[124:127], v[188:191]\n"
    "ds_read_b64_tr_b16 v[194:195], v225 offset:4608\n"
    "v_cvt_pk_bf16_f32 v220, v128, v129\n"
    "v_cvt_pk_bf16_f32 v221, v130, v131\n"
    "v_cvt_pk_bf16_f32 v222, v136, v137\n"
    "v_cvt_pk_bf16_f32 v223, v138, v139\n"
    "v_cvt_pk_bf16_f32 v236, v132, v133\n"
    "v_cvt_pk_bf16_f32 v237, v134, v135\n"
    "v_cvt_pk_bf16_f32 v238, v140, v141\n"
    "v_cvt_pk_bf16_f32 v239, v142, v143\n"
    "buffer_store_dwordx4 v[220:223], v235, s[52:55], 0 offen offset:0 sc1\n"
    "v_mfma_f32_16x16x32_bf16 v[128:131], v[0:3], v[200:203], 0\n"
    "s_waitcnt lgkmcnt(8)\n"
    "ds_read_b64_tr_b16 v[196:197], v225 offset:16384\n"
    "v_mfma_f32_16x16x32_bf16 v[132:135], v[64:67], v[200:203], 0\n"
    "ds_read_b64_tr_b16 v[198:199], v225 offset:16896\n"
    "v_mfma_f32_16x16x32_bf16 v[128:131], v[4:7], v[204:207], v[128:131]\n"
    "s_waitcnt lgkmcnt(8)\n"
    "ds_read_b64_tr_b16 v[200:201], v225 offset:20480\n"
    "v_mfma_f32_16x16x32_bf16 v[132:135], v[68:71], v[204:207], v[132:135]\n"
    "ds_read_b64_tr_b16 v[202:203], v225 offset:20992\n"
    "v_mfma_f32_16x16x32_bf16 v[128:131], v[8:11], v[208:211], v[128:131]\n"
    "s_waitcnt lgkmcnt(8)\n"
    "ds_read_b64_tr_b16 v[204:205], v226 offset:0\n"
    "s_add_u32 m0, s61, 81920\n"
    "s_add_u32 s65, s63, 1572864\n"
    "buffer_load_dwordx4 v234, s[48:51], s65 offen lds\n"
    "v_mfma_f32_16x16x32_bf16 v[132:135], v[72:75], v[208:211], v[132:135]\n"
    "ds_read_b64_tr_b16 v[206:207], v226 offset:512\n"
    "v_mfma_f32_16x16x32_bf16 v[128:131], v[12:15], v[212:215], v[128:131]\n"
    "s_waitcnt lgkmcnt(8)\n"
    "ds_read_b64_tr_b16 v[208:209], v226 offset:4096\n"
    "v_mfma_f32_16x16x32_bf16 v[132:135], v[76:79], v[212:215], v[132:135]\n"
    "ds_read_b64_tr_b16 v[210:211], v226 offset:4608\n"
    "v_mfma_f32_16x16x32_bf16 v[136:139], v[0:3], v[216:219], 0\n"
    "s_waitcnt lgkmcnt(8)\n"
    "ds_read_b64_tr_b16 v[212:213], v226 offset:16384\n"
    "v_mfma_f32_16x16x32_bf16 v[140:143], v[64:67], v[216:219], 0\n"
    "ds_read_b64_tr_b16 v[214:215], v226 offset:16896\n"
    "v_mfma_f32_16x16x32_bf16 v[136:139], v[4:7], v[192:195], v[136:139]\n"
    "s_waitcnt lgkmcnt(8)\n"
    "ds_read_b64_tr_b16 v[216:217], v226 offset:20480\n"
    "v_mfma_f32_16x16x32_bf16 v[140:143], v[68:71], v[192:195], v[140:143]\n"
    "ds_read_b64_tr_b16 v[218:219], v226 offset:20992\n"
    "v_mfma_f32_16x16x32_bf16 v[136:139], v[8:11], v[196:199], v[136:139]\n"
    "s_waitcnt lgkmcnt(8)\n"
    "ds_read_b64_tr_b16 v[192:193], v227 offset:0\n"
    "s_add_u32 m0, s61, 90112\n"
    "s_add_u32 s65, s63, 1572992\n"
    "buffer_load_dwordx4 v234, s[48:51], s65 offen lds\n"
    "v_mfma_f32_16x16x32_bf16 v[140:143], v[72:75], v[196:199], v[140:143]\n"
    "ds_read_b64_tr_b16 v[194:195], v227 offset:512\n"
    "v_mfma_f32_16x16x32_bf16 v[136:139], v[12:15], v[200:203], v[136:139]\n"
    "s_waitcnt lgkmcnt(8)\n"
    "ds_read_b64_tr_b16 v[196:197], v227 offset:4096\n"
    "v_mfma_f32_16x16x32_bf16 v[140:143], v[76:79], v[200:203], v[140:143]\n"
    "ds_read_b64_tr_b16 v[198:199], v227 offset:4608\n"
    "v_cvt_pk_bf16_f32 v220, v144, v145\n"
    "v_cvt_pk_bf16_f32 v221, v146, v147\n"
    "v_cvt_pk_bf16_f32 v222, v152, v153\n"
    "v_cvt_pk_bf16_f32 v223, v154, v155\n"
    "v_cvt_pk_bf16_f32 v240, v148, v149\n"
    "v_cvt_pk_bf16_f32 v241, v150, v151\n"
    "v_cvt_pk_bf16_f32 v242, v156, v157\n"
    "v_cvt_pk_bf16_f32 v243, v158, v159\n"
    "buffer_store_dwordx4 v[220:223], v235, s[52:55], 0 offen offset:1024 sc1\n"
    "v_mfma_f32_16x16x32_bf16 v[144:147], v[0:3], v[204:207], 0\n"
    "s_waitcnt lgkmcnt(8)\n"
    "ds_read_b64_tr_b16 v[200:201], v227 offset:16384\n"
    "v_mfma_f32_16x16x32_bf16 v[148:151], v[64:67], v[204:207], 0\n"
    "ds_read_b64_tr_b16 v[202:203], v227 offset:16896\n"
    "s_add_u32 m0, s96, 33792\n"
    "s_add_u32 s65, s94, 2048\n"
    "buffer_load_dwordx4 v253, s[84:87], s65 offen nt lds\n"
    "v_mfma_f32_16x16x32_bf16 v[144:147], v[4:7], v[208:211], v[144:147]\n"
    "s_waitcnt lgkmcnt(8)\n"
    "ds_read_b64_tr_b16 v[204:205], v227 offset:20480\n"
    "v_mfma_f32_16x16x32_bf16 v[148:151], v[68:71], v[208:211], v[148:151]\n"
    "ds_read_b64_tr_b16 v[206:207], v227 offset:20992\n"
    "v_mfma_f32_16x16x32_bf16 v[144:147], v[8:11], v[212:215], v[144:147]\n"
    "s_waitcnt lgkmcnt(8)\n"
    "ds_read_b64_tr_b16 v[208:209], v224 offset:8192\n"
    "s_add_u32 m0, s61, 98304\n"
    "s_add_u32 s65, s63, 2097152\n"
    "buffer_load_dwordx4 v234, s[48:51], s65 offen lds\n"
    "v_mfma_f32_16x16x32_bf16 v[148:151], v[72:75], v[212:215], v[148:151]\n"
    "ds_read_b64_tr_b16 v[210:211], v224 offset:8704\n"
    "v_mfma_f32_16x16x32_bf16 v[144:147], v[12:15], v[216:219], v[144:147]\n"
    "s_waitcnt lgkmcnt(8)\n"
    "ds_read_b64_tr_b16 v[212:213], v224 offset:12288\n"
    "v_mfma_f32_16x16x32_bf16 v[148:151], v[76:79], v[216:219], v[148:151]\n"
    "ds_read_b64_tr_b16 v[214:215], v224 offset:12800\n"
    "v_mfma_f32_16x16x32_bf16 v[152:155], v[0:3], v[192:195], 0\n"
    "s_waitcnt lgkmcnt(8)\n"
    "ds_read_b64_tr_b16 v[216:217], v224 offset:24576\n"
    "v_mfma_f32_16x16x32_bf16 v[156:159], v[64:67], v[192:195], 0\n"
    "ds_read_b64_tr_b16 v[218:219], v224 offset:25088\n"
    "v_mfma_f32_16x16x32_bf16 v[152:155], v[4:7], v[196:199], v[152:155]\n"
    "s_waitcnt lgkmcnt(8)\n"
    "ds_read_b64_tr_b16 v[192:193], v224 offset:28672\n"
    "v_mfma_f32_16x16x32_bf16 v[156:159], v[68:71], v[196:199], v[156:159]\n"
    "ds_read_b64_tr_b16 v[194:195], v224 offset:29184\n"
    "v_mfma_f32_16x16x32_bf16 v[152:155], v[8:11], v[200:203], v[152:155]\n"
    "s_waitcnt lgkmcnt(8)\n"
    "ds_read_b64_tr_b16 v[196:197], v225 offset:8192\n"
    "s_add_u32 m0, s61, 106496\n"
    "s_add_u32 s65, s63, 2097280\n"
    "buffer_load_dwordx4 v234, s[48:51], s65 offen lds\n"
    "v_mfma_f32_16x16x32_bf16 v[156:159], v[72:75], v[200:203], v[156:159]\n"
    "ds_read_b64_tr_b16 v[198:199], v225 offset:8704\n"
    "v_mfma_f32_16x16x32_bf16 v[152:155], v[12:15], v[204:207], v[152:155]\n"
    "s_waitcnt lgkmcnt(8)\n"
    "ds_read_b64_tr_b16 v[200:201], v225 offset:12288\n"
    "v_mfma_f32_16x16x32_bf16 v[156:159], v[76:79], v[204:207], v[156:159]\n"
    "ds_read_b64_tr_b16 v[202:203], v225 offset:12800\n"
    "v_cvt_pk_bf16_f32 v220, v160, v161\n"
    "v_cvt_pk_bf16_f32 v221, v162, v163\n"
    "v_cvt_pk_bf16_f32 v222, v168, v169\n"
    "v_cvt_pk_bf16_f32 v223, v170, v171\n"
    "v_cvt_pk_bf16_f32 v244, v164, v165\n"
    "v_cvt_pk_bf16_f32 v245, v166, v167\n"
    "v_cvt_pk_bf16_f32 v246, v172, v173\n"
    "v_cvt_pk_bf16_f32 v247, v174, v175\n"
    "buffer_store_dwordx4 v[220:223], v235, s[52:55], 0 offen offset:2048 sc1\n"
    "v_mfma_f32_16x16x32_bf16 v[160:163], v[0:3], v[208:211], 0\n"
    "s_waitcnt lgkmcnt(8)\n"
    "ds_read_b64_tr_b16 v[204:205], v225 offset:24576\n"
    "v_mfma_f32_16x16x32_bf16 v[164:167], v[64:67], v[208:211], 0\n"
    "ds_read_b64_tr_b16 v[206:207], v225 offset:25088\n"
    "v_mfma_f32_16x16x32_bf16 v[160:163], v[4:7], v[212:215], v[160:163]\n"
    "s_waitcnt lgkmcnt(8)\n"
    "ds_read_b64_tr_b16 v[208:209], v225 offset:28672\n"
    "v_mfma_f32_16x16x32_bf16 v[164:167], v[68:71], v[212:215], v[164:167]\n"
    "ds_read_b64_tr_b16 v[210:211], v225 offset:29184\n"
    "v_mfma_f32_16x16x32_bf16 v[160:163], v[8:11], v[216:219], v[160:163]\n"
    "s_waitcnt lgkmcnt(8)\n"
    "ds_read_b64_tr_b16 v[212:213], v226 offset:8192\n"
    "s_add_u32 m0, s61, 114688\n"
    "s_add_u32 s65, s63, 2621440\n"
    "buffer_load_dwordx4 v234, s[48:51], s65 offen lds\n"
    "v_mfma_f32_16x16x32_bf16 v[164:167], v[72:75], v[216:219], v[164:167]\n"
    "ds_read_b64_tr_b16 v[214:215], v226 offset:8704\n"
    "v_mfma_f32_16x16x32_bf16 v[160:163], v[12:15], v[192:195], v[160:163]\n"
    "s_waitcnt lgkmcnt(8)\n"
    "ds_read_b64_tr_b16 v[216:217], v226 offset:12288\n"
    "v_mfma_f32_16x16x32_bf16 v[164:167], v[76:79], v[192:195], v[164:167]\n"
    "ds_read_b64_tr_b16 v[218:219], v226 offset:12800\n"
    "v_mfma_f32_16x16x32_bf16 v[168:171], v[0:3], v[196:199], 0\n"
    "s_waitcnt lgkmcnt(8)\n"
    "ds_read_b64_tr_b16 v[192:193], v226 offset:24576\n"
    "v_mfma_f32_16x16x32_bf16 v[172:175], v[64:67], v[196:199], 0\n"
    "ds_read_b64_tr_b16 v[194:195], v226 offset:25088\n"
    "v_mfma_f32_16x16x32_bf16 v[168:171], v[4:7], v[200:203], v[168:171]\n"
    "s_waitcnt lgkmcnt(8)\n"
    "ds_read_b64_tr_b16 v[196:197], v226 offset:28672\n"
    "v_mfma_f32_16x16x32_bf16 v[172:175], v[68:71], v[200:203], v[172:175]\n"
    "ds_read_b64_tr_b16 v[198:199], v226 offset:29184\n"
    "v_mfma_f32_16x16x32_bf16 v[168:171], v[8:11], v[204:207], v[168:171]\n"
    "s_waitcnt lgkmcnt(8)\n"
    "ds_read_b64_tr_b16 v[200:201], v227 offset:8192\n"
    "s_add_u32 m0, s61, 122880\n"
    "s_add_u32 s65, s63, 2621568\n"
    "buffer_load_dwordx4 v234, s[48:51], s65 offen lds\n"
    "v_mfma_f32_16x16x32_bf16 v[172:175], v[72:75], v[204:207], v[172:175]\n"
    "ds_read_b64_tr_b16 v[202:203], v227 offset:8704\n"
    "v_mfma_f32_16x16x32_bf16 v[168:171], v[12:15], v[208:211], v[168:171]\n"
    "s_waitcnt lgkmcnt(8)\n"
    "ds_read_b64_tr_b16 v[204:205], v227 offset:12288\n"
    "v_mfma_f32_16x16x32_bf16 v[172:175], v[76:79], v[208:211], v[172:175]\n"
    "ds_read_b64_tr_b16 v[206:207], v227 offset:12800\n"
    "v_cvt_pk_bf16_f32 v220, v176, v177\n"
    "v_cvt_pk_bf16_f32 v221, v178, v179\n"
    "v_cvt_pk_bf16_f32 v222, v184, v185\n"
    "v_cvt_pk_bf16_f32 v223, v186, v187\n"
    "v_cvt_pk_bf16_f32 v248, v180, v181\n"
    "v_cvt_pk_bf16_f32 v249, v182, v183\n"
    "v_cvt_pk_bf16_f32 v250, v188, v189\n"
    "v_cvt_pk_bf16_f32 v251, v190, v191\n"
    "buffer_store_dwordx4 v[220:223], v235, s[52:55], 0 offen offset:3072 sc1\n"
    "v_mfma_f32_16x16x32_bf16 v[176:179], v[0:3], v[212:215], 0\n"
    "s_waitcnt lgkmcnt(8)\n"
    "ds_read_b64_tr_b16 v[208:209], v227 offset:24576\n"
    "v_mfma_f32_16x16x32_bf16 v[180:183], v[64:67], v[212:215], 0\n"
    "ds_read_b64_tr_b16 v[210:211], v227 offset:25088\n"
    "v_mfma_f32_16x16x32_bf16 v[176:179], v[4:7], v[216:219], v[176:179]\n"
    "s_waitcnt lgkmcnt(8)\n"
    "ds_read_b64_tr_b16 v[212:213], v227 offset:28672\n"
    "v_mfma_f32_16x16x32_bf16 v[180:183], v[68:71], v[216:219], v[180:183]\n"
    "ds_read_b64_tr_b16 v[214:215], v227 offset:29184\n"
    "v_mfma_f32_16x16x32_bf16 v[176:179], v[8:11], v[192:195], v[176:179]\n"
    "s_waitcnt vmcnt(11)\n"
    "s_barrier\n"
    "s_waitcnt lgkmcnt(8)\n"
    "v_add_u32 v224, 65536, v224\n"
    "v_add_u32 v225, 65536, v225\n"
    "v_add_u32 v226, 65536, v226\n"
    "v_add_u32 v227, 65536, v227\n"
    "ds_read_b64_tr_b16 v[216:217], v224 offset:0\n"
    "s_add_u32 m0, s61, 131072\n"
    "s_add_u32 s65, s63, 3145728\n"
    "buffer_load_dwordx4 v234, s[48:51], s65 offen lds\n"
    "v_mfma_f32_16x16x32_bf16 v[180:183], v[72:75], v[192:195], v[180:183]\n"
    "ds_read_b64_tr_b16 v[218:219], v224 offset:512\n"
    "v_mfma_f32_16x16x32_bf16 v[176:179], v[12:15], v[196:199], v[176:179]\n"
    "s_waitcnt lgkmcnt(8)\n"
    "ds_read_b64_tr_b16 v[192:193], v225 offset:0\n"
    "v_mfma_f32_16x16x32_bf16 v[180:183], v[76:79], v[196:199], v[180:183]\n"
    "ds_read_b64_tr_b16 v[194:195], v225 offset:512\n"
    "s_add_u32 m0, s61, 49152\n"
    "s_add_u32 s65, s95, 0\n"
    "buffer_load_dwordx4 v235, s[80:83], s65 offen lds\n"
    "v_mfma_f32_16x16x32_bf16 v[184:187], v[0:3], v[200:203], 0\n"
    "s_waitcnt lgkmcnt(8)\n"
    "ds_read_b64_tr_b16 v[196:197], v226 offset:0\n"
    "v_mfma_f32_16x16x32_bf16 v[188:191], v[64:67], v[200:203], 0\n"
    "ds_read_b64_tr_b16 v[198:199], v226 offset:512\n"
    "v_mfma_f32_16x16x32_bf16 v[184:187], v[4:7], v[204:207], v[184:187]\n"
    "s_waitcnt lgkmcnt(8)\n"
    "ds_read_b64_tr_b16 v[200:201], v227 offset:0\n"
    "v_mfma_f32_16x16x32_bf16 v[188:191], v[68:71], v[204:207], v[188:191]\n"
    "ds_read_b64_tr_b16 v[202:203], v227 offset:512\n"
    "v_mfma_f32_16x16x32_bf16 v[184:187], v[8:11], v[208:211], v[184:187]\n"
    "s_waitcnt lgkmcnt(8)\n"
    "ds_read_b64_tr_b16 v[204:205], v224 offset:8192\n"
    "s_add_u32 m0, s61, 139264\n"
    "s_add_u32 s65, s63, 3145856\n"
    "buffer_load_dwordx4 v234, s[48:51], s65 offen lds\n"
    "v_mfma_f32_16x16x32_bf16 v[188:191], v[72:75], v[208:211], v[188:191]\n"
    "ds_read_b64_tr_b16 v[206:207], v224 offset:8704\n"
    "v_mfma_f32_16x16x32_bf16 v[184:187], v[12:15], v[212:215], v[184:187]\n"
    "s_waitcnt lgkmcnt(8)\n"
    "ds_read_b64_tr_b16 v[208:209], v225 offset:8192\n"
    "v_mfma_f32_16x16x32_bf16 v[188:191], v[76:79], v[212:215], v[188:191]\n"
    "ds_read_b64_tr_b16 v[210:211], v225 offset:8704\n"
    "v_mfma_f32_16x16x32_bf16 v[128:131], v[16:19], v[216:219], v[128:131]\n"
    "s_waitcnt lgkmcnt(8)\n"
    "ds_read_b64_tr_b16 v[212:213], v226 offset:8192\n"
    "v_mfma_f32_16x16x32_bf16 v[132:135], v[80:83], v[216:219], v[132:135]\n"
    "ds_read_b64_tr_b16 v[214:215], v226 offset:8704\n"
    "v_mfma_f32_16x16x32_bf16 v[136:139], v[16:19], v[192:195], v[136:139]\n"
    "s_waitcnt lgkmcnt(8)\n"
    "ds_read_b64_tr_b16 v[216:217], v227 offset:8192\n"
    "v_mfma_f32_16x16x32_bf16 v[140:143], v[80:83], v[192:195], v[140:143]\n"
    "ds_read_b64_tr_b16 v[218:219], v227 offset:8704\n"
    "v_mfma_f32_16x16x32_bf16 v[144:147], v[16:19], v[196:199], v[144:147]\n"
    "s_waitcnt lgkmcnt(8)\n"
    "ds_read_b64_tr_b16 v[192:193], v224 offset:4096\n"
    "s_add_u32 m0, s61, 147456\n"
    "s_add_u32 s65, s63, 3670016\n"
    "buffer_load_dwordx4 v234, s[48:51], s65 offen lds\n"
    "v_mfma_f32_16x16x32_bf16 v[148:151], v[80:83], v[196:199], v[148:151]\n"
    "ds_read_b64_tr_b16 v[194:195], v224 offset:4608\n"
    "v_mfma_f32_16x16x32_bf16 v[152:155], v[16:19], v[200:203], v[152:155]\n"
    "s_waitcnt lgkmcnt(8)\n"
    "ds_read_b64_tr_b16 v[196:197], v225 offset:4096\n"
    "v_mfma_f32_16x16x32_bf16 v[156:159], v[80:83], v[200:203], v[156:159]\n"
    "ds_read_b64_tr_b16 v[198:199], v225 offset:4608\n"
    "buffer_store_dwordx4 v[236:239], v235, s[52:55], s98 offen offset:0 sc1\n"
    "v_mfma_f32_16x16x32_bf16 v[160:163], v[16:19], v[204:207], v[160:163]\n"
    "s_waitcnt lgkmcnt(8)\n"
    "ds_read_b64_tr_b16 v[200:201], v226 offset:4096\n"
    "v_mfma_f32_16x16x32_bf16 v[164:167], v[80:83], v[204:207], v[164:167]\n"
    "ds_read_b64_tr_b16 v[202:203], v226 offset:4608\n"
    "v_mfma_f32_16x16x32_bf16 v[168:171], v[16:19], v[208:211], v[168:171]\n"
    "s_waitcnt lgkmcnt(8)\n"
    "ds_read_b64_tr_b16 v[204:205], v227 offset:4096\n"
    "v_mfma_f32_16x16x32_bf16 v[172:175], v[80:83], v[208:211], v[172:175]\n"
    "ds_read_b64_tr_b16 v[206:207], v227 offset:4608\n"
    "v_mfma_f32_16x16x32_bf16 v[176:179], v[16:19], v[212:215], v[176:179]\n"
    "s_waitcnt lgkmcnt(8)\n"
    "ds_read_b64_tr_b16 v[208:209], v224 offset:12288\n"
    "s_add_u32 m0, s61, 155648\n"
    "s_add_u32 s65, s63, 3670144\n"
    "buffer_load_dwordx4 v234, s[48:51], s65 offen lds\n"
    "v_mfma_f32_16x16x32_bf16 v[180:183], v[80:83], v[212:215], v[180:183]\n"
    "ds_read_b64_tr_b16 v[210:211], v224 offset:12800\n"
    "v_mfma_f32_16x16x32_bf16 v[184:187], v[16:19], v[216:219], v[184:187]\n"
    "s_waitcnt lgkmcnt(8)\n"
    "ds_read_b64_tr_b16 v[212:213], v225 offset:12288\n"
    "v_mfma_f32_16x16x32_bf16 v[188:191], v[80:83], v[216:219], v[188:191]\n"
    "ds_read_b64_tr_b16 v[214:215], v225 offset:12800\n"
    "v_mfma_f32_16x16x32_bf16 v[128:131], v[20:23], v[192:195], v[128:131]\n"
    "s_waitcnt lgkmcnt(8)\n"
    "ds_read_b64_tr_b16 v[216:217], v226 offset:12288\n"
    "v_mfma_f32_16x16x32_bf16 v[132:135], v[84:87], v[192:195], v[132:135]\n"
    "ds_read_b64_tr_b16 v[218:219], v226 offset:12800\n"
    "v_mfma_f32_16x16x32_bf16 v[136:139], v[20:23], v[196:199], v[136:139]\n"
    "s_waitcnt lgkmcnt(8)\n"
    "ds_read_b64_tr_b16 v[192:193], v227 offset:12288\n"
    "v_mfma_f32_16x16x32_bf16 v[140:143], v[84:87], v[196:199], v[140:143]\n"
    "ds_read_b64_tr_b16 v[194:195], v227 offset:12800\n"
    "v_mfma_f32_16x16x32_bf16 v[144:147], v[20:23], v[200:203], v[144:147]\n"
    "s_waitcnt vmcnt(14)\n"
    "s_barrier\n"
    "s_waitcnt lgkmcnt(8)\n"
    "ds_read_b64_tr_b16 v[196:197], v224 offset:16384\n"
    "v_mfma_f32_16x16x32_bf16 v[148:151], v[84:87], v[200:203], v[148:151]\n"
    "ds_read_b64_tr_b16 v[198:199], v224 offset:16896\n"
    "v_mfma_f32_16x16x32_bf16 v[152:155], v[20:23], v[204:207], v[152:155]\n"
    "s_waitcnt lgkmcnt(8)\n"
    "ds_read_b64_tr_b16 v[200:201], v225 offset:16384\n"
    "v_mfma_f32_16x16x32_bf16 v[156:159], v[84:87], v[204:207], v[156:159]\n"
    "ds_read_b64_tr_b16 v[202:203], v225 offset:16896\n"
    "s_add_u32 m0, s61, 1024\n"
    "s_add_u32 s65, s93, 0\n"
    "buffer_load_dwordx4 v252, s[72:75], s65 offen nt lds\n"
    "buffer_store_dwordx4 v[240:243], v235, s[52:55], s98 offen offset:1024 sc1\n"
    "v_mfma_f32_16x16x32_bf16 v[160:163], v[20:23], v[208:211], v[160:163]\n"
    "s_waitcnt lgkmcnt(8)\n"
    "ds_read_b64_tr_b16 v[204:205], v226 offset:16384\n"
    "v_mfma_f32_16x16x32_bf16 v[164:167], v[84:87], v[208:211], v[164:167]\n"
    "ds_read_b64_tr_b16 v[206:207], v226 offset:16896\n"
    "v_mfma_f32_16x16x32_bf16 v[168:171], v[20:23], v[212:215], v[168:171]\n"
    "s_waitcnt lgkmcnt(8)\n"
    "ds_read_b64_tr_b16 v[208:209], v227 offset:16384\n"
    "v_mfma_f32_16x16x32_bf16 v[172:175], v[84:87], v[212:215], v[172:175]\n"
    "ds_read_b64_tr_b16 v[210:211], v227 offset:16896\n"
    "v_mfma_f32_16x16x32_bf16 v[176:179], v[20:23], v[216:219], v[176:179]\n"
    "s_waitcnt lgkmcnt(8)\n"
    "ds_read_b64_tr_b16 v[212:213], v224 offset:24576\n"
    "v_mfma_f32_16x16x32_bf16 v[180:183], v[84:87], v[216:219], v[180:183]\n"
    "ds_read_b64_tr_b16 v[214:215], v224 offset:25088\n"
    "v_mfma_f32_16x16x32_bf16 v[184:187], v[20:23], v[192:195], v[184:187]\n"
    "s_waitcnt lgkmcnt(8)\n"
    "ds_read_b64_tr_b16 v[216:217], v225 offset:24576\n"
    "v_mfma_f32_16x16x32_bf16 v[188:191], v[84:87], v[192:195], v[188:191]\n"
    "ds_read_b64_tr_b16 v[218:219], v225 offset:25088\n"
    "v_mfma_f32_16x16x32_bf16 v[128:131], v[24:27], v[196:199], v[128:131]\n"
    "s_waitcnt lgkmcnt(8)\n"
    "ds_read_b64_tr_b16 v[192:193], v226 offset:24576\n"
    "v_mfma_f32_16x16x32_bf16 v[132:135], v[88:91], v[196:199], v[132:135]\n"
    "ds_read_b64_tr_b16 v[194:195], v226 offset:25088\n"
    "v_mfma_f32_16x16x32_bf16 v[136:139], v[24:27], v[200:203], v[136:139]\n"
    "s_waitcnt lgkmcnt(8)\n"
    "ds_read_b64_tr_b16 v[196:197], v227 offset:24576\n"
    "v_mfma_f32_16x16x32_bf16 v[140:143], v[88:91], v[200:203], v[140:143]\n"
    "ds_read_b64_tr_b16 v[198:199], v227 offset:25088\n"
    "v_mfma_f32_16x16x32_bf16 v[144:147], v[24:27], v[204:207], v[144:147]\n"
    "s_waitcnt lgkmcnt(8)\n"
    "ds_read_b64_tr_b16 v[200:201], v224 offset:20480\n"
    "v_mfma_f32_16x16x32_bf16 v[148:151], v[88:91], v[204:207], v[148:151]\n"
    "ds_read_b64_tr_b16 v[202:203], v224 offset:20992\n"
    "v_mfma_f32_16x16x32_bf16 v[152:155], v[24:27], v[208:211], v[152:155]\n"
    "s_waitcnt lgkmcnt(8)\n"
    "ds_read_b64_tr_b16 v[204:205], v225 offset:20480\n"
    "v_mfma_f32_16x16x32_bf16 v[156:159], v[88:91], v[208:211], v[156:159]\n"
    "ds_read_b64_tr_b16 v[206:207], v225 offset:20992\n"
    "buffer_store_dwordx4 v[244:247], v235, s[52:55], s98 offen offset:2048 sc1\n"
    "v_mfma_f32_16x16x32_bf16 v[160:163], v[24:27], v[212:215], v[160:163]\n"
    "s_waitcnt lgkmcnt(8)\n"
    "ds_read_b64_tr_b16 v[208:209], v226 offset:20480\n"
    "v_mfma_f32_16x16x32_bf16 v[164:167], v[88:91], v[212:215], v[164:167]\n"
    "ds_read_b64_tr_b16 v[210:211], v226 offset:20992\n"
    "v_mfma_f32_16x16x32_bf16 v[168:171], v[24:27], v[216:219], v[168:171]\n"
    "s_waitcnt lgkmcnt(8)\n"
    "ds_read_b64_tr_b16 v[212:213], v227 offset:20480\n"
    "v_mfma_f32_16x16x32_bf16 v[172:175], v[88:91], v[216:219], v[172:175]\n"
    "ds_read_b64_tr_b16 v[214:215], v227 offset:20992\n"
    "v_mfma_f32_16x16x32_bf16 v[176:179], v[24:27], v[192:195], v[176:179]\n"
    "s_waitcnt lgkmcnt(8)\n"
    "ds_read_b64_tr_b16 v[216:217], v224 offset:28672\n"
    "v_mfma_f32_16x16x32_bf16 v[180:183], v[88:91], v[192:195], v[180:183]\n"
    "ds_read_b64_tr_b16 v[218:219], v224 offset:29184\n"
    "s_add_u32 m0, s61, 9216\n"
    "s_add_u32 s65, s93, 0\n"
    "buffer_load_dwordx4 v252, s[76:79], s65 offen nt lds\n"
    "v_mfma_f32_16x16x32_bf16 v[184:187], v[24:27], v[196:199], v[184:187]\n"
    "s_waitcnt lgkmcnt(8)\n"
    "ds_read_b64_tr_b16 v[192:193], v225 offset:28672\n"
    "v_mfma_f32_16x16x32_bf16 v[188:191], v[88:91], v[196:199], v[188:191]\n"
    "ds_read_b64_tr_b16 v[194:195], v225 offset:29184\n"
    "v_mfma_f32_16x16x32_bf16 v[128:131], v[28:31], v[200:203], v[128:131]\n"
    "s_waitcnt lgkmcnt(8)\n"
    "ds_read_b64_tr_b16 v[196:197], v226 offset:28672\n"
    "v_mfma_f32_16x16x32_bf16 v[132:135], v[92:95], v[200:203], v[132:135]\n"
    "ds_read_b64_tr_b16 v[198:199], v226 offset:29184\n"
    "v_mfma_f32_16x16x32_bf16 v[136:139], v[28:31], v[204:207], v[136:139]\n"
    "s_waitcnt lgkmcnt(8)\n"
    "ds_read_b64_tr_b16 v[200:201], v227 offset:28672\n"
    "v_mfma_f32_16x16x32_bf16 v[140:143], v[92:95], v[204:207], v[140:143]\n"
    "ds_read_b64_tr_b16 v[202:203], v227 offset:29184\n"
    "v_mfma_f32_16x16x32_bf16 v[144:147], v[28:31], v[208:211], v[144:147]\n"
    "s_waitcnt vmcnt(14)\n"
    "s_barrier\n"
    "s_waitcnt lgkmcnt(8)\n"
    "ds_read_b64_tr_b16 v[204:205], v224 offset:32768\n"
    "v_mfma_f32_16x16x32_bf16 v[148:151], v[92:95], v[208:211], v[148:151]\n"
    "ds_read_b64_tr_b16 v[206:207], v224 offset:33280\n"
    "v_mfma_f32_16x16x32_bf16 v[152:155], v[28:31], v[212:215], v[152:155]\n"
    "s_waitcnt lgkmcnt(8)\n"
    "ds_read_b64_tr_b16 v[208:209], v225 offset:32768\n"
    "v_mfma_f32_16x16x32_bf16 v[156:159], v[92:95], v[212:215], v[156:159]\n"
    "ds_read_b64_tr_b16 v[210:211], v225 offset:33280\n"
    "buffer_store_dwordx4 v[248:251], v235, s[52:55], s98 offen offset:3072 sc1\n"
    "v_mfma_f32_16x16x32_bf16 v[160:163], v[28:31], v[216:219], v[160:163]\n"
    "s_waitcnt lgkmcnt(8)\n"
    "ds_read_b64_tr_b16 v[212:213], v226 offset:32768\n"
    "v_mfma_f32_16x16x32_bf16 v[164:167], v[92:95], v[216:219], v[164:167]\n"
    "ds_read_b64_tr_b16 v[214:215], v226 offset:33280\n"
    "v_mfma_f32_16x16x32_bf16 v[168:171], v[28:31], v[192:195], v[168:171]\n"
    "s_waitcnt lgkmcnt(8)\n"
    "ds_read_b64_tr_b16 v[216:217], v227 offset:32768\n"
    "v_mfma_f32_16x16x32_bf16 v[172:175], v[92:95], v[192:195], v[172:175]\n"
    "ds_read_b64_tr_b16 v[218:219], v227 offset:33280\n"
    "v_mfma_f32_16x16x32_bf16 v[176:179], v[28:31], v[196:199], v[176:179]\n"
    "s_waitcnt lgkmcnt(8)\n"
    "ds_read_b64_tr_b16 v[192:193], v224 offset:40960\n"
    "v_mfma_f32_16x16x32_bf16 v[180:183], v[92:95], v[196:199], v[180:183]\n"
    "ds_read_b64_tr_b16 v[194:195], v224 offset:41472\n"
    "v_mfma_f32_16x16x32_bf16 v[184:187], v[28:31], v[200:203], v[184:187]\n"
    "s_waitcnt lgkmcnt(8)\n"
    "ds_read_b64_tr_b16 v[196:197], v225 offset:40960\n"
    "v_mfma_f32_16x16x32_bf16 v[188:191], v[92:95], v[200:203], v[188:191]\n"
    "ds_read_b64_tr_b16 v[198:199], v225 offset:41472\n"
    "v_mfma_f32_16x16x32_bf16 v[128:131], v[32:35], v[204:207], v[128:131]\n"
    "s_waitcnt lgkmcnt(8)\n"
    "ds_read_b64_tr_b16 v[200:201], v226 offset:40960\n"
    "v_mfma_f32_16x16x32_bf16 v[132:135], v[96:99], v[204:207], v[132:135]\n"
    "ds_read_b64_tr_b16 v[202:203], v226 offset:41472\n"
    "v_mfma_f32_16x16x32_bf16 v[136:139], v[32:35], v[208:211], v[136:139]\n"
    "s_waitcnt lgkmcnt(8)\n"
    "ds_read_b64_tr_b16 v[204:205], v227 offset:40960\n"
    "v_mfma_f32_16x16x32_bf16 v[140:143], v[96:99], v[208:211], v[140:143]\n"
    "ds_read_b64_tr_b16 v[206:207], v227 offset:41472\n"
    "s_add_u32 m0, s61, 17408\n"
    "s_add_u32 s65, s93, s97\n"
    "buffer_load_dwordx4 v252, s[80:83], s65 offen lds\n"
    "v_mfma_f32_16x16x32_bf16 v[144:147], v[32:35], v[212:215], v[144:147]\n"
    "s_waitcnt lgkmcnt(8)\n"
    "ds_read_b64_tr_b16 v[208:209], v224 offset:36864\n"
    "v_mfma_f32_16x16x32_bf16 v[148:151], v[96:99], v[212:215], v[148:151]\n"
    "ds_read_b64_tr_b16 v[210:211], v224 offset:37376\n"
    "v_mfma_f32_16x16x32_bf16 v[152:155], v[32:35], v[216:219], v[152:155]\n"
    "s_waitcnt lgkmcnt(8)\n"
    "ds_read_b64_tr_b16 v[212:213], v225 offset:36864\n"
    "v_mfma_f32_16x16x32_bf16 v[156:159], v[96:99], v[216:219], v[156:159]\n"
    "ds_read_b64_tr_b16 v[214:215], v225 offset:37376\n"
    "v_mfma_f32_16x16x32_bf16 v[160:163], v[32:35], v[192:195], v[160:163]\n"
    "s_waitcnt lgkmcnt(8)\n"
    "ds_read_b64_tr_b16 v[216:217], v226 offset:36864\n"
    "v_mfma_f32_16x16x32_bf16 v[164:167], v[96:99], v[192:195], v[164:167]\n"
    "ds_read_b64_tr_b16 v[218:219], v226 offset:37376\n"
    "v_mfma_f32_16x16x32_bf16 v[168:171], v[32:35], v[196:199], v[168:171]\n"
    "s_waitcnt lgkmcnt(8)\n"
    "ds_read_b64_tr_b16 v[192:193], v227 offset:36864\n"
    "v_mfma_f32_16x16x32_bf16 v[172:175], v[96:99], v[196:199], v[172:175]\n"
    "ds_read_b64_tr_b16 v[194:195], v227 offset:37376\n"
    "v_mfma_f32_16x16x32_bf16 v[176:179], v[32:35], v[200:203], v[176:179]\n"
    "s_waitcnt lgkmcnt(8)\n"
    "ds_read_b64_tr_b16 v[196:197], v224 offset:45056\n"
    "v_mfma_f32_16x16x32_bf16 v[180:183], v[96:99], v[200:203], v[180:183]\n"
    "ds_read_b64_tr_b16 v[198:199], v224 offset:45568\n"
    "v_mfma_f32_16x16x32_bf16 v[184:187], v[32:35], v[204:207], v[184:187]\n"
    "s_waitcnt lgkmcnt(8)\n"
    "ds_read_b64_tr_b16 v[200:201], v225 offset:45056\n"
    "v_mfma_f32_16x16x32_bf16 v[188:191], v[96:99], v[204:207], v[188:191]\n"
    "ds_read_b64_tr_b16 v[202:203], v225 offset:45568\n"
    "v_mfma_f32_16x16x32_bf16 v[128:131], v[36:39], v[208:211], v[128:131]\n"
    "s_waitcnt lgkmcnt(8)\n"
    "ds_read_b64_tr_b16 v[204:205], v226 offset:45056\n"
    "v_mfma_f32_16x16x32_bf16 v[132:135], v[100:103], v[208:211], v[132:135]\n"
    "ds_read_b64_tr_b16 v[206:207], v226 offset:45568\n"
    "v_mfma_f32_16x16x32_bf16 v[136:139], v[36:39], v[212:215], v[136:139]\n"
    "s_waitcnt lgkmcnt(8)\n"
    "ds_read_b64_tr_b16 v[208:209], v227 offset:45056\n"
    "v_mfma_f32_16x16x32_bf16 v[140:143], v[100:103], v[212:215], v[140:143]\n"
    "ds_read_b64_tr_b16 v[210:211], v227 offset:45568\n"
    "v_mfma_f32_16x16x32_bf16 v[144:147], v[36:39], v[216:219], v[144:147]\n"
    "s_waitcnt vmcnt(13)\n"
    "s_barrier\n"
    "s_waitcnt lgkmcnt(8)\n"
    "ds_read_b64_tr_b16 v[212:213], v224 offset:49152\n"
    "v_mfma_f32_16x16x32_bf16 v[148:151], v[100:103], v[216:219], v[148:151]\n"
    "ds_read_b64_tr_b16 v[214:215], v224 offset:49664\n"
    "v_mfma_f32_16x16x32_bf16 v[152:155], v[36:39], v[192:195], v[152:155]\n"
    "s_waitcnt lgkmcnt(8)\n"
    "ds_read_b64_tr_b16 v[216:217], v225 offset:49152\n"
    "v_mfma_f32_16x16x32_bf16 v[156:159], v[100:103], v[192:195], v[156:159]\n"
    "ds_read_b64_tr_b16 v[218:219], v225 offset:49664\n"
    "v_mfma_f32_16x16x32_bf16 v[160:163], v[36:39], v[196:199], v[160:163]\n"
    "s_waitcnt lgkmcnt(8)\n"
    "ds_read_b64_tr_b16 v[192:193], v226 offset:49152\n"
    "v_mfma_f32_16x16x32_bf16 v[164:167], v[100:103], v[196:199], v[164:167]\n"
    "ds_read_b64_tr_b16 v[194:195], v226 offset:49664\n"
    "s_add_u32 m0, s92, 0\n"
    "s_add_u32 s65, 0, 0\n"
    "buffer_load_dwordx4 v254, s[88:91], s65 offen lds\n"
    "v_mfma_f32_16x16x32_bf16 v[168:171], v[36:39], v[200:203], v[168:171]\n"
    "s_waitcnt lgkmcnt(8)\n"
    "ds_read_b64_tr_b16 v[196:197], v227 offset:49152\n"
    "v_mfma_f32_16x16x32_bf16 v[172:175], v[100:103], v[200:203], v[172:175]\n"
    "ds_read_b64_tr_b16 v[198:199], v227 offset:49664\n"
    "v_mfma_f32_16x16x32_bf16 v[176:179], v[36:39], v[204:207], v[176:179]\n"
    "s_waitcnt lgkmcnt(8)\n"
    "ds_read_b64_tr_b16 v[200:201], v224 offset:57344\n"
    "v_mfma_f32_16x16x32_bf16 v[180:183], v[100:103], v[204:207], v[180:183]\n"
    "ds_read_b64_tr_b16 v[202:203], v224 offset:57856\n"
    "v_mfma_f32_16x16x32_bf16 v[184:187], v[36:39], v[208:211], v[184:187]\n"
    "s_waitcnt lgkmcnt(8)\n"
    "ds_read_b64_tr_b16 v[204:205], v225 offset:57344\n"
    "v_mfma_f32_16x16x32_bf16 v[188:191], v[100:103], v[208:211], v[188:191]\n"
    "ds_read_b64_tr_b16 v[206:207], v225 offset:57856\n"
    "v_mfma_f32_16x16x32_bf16 v[128:131], v[40:43], v[212:215], v[128:131]\n"
    "s_waitcnt lgkmcnt(8)\n"
    "ds_read_b64_tr_b16 v[208:209], v226 offset:57344\n"
    "v_mfma_f32_16x16x32_bf16 v[132:135], v[104:107], v[212:215], v[132:135]\n"
    "ds_read_b64_tr_b16 v[210:211], v226 offset:57856\n"
    "v_mfma_f32_16x16x32_bf16 v[136:139], v[40:43], v[216:219], v[136:139]\n"
    "s_waitcnt lgkmcnt(8)\n"
    "ds_read_b64_tr_b16 v[212:213], v227 offset:57344\n"
    "v_mfma_f32_16x16x32_bf16 v[140:143], v[104:107], v[216:219], v[140:143]\n"
    "ds_read_b64_tr_b16 v[214:215], v227 offset:57856\n"
    "v_mfma_f32_16x16x32_bf16 v[144:147], v[40:43], v[192:195], v[144:147]\n"
    "s_waitcnt lgkmcnt(8)\n"
    "ds_read_b64_tr_b16 v[216:217], v224 offset:53248\n"
    "v_mfma_f32_16x16x32_bf16 v[148:151], v[104:107], v[192:195], v[148:151]\n"
    "ds_read_b64_tr_b16 v[218:219], v224 offset:53760\n"
    "v_mfma_f32_16x16x32_bf16 v[152:155], v[40:43], v[196:199], v[152:155]\n"
    "s_waitcnt lgkmcnt(8)\n"
    "ds_read_b64_tr_b16 v[192:193], v225 offset:53248\n"
    "v_mfma_f32_16x16x32_bf16 v[156:159], v[104:107], v[196:199], v[156:159]\n"
    "ds_read_b64_tr_b16 v[194:195], v225 offset:53760\n"
    "v_mfma_f32_16x16x32_bf16 v[160:163], v[40:43], v[200:203], v[160:163]\n"
    "s_waitcnt lgkmcnt(8)\n"
    "ds_read_b64_tr_b16 v[196:197], v226 offset:53248\n"
    "v_mfma_f32_16x16x32_bf16 v[164:167], v[104:107], v[200:203], v[164:167]\n"
    "ds_read_b64_tr_b16 v[198:199], v226 offset:53760\n"
    "v_mfma_f32_16x16x32_bf16 v[168:171], v[40:43], v[204:207], v[168:171]\n"
    "s_waitcnt lgkmcnt(8)\n"
    "ds_read_b64_tr_b16 v[200:201], v227 offset:53248\n"
    "v_mfma_f32_16x16x32_bf16 v[172:175], v[104:107], v[204:207], v[172:175]\n"
    "ds_read_b64_tr_b16 v[202:203], v227 offset:53760\n"
    "v_mfma_f32_16x16x32_bf16 v[176:179], v[40:43], v[208:211], v[176:179]\n"
    "s_waitcnt lgkmcnt(8)\n"
    "ds_read_b64_tr_b16 v[204:205], v224 offset:61440\n"
    "v_mfma_f32_16x16x32_bf16 v[180:183], v[104:107], v[208:211], v[180:183]\n"
    "ds_read_b64_tr_b16 v[206:207], v224 offset:61952\n"
    "v_mfma_f32_16x16x32_bf16 v[184:187], v[40:43], v[212:215], v[184:187]\n"
    "s_waitcnt lgkmcnt(8)\n"
    "ds_read_b64_tr_b16 v[208:209], v225 offset:61440\n"
    "v_mfma_f32_16x16x32_bf16 v[188:191], v[104:107], v[212:215], v[188:191]\n"
    "ds_read_b64_tr_b16 v[210:211], v225 offset:61952\n"
    "v_lshrrev_b32 v0, 5, v235\n"
    "v_bfe_u32 v1, v235, 4, 1\n"
    "v_mul_u32_u24 v0, s70, v0\n"
    "v_mul_u32_u24 v1, s71, v1\n"
    "v_add_u32 v0, v0, v1\n"
    "global_load_dword v0, v0, s[68:69]\n"
    "v_mfma_f32_16x16x32_bf16 v[128:131], v[44:47], v[216:219], v[128:131]\n"
    "s_waitcnt lgkmcnt(8)\n"
    "ds_read_b64_tr_b16 v[212:213], v226 offset:61440\n"
    "v_mfma_f32_16x16x32_bf16 v[132:135], v[108:111], v[216:219], v[132:135]\n"
    "ds_read_b64_tr_b16 v[214:215], v226 offset:61952\n"
    "v_mfma_f32_16x16x32_bf16 v[136:139], v[44:47], v[192:195], v[136:139]\n"
    "s_waitcnt lgkmcnt(8)\n"
    "ds_read_b64_tr_b16 v[216:217], v227 offset:61440\n"
    "v_mfma_f32_16x16x32_bf16 v[140:143], v[108:111], v[192:195], v[140:143]\n"
    "ds_read_b64_tr_b16 v[218:219], v227 offset:61952\n"
    "v_mfma_f32_16x16x32_bf16 v[144:147], v[44:47], v[196:199], v[144:147]\n"
    "s_waitcnt vmcnt(11)\n"
    "s_barrier\n"
    "s_waitcnt lgkmcnt(8)\n"
    "v_add_u32 v224, 65536, v224\n"
    "v_add_u32 v225, 65536, v225\n"
    "v_add_u32 v226, 65536, v226\n"
    "v_add_u32 v227, 65536, v227\n"
    "ds_read_b64_tr_b16 v[192:193], v224 offset:0\n"
    "v_mfma_f32_16x16x32_bf16 v[148:151], v[108:111], v[196:199], v[148:151]\n"
    "ds_read_b64_tr_b16 v[194:195], v224 offset:512\n"
    "v_mfma_f32_16x16x32_bf16 v[152:155], v[44:47], v[200:203], v[152:155]\n"
    "s_waitcnt lgkmcnt(8)\n"
    "ds_read_b64_tr_b16 v[196:197], v224 offset:4096\n"
    "v_mfma_f32_16x16x32_bf16 v[156:159], v[108:111], v[200:203], v[156:159]\n"
    "ds_read_b64_tr_b16 v[198:199], v224 offset:4608\n"
    "v_mfma_f32_16x16x32_bf16 v[160:163], v[44:47], v[204:207], v[160:163]\n"
    "s_waitcnt vmcnt(8)\n"
    "s_barrier\n"
    "s_waitcnt lgkmcnt(8)\n"
    "ds_read_b64_tr_b16 v[200:201], v224 offset:16384\n"
    "v_mfma_f32_16x16x32_bf16 v[164:167], v[108:111], v[204:207], v[164:167]\n"
    "ds_read_b64_tr_b16 v[202:203], v224 offset:16896\n"
    "v_mfma_f32_16x16x32_bf16 v[168:171], v[44:47], v[208:211], v[168:171]\n"
    "s_waitcnt lgkmcnt(8)\n"
    "ds_read_b64_tr_b16 v[204:205], v224 offset:20480\n"
    "v_mfma_f32_16x16x32_bf16 v[172:175], v[108:111], v[208:211], v[172:175]\n"
    "ds_read_b64_tr_b16 v[206:207], v224 offset:20992\n"
    "v_mfma_f32_16x16x32_bf16 v[176:179], v[44:47], v[212:215], v[176:179]\n"
    "s_waitcnt lgkmcnt(8)\n"
    "ds_read_b64_tr_b16 v[208:209], v225 offset:0\n"
    "v_mfma_f32_16x16x32_bf16 v[180:183], v[108:111], v[212:215], v[180:183]\n"
    "ds_read_b64_tr_b16 v[210:211], v225 offset:512\n"
    "v_mfma_f32_16x16x32_bf16 v[184:187], v[44:47], v[216:219], v[184:187]\n"
    "s_waitcnt lgkmcnt(8)\n"
    "ds_read_b64_tr_b16 v[212:213], v225 offset:4096\n"
    "v_mfma_f32_16x16x32_bf16 v[188:191], v[108:111], v[216:219], v[188:191]\n"
    "ds_read_b64_tr_b16 v[214:215], v225 offset:4608\n"
    "v_mfma_f32_16x16x32_bf16 v[128:131], v[48:51], v[192:195], v[128:131]\n"
    "s_waitcnt lgkmcnt(8)\n"
    "ds_read_b64_tr_b16 v[216:217], v225 offset:16384\n"
    "v_mfma_f32_16x16x32_bf16 v[132:135], v[112:115], v[192:195], v[132:135]\n"
    "ds_read_b64_tr_b16 v[218:219], v225 offset:16896\n"
    "v_mfma_f32_16x16x32_bf16 v[128:131], v[52:55], v[196:199], v[128:131]\n"
    "s_waitcnt lgkmcnt(8)\n"
    "ds_read_b64_tr_b16 v[192:193], v225 offset:20480\n"
    "v_mfma_f32_16x16x32_bf16 v[132:135], v[116:119], v[196:199], v[132:135]\n"
    "ds_read_b64_tr_b16 v[194:195], v225 offset:20992\n"
    "v_mfma_f32_16x16x32_bf16 v[128:131], v[56:59], v[200:203], v[128:131]\n"
    "s_waitcnt lgkmcnt(8)\n"
    "ds_read_b64_tr_b16 v[196:197], v226 offset:0\n"
    "v_mfma_f32_16x16x32_bf16 v[132:135], v[120:123], v[200:203], v[132:135]\n"
    "ds_read_b64_tr_b16 v[198:199], v226 offset:512\n"
    "v_mfma_f32_16x16x32_bf16 v[128:131], v[60:63], v[204:207], v[128:131]\n"
    "s_waitcnt lgkmcnt(8)\n"
    "ds_read_b64_tr_b16 v[200:201], v226 offset:4096\n"
    "v_mfma_f32_16x16x32_bf16 v[132:135], v[124:127], v[204:207], v[132:135]\n"
    "ds_read_b64_tr_b16 v[202:203], v226 offset:4608\n"
    "v_mfma_f32_16x16x32_bf16 v[136:139], v[48:51], v[208:211], v[136:139]\n"
    "s_waitcnt lgkmcnt(8)\n"
    "ds_read_b64_tr_b16 v[204:205], v226 offset:16384\n"
    "v_mfma_f32_16x16x32_bf16 v[140:143], v[112:115], v[208:211], v[140:143]\n"
    "ds_read_b64_tr_b16 v[206:207], v226 offset:16896\n"
    "v_mfma_f32_16x16x32_bf16 v[136:139], v[52:55], v[212:215], v[136:139]\n"
    "s_waitcnt lgkmcnt(8)\n"
    "ds_read_b64_tr_b16 v[208:209], v226 offset:20480\n"
    "v_mfma_f32_16x16x32_bf16 v[140:143], v[116:119], v[212:215], v[140:143]\n"
    "ds_read_b64_tr_b16 v[210:211], v226 offset:20992\n"
    "v_cvt_pk_bf16_f32 v236, v128, v129\n"
    "v_cvt_pk_bf16_f32 v237, v130, v131\n"
    "v_cvt_pk_bf16_f32 v238, v132, v133\n"
    "v_cvt_pk_bf16_f32 v239, v134, v135\n"
    "buffer_store_dwordx4 v[236:239], v235, s[56:59], 0 offen offset:0 sc1\n"
    "v_mfma_f32_16x16x32_bf16 v[136:139], v[56:59], v[216:219], v[136:139]\n"
    "s_waitcnt lgkmcnt(8)\n"
    "ds_read_b64_tr_b16 v[212:213], v227 offset:0\n"
    "v_mfma_f32_16x16x32_bf16 v[140:143], v[120:123], v[216:219], v[140:143]\n"
    "ds_read_b64_tr_b16 v[214:215], v227 offset:512\n"
    "v_mfma_f32_16x16x32_bf16 v[136:139], v[60:63], v[192:195], v[136:139]\n"
    "s_waitcnt lgkmcnt(8)\n"
    "ds_read_b64_tr_b16 v[216:217], v227 offset:4096\n"
    "v_mfma_f32_16x16x32_bf16 v[140:143], v[124:127], v[192:195], v[140:143]\n"
    "ds_read_b64_tr_b16 v[218:219], v227 offset:4608\n"
    "v_mfma_f32_16x16x32_bf16 v[144:147], v[48:51], v[196:199], v[144:147]\n"
    "s_waitcnt lgkmcnt(8)\n"
    "ds_read_b64_tr_b16 v[192:193], v227 offset:16384\n"
    "v_mfma_f32_16x16x32_bf16 v[148:151], v[112:115], v[196:199], v[148:151]\n"
    "ds_read_b64_tr_b16 v[194:195], v227 offset:16896\n"
    "v_mfma_f32_16x16x32_bf16 v[144:147], v[52:55], v[200:203], v[144:147]\n"
    "s_waitcnt lgkmcnt(8)\n"
    "ds_read_b64_tr_b16 v[196:197], v227 offset:20480\n"
    "v_mfma_f32_16x16x32_bf16 v[148:151], v[116:119], v[200:203], v[148:151]\n"
    "ds_read_b64_tr_b16 v[198:199], v227 offset:20992\n"
    "v_cvt_pk_bf16_f32 v240, v136, v137\n"
    "v_cvt_pk_bf16_f32 v241, v138, v139\n"
    "v_cvt_pk_bf16_f32 v242, v140, v141\n"
    "v_cvt_pk_bf16_f32 v243, v142, v143\n"
    "buffer_store_dwordx4 v[240:243], v235, s[56:59], 0 offen offset:1024 sc1\n"
    "v_mfma_f32_16x16x32_bf16 v[144:147], v[56:59], v[204:207], v[144:147]\n"
    "s_waitcnt lgkmcnt(8)\n"
    "ds_read_b64_tr_b16 v[200:201], v224 offset:8192\n"
    "v_mfma_f32_16x16x32_bf16 v[148:151], v[120:123], v[204:207], v[148:151]\n"
    "ds_read_b64_tr_b16 v[202:203], v224 offset:8704\n"
    "v_mfma_f32_16x16x32_bf16 v[144:147], v[60:63], v[208:211], v[144:147]\n"
    "s_waitcnt lgkmcnt(8)\n"
    "ds_read_b64_tr_b16 v[204:205], v224 offset:12288\n"
    "v_mfma_f32_16x16x32_bf16 v[148:151], v[124:127], v[208:211], v[148:151]\n"
    "ds_read_b64_tr_b16 v[206:207], v224 offset:12800\n"
    "v_mfma_f32_16x16x32_bf16 v[152:155], v[48:51], v[212:215], v[152:155]\n"
    "s_waitcnt lgkmcnt(8)\n"
    "ds_read_b64_tr_b16 v[208:209], v224 offset:24576\n"
    "v_mfma_f32_16x16x32_bf16 v[156:159], v[112:115], v[212:215], v[156:159]\n"
    "ds_read_b64_tr_b16 v[210:211], v224 offset:25088\n"
    "v_mfma_f32_16x16x32_bf16 v[152:155], v[52:55], v[216:219], v[152:155]\n"
    "s_waitcnt lgkmcnt(8)\n"
    "ds_read_b64_tr_b16 v[212:213], v224 offset:28672\n"
    "v_mfma_f32_16x16x32_bf16 v[156:159], v[116:119], v[216:219], v[156:159]\n"
    "ds_read_b64_tr_b16 v[214:215], v224 offset:29184\n"
    "v_cvt_pk_bf16_f32 v244, v144, v145\n"
    "v_cvt_pk_bf16_f32 v245, v146, v147\n"
    "v_cvt_pk_bf16_f32 v246, v148, v149\n"
    "v_cvt_pk_bf16_f32 v247, v150, v151\n"
    "buffer_store_dwordx4 v[244:247], v235, s[56:59], 0 offen offset:2048 sc1\n"
    "v_mfma_f32_16x16x32_bf16 v[152:155], v[56:59], v[192:195], v[152:155]\n"
    "s_waitcnt lgkmcnt(8)\n"
    "ds_read_b64_tr_b16 v[216:217], v225 offset:8192\n"
    "v_mfma_f32_16x16x32_bf16 v[156:159], v[120:123], v[192:195], v[156:159]\n"
    "ds_read_b64_tr_b16 v[218:219], v225 offset:8704\n"
    "v_mfma_f32_16x16x32_bf16 v[152:155], v[60:63], v[196:199], v[152:155]\n"
    "s_waitcnt lgkmcnt(8)\n"
    "ds_read_b64_tr_b16 v[192:193], v225 offset:12288\n"
    "v_mfma_f32_16x16x32_bf16 v[156:159], v[124:127], v[196:199], v[156:159]\n"
    "ds_read_b64_tr_b16 v[194:195], v225 offset:12800\n"
    "v_mfma_f32_16x16x32_bf16 v[160:163], v[48:51], v[200:203], v[160:163]\n"
    "s_waitcnt lgkmcnt(8)\n"
    "ds_read_b64_tr_b16 v[196:197], v225 offset:24576\n"
    "v_mfma_f32_16x16x32_bf16 v[164:167], v[112:115], v[200:203], v[164:167]\n"
    "ds_read_b64_tr_b16 v[198:199], v225 offset:25088\n"
    "v_mfma_f32_16x16x32_bf16 v[160:163], v[52:55], v[204:207], v[160:163]\n"
    "s_waitcnt lgkmcnt(8)\n"
    "ds_read_b64_tr_b16 v[200:201], v225 offset:28672\n"
    "v_mfma_f32_16x16x32_bf16 v[164:167], v[116:119], v[204:207], v[164:167]\n"
    "ds_read_b64_tr_b16 v[202:203], v225 offset:29184\n"
    "v_cvt_pk_bf16_f32 v248, v152, v153\n"
    "v_cvt_pk_bf16_f32 v249, v154, v155\n"
    "v_cvt_pk_bf16_f32 v250, v156, v157\n"
    "v_cvt_pk_bf16_f32 v251, v158, v159\n"
    "buffer_store_dwordx4 v[248:251], v235, s[56:59], 0 offen offset:3072 sc1\n"
    "v_mfma_f32_16x16x32_bf16 v[160:163], v[56:59], v[208:211], v[160:163]\n"
    "s_waitcnt lgkmcnt(8)\n"
    "ds_read_b64_tr_b16 v[204:205], v226 offset:8192\n"
    "v_mfma_f32_16x16x32_bf16 v[164:167], v[120:123], v[208:211], v[164:167]\n"
    "ds_read_b64_tr_b16 v[206:207], v226 offset:8704\n"
    "v_mfma_f32_16x16x32_bf16 v[160:163], v[60:63], v[212:215], v[160:163]\n"
    "s_waitcnt lgkmcnt(8)\n"
    "ds_read_b64_tr_b16 v[208:209], v226 offset:12288\n"
    "v_mfma_f32_16x16x32_bf16 v[164:167], v[124:127], v[212:215], v[164:167]\n"
    "ds_read_b64_tr_b16 v[210:211], v226 offset:12800\n"
    "v_mfma_f32_16x16x32_bf16 v[168:171], v[48:51], v[216:219], v[168:171]\n"
    "s_waitcnt lgkmcnt(8)\n"
    "ds_read_b64_tr_b16 v[212:213], v226 offset:24576\n"
    "v_mfma_f32_16x16x32_bf16 v[172:175], v[112:115], v[216:219], v[172:175]\n"
    "ds_read_b64_tr_b16 v[214:215], v226 offset:25088\n"
    "v_mfma_f32_16x16x32_bf16 v[168:171], v[52:55], v[192:195], v[168:171]\n"
    "s_waitcnt lgkmcnt(8)\n"
    "ds_read_b64_tr_b16 v[216:217], v226 offset:28672\n"
    "v_mfma_f32_16x16x32_bf16 v[172:175], v[116:119], v[192:195], v[172:175]\n"
    "ds_read_b64_tr_b16 v[218:219], v226 offset:29184\n"
    "v_cvt_pk_bf16_f32 v236, v160, v161\n"
    "v_cvt_pk_bf16_f32 v237, v162, v163\n"
    "v_cvt_pk_bf16_f32 v238, v164, v165\n"
    "v_cvt_pk_bf16_f32 v239, v166, v167\n"
    "buffer_store_dwordx4 v[236:239], v235, s[56:59], s98 offen offset:0 sc1\n"
    "v_mfma_f32_16x16x32_bf16 v[168:171], v[56:59], v[196:199], v[168:171]\n"
    "s_waitcnt lgkmcnt(8)\n"
    "ds_read_b64_tr_b16 v[192:193], v227 offset:8192\n"
    "v_mfma_f32_16x16x32_bf16 v[172:175], v[120:123], v[196:199], v[172:175]\n"
    "ds_read_b64_tr_b16 v[194:195], v227 offset:8704\n"
    "v_mfma_f32_16x16x32_bf16 v[168:171], v[60:63], v[200:203], v[168:171]\n"
    "s_waitcnt lgkmcnt(8)\n"
    "ds_read_b64_tr_b16 v[196:197], v227 offset:12288\n"
    "v_mfma_f32_16x16x32_bf16 v[172:175], v[124:127], v[200:203], v[172:175]\n"
    "ds_read_b64_tr_b16 v[198:199], v227 offset:12800\n"
    "v_mfma_f32_16x16x32_bf16 v[176:179], v[48:51], v[204:207], v[176:179]\n"
    "s_waitcnt lgkmcnt(8)\n"
    "ds_read_b64_tr_b16 v[200:201], v227 offset:24576\n"
    "v_mfma_f32_16x16x32_bf16 v[180:183], v[112:115], v[204:207], v[180:183]\n"
    "ds_read_b64_tr_b16 v[202:203], v227 offset:25088\n"
    "v_mfma_f32_16x16x32_bf16 v[176:179], v[52:55], v[208:211], v[176:179]\n"
    "s_waitcnt lgkmcnt(8)\n"
    "ds_read_b64_tr_b16 v[204:205], v227 offset:28672\n"
    "v_mfma_f32_16x16x32_bf16 v[180:183], v[116:119], v[208:211], v[180:183]\n"
    "ds_read_b64_tr_b16 v[206:207], v227 offset:29184\n"
    "v_cvt_pk_bf16_f32 v240, v168, v169\n"
    "v_cvt_pk_bf16_f32 v241, v170, v171\n"
    "v_cvt_pk_bf16_f32 v242, v172, v173\n"
    "v_cvt_pk_bf16_f32 v243, v174, v175\n"
    "buffer_store_dwordx4 v[240:243], v235, s[56:59], s98 offen offset:1024 sc1\n"
    "v_mfma_f32_16x16x32_bf16 v[176:179], v[56:59], v[212:215], v[176:179]\n"
    "s_waitcnt lgkmcnt(8)\n"
    "v_mfma_f32_16x16x32_bf16 v[180:183], v[120:123], v[212:215], v[180:183]\n"
    "v_mfma_f32_16x16x32_bf16 v[176:179], v[60:63], v[216:219], v[176:179]\n"
    "s_waitcnt lgkmcnt(6)\n"
    "v_mfma_f32_16x16x32_bf16 v[180:183], v[124:127], v[216:219], v[180:183]\n"
    "v_mfma_f32_16x16x32_bf16 v[184:187], v[48:51], v[192:195], v[184:187]\n"
    "s_waitcnt lgkmcnt(4)\n"
    "v_mfma_f32_16x16x32_bf16 v[188:191], v[112:115], v[192:195], v[188:191]\n"
    "v_mfma_f32_16x16x32_bf16 v[184:187], v[52:55], v[196:199], v[184:187]\n"
    "s_waitcnt lgkmcnt(2)\n"
    "v_mfma_f32_16x16x32_bf16 v[188:191], v[116:119], v[196:199], v[188:191]\n"
    "v_cvt_pk_bf16_f32 v244, v176, v177\n"
    "v_cvt_pk_bf16_f32 v245, v178, v179\n"
    "v_cvt_pk_bf16_f32 v246, v180, v181\n"
    "v_cvt_pk_bf16_f32 v247, v182, v183\n"
    "buffer_store_dwordx4 v[244:247], v235, s[56:59], s98 offen offset:2048 sc1\n"
    "v_mfma_f32_16x16x32_bf16 v[184:187], v[56:59], v[200:203], v[184:187]\n"
    "s_waitcnt lgkmcnt(0)\n"
    "v_mfma_f32_16x16x32_bf16 v[188:191], v[120:123], v[200:203], v[188:191]\n"
    "v_mfma_f32_16x16x32_bf16 v[184:187], v[60:63], v[204:207], v[184:187]\n"
    "v_mfma_f32_16x16x32_bf16 v[188:191], v[124:127], v[204:207], v[188:191]\n"
    "s_nop 6\n"
    "v_cvt_pk_bf16_f32 v248, v184, v185\n"
    "v_cvt_pk_bf16_f32 v249, v186, v187\n"
    "v_cvt_pk_bf16_f32 v250, v188, v189\n"
    "v_cvt_pk_bf16_f32 v251, v190, v191\n"
    "buffer_store_dwordx4 v[248:251], v235, s[56:59], s98 offen offset:3072 sc1\n"
    "s_waitcnt vmcnt(8)\n"
    "s_barrier\n"
      : "+{v[224:227]}"(rw)
      : "{v[228:231]}"(P.ra), "{v232}"(P.va0), "{v233}"(P.va1), "{v234}"(P.vw), "{v235}"(lane * 16),
        "{v252}"(v8), "{v253}"((lane >> 5) * 1024 + (lane & 31) * 16), "{v254}"(xv),
        "{s[40:43]}"(sC), "{s[44:47]}"(sK), "{s[48:51]}"(sV), "{s[52:55]}"(dK), "{s[56:59]}"(dV),
        "{s60}"(s60), "{s61}"(s61), "{s62}"(s62), "{s63}"(s63),
        "{s[72:75]}"(pk), "{s[76:79]}"(pv), "{s[80:83]}"(pq2), "{s[84:87]}"(pw), "{s[88:91]}"(srd4(xb, xn)),
        "{s92}"(rfl(ldsb + xl)), "{s93}"(rfl((t0 + 8 * wid) * 1024)), "{s94}"(rfl(((bid >> 1) * 32 + 4 * wid) * 1024 + (bid & 1) * 512)),
        "{s95}"(rfl((bid * 8 + wid) * 1024)), "{s96}"(rfl(ldsb + wid * 2048)), "{s97}"(rfl(d0 * 2)),
        "{s[68:69]}"(u32x2{rfl((unsigned)(uint64_t)tb_), rfl((unsigned)((uint64_t)tb_ >> 32))}), "{s70}"(wid < 2 ? 8192u : 208u), "{s71}"(wid < 2 ? 128u : 104u)
      : "v0", "v1", "v2", "v3", "v4", "v5", "v6", "v7", "v8", "v9", "v10", "v11", "v12", "v13", "v14", "v15", "v16", "v17", "v18", "v19", "v20", "v21", "v22", "v23", "v24", "v25", "v26", "v27", "v28", "v29", "v30", "v31", "v32", "v33", "v34", "v35", "v36", "v37", "v38", "v39", "v40", "v41", "v42", "v43", "v44", "v45", "v46", "v47", "v48", "v49", "v50", "v51", "v52", "v53", "v54", "v55",
        "v56", "v57", "v58", "v59",
        "v60", "v61", "v62", "v63", "v64", "v65", "v66", "v67", "v68", "v69", "v70", "v71", "v72", "v73", "v74", "v75", "v76", "v77", "v78", "v79", "v80", "v81", "v82", "v83", "v84", "v85", "v86", "v87", "v88", "v89", "v90", "v91", "v92", "v93", "v94", "v95", "v96", "v97", "v98", "v99", "v100", "v101", "v102", "v103", "v104", "v105", "v106", "v107", "v108", "v109", "v110", "v111", "v112", "v113", "v114", "v115", "v116", "v117", "v118", "v119", "v120", "v121", "v122", "v123", "v124", "v125", "v126", "v127", "v128", "v129", "v130", "v131", "v132", "v133", "v134", "v135", "v136", "v137", "v138", "v139", "v140", "v141", "v142", "v143", "v144", "v145", "v146", "v147", "v148", "v149", "v150", "v151", "v152", "v153", "v154", "v155", "v156", "v157", "v158", "v159", "v160", "v161", "v162", "v163", "v164", "v165", "v166", "v167", "v168", "v169", "v170", "v171", "v172", "v173", "v174", "v175", "v176", "v177", "v178", "v179", "v180", "v181", "v182", "v183", "v184", "v185", "v186", "v187", "v188", "v189", "v190", "v191", "v192", "v193", "v194", "v195", "v196", "v197", "v198", "v199", "v200", "v201", "v202", "v203", "v204", "v205", "v206", "v207", "v208", "v209", "v210", "v211", "v212", "v213", "v214", "v215", "v216", "v217", "v218", "v219", "v220", "v221", "v222", "v223", "v236", "v237", "v238", "v239", "v240", "v241", "v242", "v243", "v244", "v245", "v246", "v247", "v248", "v249", "v250", "v251", "v255", "s64", "s65", "s66", "s67", "s98", "vcc", "scc", "memory");
    SBAR();
  }
  prep(lds_, ws, wid);
}

constexpr unsigned CV_HR = 4608, CV_CS = 4160, CV_CH = CV_HR + 4 * CV_CS;
struct WoRing { u32x4 srd; unsigned off; f32x16 w[15]; };
#define WO_STEP(R, k, lane16) asm volatile("s_add_u32 s50, %2, %4\n\tbuffer_load_dwordx4 a[%c5:%c6], %1, %3, s50 offen\n\tbuffer_load_dwordx4 a[%c7:%c8], %1, %3, s50 offen offset:1024\n\t" \
    "buffer_load_dwordx4 a[%c9:%c10], %1, %3, s50 offen offset:2048\n\tbuffer_load_dwordx4 a[%c11:%c12], %1, %3, s50 offen offset:3072" \
    : WO_OUT_##k((R).w[k]) : "v"(lane16), "s"((R).off), "s"((R).srd), "i"((k) * 32768), "i"(16 * (k)), "i"(16 * (k) + 3), "i"(16 * (k) + 4), "i"(16 * (k) + 7), \
      "i"(16 * (k) + 8), "i"(16 * (k) + 11), "i"(16 * (k) + 12), "i"(16 * (k) + 15) : "s50", "scc", "memory")
#define WO_OUT_9 "={a[144:159]}"
#define WO_OUT_10 "={a[160:175]}"
#define WO_OUT_11 "={a[176:191]}"
#define WO_OUT_12 "={a[192:207]}"
#define WO_OUT_13 "={a[208:223]}"
#define WO_OUT_14 "={a[224:239]}"
template <class H> DI void conv2(char* lds, const char* stage, unsigned dpair, char* __restrict__ ws, unsigned wid, WoRing& WR, H&& mid) {
  const unsigned lane = lane_id(), i = wid * 64 + lane;
  const unsigned ch0 = blockIdx.x * 2;
  u32x4 av[2], qv[2];
#pragma unroll
  for (int c2 = 0; c2 < 2; ++c2) {
    const char* sc = stage + c2 * 12288 + i * 16;
    const u32x4 hv = *(const u32x4*)sc, a = *(const u32x4*)(sc + 4096);
    u32x4 b = *(const u32x4*)(sc + 4096 + 16);
    av[c2] = a; qv[c2] = *(const u32x4*)(sc + 8192);
    if (i == 255) b = u32x4{0u, 0u, 0u, 0u};
    char* base = lds + c2 * CV_CH;
    u32x4 rv;
#pragma unroll
    for (int e = 0; e < 4; ++e) rv[e] = (hv[3 - e] >> 16) | (hv[3 - e] << 16);
    *(u32x4*)(base + (255 - i) * 16) = rv;
    if (i < 32) *(u32x4*)(base + 4096 + i * 16) = u32x4{0u, 0u, 0u, 0u};
    const unsigned w[8] = {a[0], a[1], a[2], a[3], b[0], b[1], b[2], b[3]};
    char* cp = base + CV_HR;
#pragma unroll
    for (int c = 0; c < 4; ++c) {
      u32x4 o;
#pragma unroll
      for (int e = 0; e < 4; ++e) o[e] = (c & 1) ? __builtin_amdgcn_alignbit(w[(c >> 1) + e + 1], w[(c >> 1) + e], 16) : w[(c >> 1) + e];
      *(u32x4*)(cp + c * CV_CS + (i + 2) * 16) = o;
    }
    if (i == 0) {
      const unsigned z[9] = {0u, 0u, 0u, 0u, a[0], a[1], a[2], a[3], 0u};
#pragma unroll
      for (int c = 0; c < 4; ++c) {
        u32x4 o;
#pragma unroll
        for (int e = 0; e < 4; ++e) o[e] = (c & 1) ? __builtin_amdgcn_alignbit(z[(c >> 1) + e + 1], z[(c >> 1) + e], 16) : z[(c >> 1) + e];
        *(u32x4*)(cp + c * CV_CS) = u32x4{0u, 0u, 0u, 0u};
        *(u32x4*)(cp + c * CV_CS + 16) = o;
      }
    }
  }
  const f32x4 g0 = *(const f32x4*)(stage + 24576 + i * 32), g1 = *(const f32x4*)(stage + 24576 + i * 32 + 16);
  LBAR();
  const unsigned cl = wid >> 1, rr = wid & 1, r = lane & 15, g = lane >> 4;
  f32x32 accv;
  {
    const unsigned lb = (unsigned)(size_t)(lds_cptr)lds + cl * CV_CH;
    const unsigned hb = lb + (2032 - 16 * r + 8 * g) * 2 + rr * 256 - 7 * 512;
    const unsigned ub = lb + CV_HR + ((r + 1) & 3) * CV_CS + (8 * g + ((r + 1) & ~3u)) * 2 + rr * 256;
    SBAR();
    asm volatile(
    "s_nop 7\n"
    "ds_read_b128 v[32:35], v160 offset:3584\n"
    "ds_read_b64 v[64:65], v161 offset:0\n"
    "ds_read_b64 v[66:67], v161 offset:8\n"
    "ds_read_b128 v[36:39], v160 offset:3072\n"
    "ds_read_b64 v[68:69], v161 offset:512\n"
    "ds_read_b64 v[70:71], v161 offset:520\n"
    "ds_read_b128 v[40:43], v160 offset:2560\n"
    "ds_read_b64 v[72:73], v161 offset:1024\n"
    "ds_read_b64 v[74:75], v161 offset:1032\n"
    "ds_read_b128 v[44:47], v160 offset:2048\n"
    "ds_read_b64 v[76:77], v161 offset:1536\n"
    "ds_read_b64 v[78:79], v161 offset:1544\n"
    "s_waitcnt lgkmcnt(9)\n"
    "v_mfma_f32_16x16x32_bf16 v[0:3], v[64:67], v[32:35], 0\n"
    "ds_read_b128 v[48:51], v160 offset:1536\n"
    "s_waitcnt lgkmcnt(9)\n"
    "v_mfma_f32_16x16x32_bf16 v[4:7], v[64:67], v[36:39], 0\n"
    "ds_read_b64 v[80:81], v161 offset:2048\n"
    "s_waitcnt lgkmcnt(8)\n"
    "v_mfma_f32_16x16x32_bf16 v[4:7], v[68:71], v[32:35], v[4:7]\n"
    "ds_read_b64 v[82:83], v161 offset:2056\n"
    "s_waitcnt lgkmcnt(8)\n"
    "v_mfma_f32_16x16x32_bf16 v[8:11], v[64:67], v[40:43], 0\n"
    "ds_read_b128 v[52:55], v160 offset:1024\n"
    "s_add_u32 s50, s49, 0\n"
    "buffer_load_dwordx4 a[0:3], v162, s[44:47], s50 offen offset:0\n"
    "v_mfma_f32_16x16x32_bf16 v[8:11], v[68:71], v[36:39], v[8:11]\n"
    "ds_read_b64 v[84:85], v161 offset:2560\n"
    "s_waitcnt lgkmcnt(8)\n"
    "v_mfma_f32_16x16x32_bf16 v[8:11], v[72:75], v[32:35], v[8:11]\n"
    "ds_read_b64 v[86:87], v161 offset:2568\n"
    "s_waitcnt lgkmcnt(8)\n"
    "v_mfma_f32_16x16x32_bf16 v[12:15], v[64:67], v[44:47], 0\n"
    "ds_read_b128 v[56:59], v160 offset:512\n"
    "v_mfma_f32_16x16x32_bf16 v[12:15], v[68:71], v[40:43], v[12:15]\n"
    "ds_read_b64 v[88:89], v161 offset:3072\n"
    "buffer_load_dwordx4 a[4:7], v162, s[44:47], s50 offen offset:1024\n"
    "v_mfma_f32_16x16x32_bf16 v[12:15], v[72:75], v[36:39], v[12:15]\n"
    "ds_read_b64 v[90:91], v161 offset:3080\n"
    "s_waitcnt lgkmcnt(9)\n"
    "v_mfma_f32_16x16x32_bf16 v[12:15], v[76:79], v[32:35], v[12:15]\n"
    "ds_read_b128 v[60:63], v160 offset:0\n"
    "s_waitcnt lgkmcnt(9)\n"
    "v_mfma_f32_16x16x32_bf16 v[16:19], v[64:67], v[48:51], 0\n"
    "ds_read_b64 v[92:93], v161 offset:3584\n"
    "v_mfma_f32_16x16x32_bf16 v[16:19], v[68:71], v[44:47], v[16:19]\n"
    "ds_read_b64 v[94:95], v161 offset:3592\n"
    "buffer_load_dwordx4 a[8:11], v162, s[44:47], s50 offen offset:2048\n"
    "v_mfma_f32_16x16x32_bf16 v[16:19], v[72:75], v[40:43], v[16:19]\n"
    "ds_read_b128 v[96:99], v160 offset:3648\n"
    "v_mfma_f32_16x16x32_bf16 v[16:19], v[76:79], v[36:39], v[16:19]\n"
    "ds_read_b64 v[128:129], v161 offset:64\n"
    "s_waitcnt lgkmcnt(11)\n"
    "v_mfma_f32_16x16x32_bf16 v[16:19], v[80:83], v[32:35], v[16:19]\n"
    "ds_read_b64 v[130:131], v161 offset:72\n"
    "s_waitcnt lgkmcnt(11)\n"
    "v_mfma_f32_16x16x32_bf16 v[20:23], v[64:67], v[52:55], 0\n"
    "ds_read_b128 v[100:103], v160 offset:3136\n"
    "buffer_load_dwordx4 a[12:15], v162, s[44:47], s50 offen offset:3072\n"
    "v_mfma_f32_16x16x32_bf16 v[20:23], v[68:71], v[48:51], v[20:23]\n"
    "ds_read_b64 v[132:133], v161 offset:576\n"
    "v_mfma_f32_16x16x32_bf16 v[20:23], v[72:75], v[44:47], v[20:23]\n"
    "ds_read_b64 v[134:135], v161 offset:584\n"
    "v_mfma_f32_16x16x32_bf16 v[20:23], v[76:79], v[40:43], v[20:23]\n"
    "ds_read_b128 v[104:107], v160 offset:2624\n"
    "v_mfma_f32_16x16x32_bf16 v[20:23], v[80:83], v[36:39], v[20:23]\n"
    "ds_read_b64 v[136:137], v161 offset:1088\n"
    "s_add_u32 s50, s49, 32768\n"
    "buffer_load_dwordx4 a[16:19], v162, s[44:47], s50 offen offset:0\n"
    "s_waitcnt lgkmcnt(14)\n"
    "v_mfma_f32_16x16x32_bf16 v[20:23], v[84:87], v[32:35], v[20:23]\n"
    "ds_read_b64 v[138:139], v161 offset:1096\n"
    "s_waitcnt lgkmcnt(14)\n"
    "v_mfma_f32_16x16x32_bf16 v[24:27], v[64:67], v[56:59], 0\n"
    "ds_read_b128 v[108:111], v160 offset:2112\n"
    "v_mfma_f32_16x16x32_bf16 v[24:27], v[68:71], v[52:55], v[24:27]\n"
    "ds_read_b64 v[140:141], v161 offset:1600\n"
    "v_mfma_f32_16x16x32_bf16 v[24:27], v[72:75], v[48:51], v[24:27]\n"
    "ds_read_b64 v[142:143], v161 offset:1608\n"
    "buffer_load_dwordx4 a[20:23], v162, s[44:47], s50 offen offset:1024\n"
    "v_mfma_f32_16x16x32_bf16 v[24:27], v[76:79], v[44:47], v[24:27]\n"
    "ds_read_b128 v[112:115], v160 offset:1600\n"
    "v_mfma_f32_16x16x32_bf16 v[24:27], v[80:83], v[40:43], v[24:27]\n"
    "ds_read_b64 v[144:145], v161 offset:2112\n"
    "v_mfma_f32_16x16x32_bf16 v[24:27], v[84:87], v[36:39], v[24:27]\n"
    "ds_read_b64 v[146:147], v161 offset:2120\n"
    "s_waitcnt lgkmcnt(15)\n"
    "v_mfma_f32_16x16x32_bf16 v[24:27], v[88:91], v[32:35], v[24:27]\n"
    "ds_read_b128 v[116:119], v160 offset:1088\n"
    "buffer_load_dwordx4 a[24:27], v162, s[44:47], s50 offen offset:2048\n"
    "s_waitcnt lgkmcnt(15)\n"
    "v_mfma_f32_16x16x32_bf16 v[28:31], v[64:67], v[60:63], 0\n"
    "ds_read_b64 v[148:149], v161 offset:2624\n"
    "v_mfma_f32_16x16x32_bf16 v[28:31], v[68:71], v[56:59], v[28:31]\n"
    "ds_read_b64 v[150:151], v161 offset:2632\n"
    "v_mfma_f32_16x16x32_bf16 v[28:31], v[72:75], v[52:55], v[28:31]\n"
    "ds_read_b128 v[120:123], v160 offset:576\n"
    "v_mfma_f32_16x16x32_bf16 v[28:31], v[76:79], v[48:51], v[28:31]\n"
    "ds_read_b64 v[152:153], v161 offset:3136\n"
    "buffer_load_dwordx4 a[28:31], v162, s[44:47], s50 offen offset:3072\n"
    "v_mfma_f32_16x16x32_bf16 v[28:31], v[80:83], v[44:47], v[28:31]\n"
    "ds_read_b64 v[154:155], v161 offset:3144\n"
    "v_mfma_f32_16x16x32_bf16 v[28:31], v[84:87], v[40:43], v[28:31]\n"
    "ds_read_b128 v[124:127], v160 offset:64\n"
    "v_mfma_f32_16x16x32_bf16 v[28:31], v[88:91], v[36:39], v[28:31]\n"
    "ds_read_b64 v[156:157], v161 offset:3648\n"
    "s_waitcnt lgkmcnt(15)\n"
    "v_mfma_f32_16x16x32_bf16 v[28:31], v[92:95], v[32:35], v[28:31]\n"
    "ds_read_b64 v[158:159], v161 offset:3656\n"
    "s_add_u32 s50, s49, 65536\n"
    "buffer_load_dwordx4 a[32:35], v162, s[44:47], s50 offen offset:0\n"
    "s_waitcnt lgkmcnt(15)\n"
    "v_mfma_f32_16x16x32_bf16 v[0:3], v[128:131], v[96:99], v[0:3]\n"
    "ds_read_b128 v[32:35], v160 offset:3712\n"
    "s_waitcnt lgkmcnt(15)\n"
    "v_mfma_f32_16x16x32_bf16 v[4:7], v[128:131], v[100:103], v[4:7]\n"
    "ds_read_b64 v[64:65], v161 offset:128\n"
    "s_waitcnt lgkmcnt(15)\n"
    "v_mfma_f32_16x16x32_bf16 v[4:7], v[132:135], v[96:99], v[4:7]\n"
    "ds_read_b64 v[66:67], v161 offset:136\n"
    "s_waitcnt lgkmcnt(15)\n"
    "v_mfma_f32_16x16x32_bf16 v[8:11], v[128:131], v[104:107], v[8:11]\n"
    "ds_read_b128 v[36:39], v160 offset:3200\n"
    "buffer_load_dwordx4 a[36:39], v162, s[44:47], s50 offen offset:1024\n"
    "v_mfma_f32_16x16x32_bf16 v[8:11], v[132:135], v[100:103], v[8:11]\n"
    "ds_read_b64 v[68:69], v161 offset:640\n"
    "s_waitcnt lgkmcnt(15)\n"
    "v_mfma_f32_16x16x32_bf16 v[8:11], v[136:139], v[96:99], v[8:11]\n"
    "ds_read_b64 v[70:71], v161 offset:648\n"
    "s_waitcnt lgkmcnt(15)\n"
    "v_mfma_f32_16x16x32_bf16 v[12:15], v[128:131], v[108:111], v[12:15]\n"
    "ds_read_b128 v[40:43], v160 offset:2688\n"
    "v_mfma_f32_16x16x32_bf16 v[12:15], v[132:135], v[104:107], v[12:15]\n"
    "ds_read_b64 v[72:73], v161 offset:1152\n"
    "buffer_load_dwordx4 a[40:43], v162, s[44:47], s50 offen offset:2048\n"
    "v_mfma_f32_16x16x32_bf16 v[12:15], v[136:139], v[100:103], v[12:15]\n"
    "ds_read_b64 v[74:75], v161 offset:1160\n"
    "s_waitcnt lgkmcnt(15)\n"
    "v_mfma_f32_16x16x32_bf16 v[12:15], v[140:143], v[96:99], v[12:15]\n"
    "ds_read_b128 v[44:47], v160 offset:2176\n"
    "s_waitcnt lgkmcnt(15)\n"
    "v_mfma_f32_16x16x32_bf16 v[16:19], v[128:131], v[112:115], v[16:19]\n"
    "ds_read_b64 v[76:77], v161 offset:1664\n"
    "v_mfma_f32_16x16x32_bf16 v[16:19], v[132:135], v[108:111], v[16:19]\n"
    "ds_read_b64 v[78:79], v161 offset:1672\n"
    "buffer_load_dwordx4 a[44:47], v162, s[44:47], s50 offen offset:3072\n"
    "v_mfma_f32_16x16x32_bf16 v[16:19], v[136:139], v[104:107], v[16:19]\n"
    "ds_read_b128 v[48:51], v160 offset:1664\n"
    "v_mfma_f32_16x16x32_bf16 v[16:19], v[140:143], v[100:103], v[16:19]\n"
    "ds_read_b64 v[80:81], v161 offset:2176\n"
    "s_waitcnt lgkmcnt(15)\n"
    "v_mfma_f32_16x16x32_bf16 v[16:19], v[144:147], v[96:99], v[16:19]\n"
    "ds_read_b64 v[82:83], v161 offset:2184\n"
    "s_waitcnt lgkmcnt(15)\n"
    "v_mfma_f32_16x16x32_bf16 v[20:23], v[128:131], v[116:119], v[20:23]\n"
    "ds_read_b128 v[52:55], v160 offset:1152\n"
    "s_add_u32 s50, s49, 98304\n"
    "buffer_load_dwordx4 a[48:51], v162, s[44:47], s50 offen offset:0\n"
    "v_mfma_f32_16x16x32_bf16 v[20:23], v[132:135], v[112:115], v[20:23]\n"
    "ds_read_b64 v[84:85], v161 offset:2688\n"
    "v_mfma_f32_16x16x32_bf16 v[20:23], v[136:139], v[108:111], v[20:23]\n"
    "ds_read_b64 v[86:87], v161 offset:2696\n"
    "v_mfma_f32_16x16x32_bf16 v[20:23], v[140:143], v[104:107], v[20:23]\n"
    "ds_read_b128 v[56:59], v160 offset:640\n"
    "v_mfma_f32_16x16x32_bf16 v[20:23], v[144:147], v[100:103], v[20:23]\n"
    "ds_read_b64 v[88:89], v161 offset:3200\n"
    "buffer_load_dwordx4 a[52:55], v162, s[44:47], s50 offen offset:1024\n"
    "s_waitcnt lgkmcnt(15)\n"
    "v_mfma_f32_16x16x32_bf16 v[20:23], v[148:151], v[96:99], v[20:23]\n"
    "ds_read_b64 v[90:91], v161 offset:3208\n"
    "s_waitcnt lgkmcnt(15)\n"
    "v_mfma_f32_16x16x32_bf16 v[24:27], v[128:131], v[120:123], v[24:27]\n"
    "ds_read_b128 v[60:63], v160 offset:128\n"
    "v_mfma_f32_16x16x32_bf16 v[24:27], v[132:135], v[116:119], v[24:27]\n"
    "ds_read_b64 v[92:93], v161 offset:3712\n"
    "v_mfma_f32_16x16x32_bf16 v[24:27], v[136:139], v[112:115], v[24:27]\n"
    "ds_read_b64 v[94:95], v161 offset:3720\n"
    "buffer_load_dwordx4 a[56:59], v162, s[44:47], s50 offen offset:2048\n"
    "v_mfma_f32_16x16x32_bf16 v[24:27], v[140:143], v[108:111], v[24:27]\n"
    "v_mfma_f32_16x16x32_bf16 v[24:27], v[144:147], v[104:107], v[24:27]\n"
    "v_mfma_f32_16x16x32_bf16 v[24:27], v[148:151], v[100:103], v[24:27]\n"
    "s_waitcnt lgkmcnt(15)\n"
    "v_mfma_f32_16x16x32_bf16 v[24:27], v[152:155], v[96:99], v[24:27]\n"
    "buffer_load_dwordx4 a[60:63], v162, s[44:47], s50 offen offset:3072\n"
    "s_waitcnt lgkmcnt(15)\n"
    "v_mfma_f32_16x16x32_bf16 v[28:31], v[128:131], v[124:127], v[28:31]\n"
    "v_mfma_f32_16x16x32_bf16 v[28:31], v[132:135], v[120:123], v[28:31]\n"
    "v_mfma_f32_16x16x32_bf16 v[28:31], v[136:139], v[116:119], v[28:31]\n"
    "v_mfma_f32_16x16x32_bf16 v[28:31], v[140:143], v[112:115], v[28:31]\n"
    "s_add_u32 s50, s49, 131072\n"
    "buffer_load_dwordx4 a[64:67], v162, s[44:47], s50 offen offset:0\n"
    "v_mfma_f32_16x16x32_bf16 v[28:31], v[144:147], v[108:111], v[28:31]\n"
    "v_mfma_f32_16x16x32_bf16 v[28:31], v[148:151], v[104:107], v[28:31]\n"
    "v_mfma_f32_16x16x32_bf16 v[28:31], v[152:155], v[100:103], v[28:31]\n"
    "s_waitcnt lgkmcnt(15)\n"
    "v_mfma_f32_16x16x32_bf16 v[28:31], v[156:159], v[96:99], v[28:31]\n"
    "buffer_load_dwordx4 a[68:71], v162, s[44:47], s50 offen offset:1024\n"
    "s_waitcnt lgkmcnt(15)\n"
    "v_mfma_f32_16x16x32_bf16 v[0:3], v[64:67], v[32:35], v[0:3]\n"
    "ds_read_b128 v[96:99], v160 offset:3776\n"
    "s_waitcnt lgkmcnt(15)\n"
    "v_mfma_f32_16x16x32_bf16 v[4:7], v[64:67], v[36:39], v[4:7]\n"
    "ds_read_b64 v[128:129], v161 offset:192\n"
    "s_waitcnt lgkmcnt(15)\n"
    "v_mfma_f32_16x16x32_bf16 v[4:7], v[68:71], v[32:35], v[4:7]\n"
    "ds_read_b64 v[130:131], v161 offset:200\n"
    "s_waitcnt lgkmcnt(15)\n"
    "v_mfma_f32_16x16x32_bf16 v[8:11], v[64:67], v[40:43], v[8:11]\n"
    "ds_read_b128 v[100:103], v160 offset:3264\n"
    "buffer_load_dwordx4 a[72:75], v162, s[44:47], s50 offen offset:2048\n"
    "v_mfma_f32_16x16x32_bf16 v[8:11], v[68:71], v[36:39], v[8:11]\n"
    "ds_read_b64 v[132:133], v161 offset:704\n"
    "s_waitcnt lgkmcnt(15)\n"
    "v_mfma_f32_16x16x32_bf16 v[8:11], v[72:75], v[32:35], v[8:11]\n"
    "ds_read_b64 v[134:135], v161 offset:712\n"
    "s_waitcnt lgkmcnt(15)\n"
    "v_mfma_f32_16x16x32_bf16 v[12:15], v[64:67], v[44:47], v[12:15]\n"
    "ds_read_b128 v[104:107], v160 offset:2752\n"
    "v_mfma_f32_16x16x32_bf16 v[12:15], v[68:71], v[40:43], v[12:15]\n"
    "ds_read_b64 v[136:137], v161 offset:1216\n"
    "buffer_load_dwordx4 a[76:79], v162, s[44:47], s50 offen offset:3072\n"
    "v_mfma_f32_16x16x32_bf16 v[12:15], v[72:75], v[36:39], v[12:15]\n"
    "ds_read_b64 v[138:139], v161 offset:1224\n"
    "s_waitcnt lgkmcnt(15)\n"
    "v_mfma_f32_16x16x32_bf16 v[12:15], v[76:79], v[32:35], v[12:15]\n"
    "ds_read_b128 v[108:111], v160 offset:2240\n"
    "s_waitcnt lgkmcnt(15)\n"
    "v_mfma_f32_16x16x32_bf16 v[16:19], v[64:67], v[48:51], v[16:19]\n"
    "ds_read_b64 v[140:141], v161 offset:1728\n"
    "v_mfma_f32_16x16x32_bf16 v[16:19], v[68:71], v[44:47], v[16:19]\n"
    "ds_read_b64 v[142:143], v161 offset:1736\n"
    "s_add_u32 s50, s49, 163840\n"
    "buffer_load_dwordx4 a[80:83], v162, s[44:47], s50 offen offset:0\n"
    "v_mfma_f32_16x16x32_bf16 v[16:19], v[72:75], v[40:43], v[16:19]\n"
    "ds_read_b128 v[112:115], v160 offset:1728\n"
    "v_mfma_f32_16x16x32_bf16 v[16:19], v[76:79], v[36:39], v[16:19]\n"
    "ds_read_b64 v[144:145], v161 offset:2240\n"
    "s_waitcnt lgkmcnt(15)\n"
    "v_mfma_f32_16x16x32_bf16 v[16:19], v[80:83], v[32:35], v[16:19]\n"
    "ds_read_b64 v[146:147], v161 offset:2248\n"
    "s_waitcnt lgkmcnt(15)\n"
    "v_mfma_f32_16x16x32_bf16 v[20:23], v[64:67], v[52:55], v[20:23]\n"
    "ds_read_b128 v[116:119], v160 offset:1216\n"
    "buffer_load_dwordx4 a[84:87], v162, s[44:47], s50 offen offset:1024\n"
    "v_mfma_f32_16x16x32_bf16 v[20:23], v[68:71], v[48:51], v[20:23]\n"
    "ds_read_b64 v[148:149], v161 offset:2752\n"
    "v_mfma_f32_16x16x32_bf16 v[20:23], v[72:75], v[44:47], v[20:23]\n"
    "ds_read_b64 v[150:151], v161 offset:2760\n"
    "v_mfma_f32_16x16x32_bf16 v[20:23], v[76:79], v[40:43], v[20:23]\n"
    "ds_read_b128 v[120:123], v160 offset:704\n"
    "v_mfma_f32_16x16x32_bf16 v[20:23], v[80:83], v[36:39], v[20:23]\n"
    "ds_read_b64 v[152:153], v161 offset:3264\n"
    "buffer_load_dwordx4 a[88:91], v162, s[44:47], s50 offen offset:2048\n"
    "s_waitcnt lgkmcnt(15)\n"
    "v_mfma_f32_16x16x32_bf16 v[20:23], v[84:87], v[32:35], v[20:23]\n"
    "ds_read_b64 v[154:155], v161 offset:3272\n"
    "s_waitcnt lgkmcnt(15)\n"
    "v_mfma_f32_16x16x32_bf16 v[24:27], v[64:67], v[56:59], v[24:27]\n"
    "ds_read_b128 v[124:127], v160 offset:192\n"
    "v_mfma_f32_16x16x32_bf16 v[24:27], v[68:71], v[52:55], v[24:27]\n"
    "ds_read_b64 v[156:157], v161 offset:3776\n"
    "v_mfma_f32_16x16x32_bf16 v[24:27], v[72:75], v[48:51], v[24:27]\n"
    "ds_read_b64 v[158:159], v161 offset:3784\n"
    "buffer_load_dwordx4 a[92:95], v162, s[44:47], s50 offen offset:3072\n"
    "v_mfma_f32_16x16x32_bf16 v[24:27], v[76:79], v[44:47], v[24:27]\n"
    "v_mfma_f32_16x16x32_bf16 v[24:27], v[80:83], v[40:43], v[24:27]\n"
    "v_mfma_f32_16x16x32_bf16 v[24:27], v[84:87], v[36:39], v[24:27]\n"
    "s_waitcnt lgkmcnt(15)\n"
    "v_mfma_f32_16x16x32_bf16 v[24:27], v[88:91], v[32:35], v[24:27]\n"
    "s_add_u32 s50, s49, 196608\n"
    "buffer_load_dwordx4 a[96:99], v162, s[44:47], s50 offen offset:0\n"
    "s_waitcnt lgkmcnt(15)\n"
    "v_mfma_f32_16x16x32_bf16 v[28:31], v[64:67], v[60:63], v[28:31]\n"
    "v_mfma_f32_16x16x32_bf16 v[28:31], v[68:71], v[56:59], v[28:31]\n"
    "v_mfma_f32_16x16x32_bf16 v[28:31], v[72:75], v[52:55], v[28:31]\n"
    "v_mfma_f32_16x16x32_bf16 v[28:31], v[76:79], v[48:51], v[28:31]\n"
    "buffer_load_dwordx4 a[100:103], v162, s[44:47], s50 offen offset:1024\n"
    "v_mfma_f32_16x16x32_bf16 v[28:31], v[80:83], v[44:47], v[28:31]\n"
    "v_mfma_f32_16x16x32_bf16 v[28:31], v[84:87], v[40:43], v[28:31]\n"
    "v_mfma_f32_16x16x32_bf16 v[28:31], v[88:91], v[36:39], v[28:31]\n"
    "s_waitcnt lgkmcnt(15)\n"
    "v_mfma_f32_16x16x32_bf16 v[28:31], v[92:95], v[32:35], v[28:31]\n"
    "buffer_load_dwordx4 a[104:107], v162, s[44:47], s50 offen offset:2048\n"
    "s_waitcnt lgkmcnt(15)\n"
    "v_mfma_f32_16x16x32_bf16 v[0:3], v[128:131], v[96:99], v[0:3]\n"
    "s_waitcnt lgkmcnt(15)\n"
    "v_mfma_f32_16x16x32_bf16 v[4:7], v[128:131], v[100:103], v[4:7]\n"
    "s_waitcnt lgkmcnt(15)\n"
    "v_mfma_f32_16x16x32_bf16 v[4:7], v[132:135], v[96:99], v[4:7]\n"
    "s_waitcnt lgkmcnt(15)\n"
    "v_mfma_f32_16x16x32_bf16 v[8:11], v[128:131], v[104:107], v[8:11]\n"
    "buffer_load_dwordx4 a[108:111], v162, s[44:47], s50 offen offset:3072\n"
    "v_mfma_f32_16x16x32_bf16 v[8:11], v[132:135], v[100:103], v[8:11]\n"
    "s_waitcnt lgkmcnt(15)\n"
    "v_mfma_f32_16x16x32_bf16 v[8:11], v[136:139], v[96:99], v[8:11]\n"
    "s_waitcnt lgkmcnt(14)\n"
    "v_mfma_f32_16x16x32_bf16 v[12:15], v[128:131], v[108:111], v[12:15]\n"
    "v_mfma_f32_16x16x32_bf16 v[12:15], v[132:135], v[104:107], v[12:15]\n"
    "s_add_u32 s50, s49, 229376\n"
    "buffer_load_dwordx4 a[112:115], v162, s[44:47], s50 offen offset:0\n"
    "v_mfma_f32_16x16x32_bf16 v[12:15], v[136:139], v[100:103], v[12:15]\n"
    "s_waitcnt lgkmcnt(12)\n"
    "v_mfma_f32_16x16x32_bf16 v[12:15], v[140:143], v[96:99], v[12:15]\n"
    "s_waitcnt lgkmcnt(11)\n"
    "v_mfma_f32_16x16x32_bf16 v[16:19], v[128:131], v[112:115], v[16:19]\n"
    "v_mfma_f32_16x16x32_bf16 v[16:19], v[132:135], v[108:111], v[16:19]\n"
    "buffer_load_dwordx4 a[116:119], v162, s[44:47], s50 offen offset:1024\n"
    "v_mfma_f32_16x16x32_bf16 v[16:19], v[136:139], v[104:107], v[16:19]\n"
    "v_mfma_f32_16x16x32_bf16 v[16:19], v[140:143], v[100:103], v[16:19]\n"
    "s_waitcnt lgkmcnt(9)\n"
    "v_mfma_f32_16x16x32_bf16 v[16:19], v[144:147], v[96:99], v[16:19]\n"
    "s_waitcnt lgkmcnt(8)\n"
    "v_mfma_f32_16x16x32_bf16 v[20:23], v[128:131], v[116:119], v[20:23]\n"
    "buffer_load_dwordx4 a[120:123], v162, s[44:47], s50 offen offset:2048\n"
    "v_mfma_f32_16x16x32_bf16 v[20:23], v[132:135], v[112:115], v[20:23]\n"
    "v_mfma_f32_16x16x32_bf16 v[20:23], v[136:139], v[108:111], v[20:23]\n"
    "v_mfma_f32_16x16x32_bf16 v[20:23], v[140:143], v[104:107], v[20:23]\n"
    "v_mfma_f32_16x16x32_bf16 v[20:23], v[144:147], v[100:103], v[20:23]\n"
    "buffer_load_dwordx4 a[124:127], v162, s[44:47], s50 offen offset:3072\n"
    "s_waitcnt lgkmcnt(6)\n"
    "v_mfma_f32_16x16x32_bf16 v[20:23], v[148:151], v[96:99], v[20:23]\n"
    "s_waitcnt lgkmcnt(5)\n"
    "v_mfma_f32_16x16x32_bf16 v[24:27], v[128:131], v[120:123], v[24:27]\n"
    "v_mfma_f32_16x16x32_bf16 v[24:27], v[132:135], v[116:119], v[24:27]\n"
    "v_mfma_f32_16x16x32_bf16 v[24:27], v[136:139], v[112:115], v[24:27]\n"
    "s_add_u32 s50, s49, 262144\n"
    "buffer_load_dwordx4 a[128:131], v162, s[44:47], s50 offen offset:0\n"
    "v_mfma_f32_16x16x32_bf16 v[24:27], v[140:143], v[108:111], v[24:27]\n"
    "v_mfma_f32_16x16x32_bf16 v[24:27], v[144:147], v[104:107], v[24:27]\n"
    "v_mfma_f32_16x16x32_bf16 v[24:27], v[148:151], v[100:103], v[24:27]\n"
    "s_waitcnt lgkmcnt(3)\n"
    "v_mfma_f32_16x16x32_bf16 v[24:27], v[152:155], v[96:99], v[24:27]\n"
    "buffer_load_dwordx4 a[132:135], v162, s[44:47], s50 offen offset:1024\n"
    "s_waitcnt lgkmcnt(2)\n"
    "v_mfma_f32_16x16x32_bf16 v[28:31], v[128:131], v[124:127], v[28:31]\n"
    "v_mfma_f32_16x16x32_bf16 v[28:31], v[132:135], v[120:123], v[28:31]\n"
    "v_mfma_f32_16x16x32_bf16 v[28:31], v[136:139], v[116:119], v[28:31]\n"
    "v_mfma_f32_16x16x32_bf16 v[28:31], v[140:143], v[112:115], v[28:31]\n"
    "buffer_load_dwordx4 a[136:139], v162, s[44:47], s50 offen offset:2048\n"
    "v_mfma_f32_16x16x32_bf16 v[28:31], v[144:147], v[108:111], v[28:31]\n"
    "v_mfma_f32_16x16x32_bf16 v[28:31], v[148:151], v[104:107], v[28:31]\n"
    "v_mfma_f32_16x16x32_bf16 v[28:31], v[152:155], v[100:103], v[28:31]\n"
    "s_waitcnt lgkmcnt(0)\n"
    "v_mfma_f32_16x16x32_bf16 v[28:31], v[156:159], v[96:99], v[28:31]\n"
    "buffer_load_dwordx4 a[140:143], v162, s[44:47], s50 offen offset:3072\n"
    "s_nop 6\n"
      : "={v[0:31]}"(accv), "={a[0:15]}"(WR.w[0]), "={a[16:31]}"(WR.w[1]), "={a[32:47]}"(WR.w[2]), "={a[48:63]}"(WR.w[3]), "={a[64:79]}"(WR.w[4]),
        "={a[80:95]}"(WR.w[5]), "={a[96:111]}"(WR.w[6]), "={a[112:127]}"(WR.w[7]), "={a[128:143]}"(WR.w[8])
      : "{v160}"(hb), "{v161}"(ub), "{v162}"(lane * 16), "{s[44:47]}"(WR.srd), "{s49}"(WR.off) : "v32", "v33", "v34", "v35", "v36", "v37", "v38", "v39", "v40", "v41", "v42", "v43", "v44", "v45", "v46", "v47", "v48", "v49", "v50", "v51", "v52", "v53", "v54", "v55", "v56", "v57", "v58", "v59", "v60", "v61", "v62", "v63", "v64", "v65", "v66", "v67", "v68", "v69", "v70", "v71", "v72", "v73", "v74", "v75", "v76", "v77", "v78", "v79", "v80", "v81", "v82", "v83", "v84", "v85", "v86", "v87", "v88", "v89", "v90", "v91", "v92", "v93", "v94", "v95", "v96", "v97", "v98", "v99", "v100", "v101", "v102", "v103", "v104", "v105", "v106", "v107", "v108", "v109", "v110", "v111", "v112", "v113", "v114", "v115", "v116", "v117", "v118", "v119", "v120", "v121", "v122", "v123", "v124", "v125", "v126", "v127", "v128", "v129", "v130", "v131", "v132", "v133", "v134", "v135", "v136", "v137", "v138", "v139", "v140", "v141", "v142", "v143", "v144", "v145", "v146", "v147", "v148", "v149", "v150", "v151", "v152", "v153", "v154", "v155", "v156", "v157", "v158", "v159", "s50", "scc", "memory");
    SBAR();
  }
  f32x4 acc[8];
#pragma unroll
  for (int R = 0; R < 8; ++R) acc[R] = f32x4{accv[4 * R], accv[4 * R + 1], accv[4 * R + 2], accv[4 * R + 3]};
  LBAR();
  char* red = lds;
#pragma unroll
  for (int R = 0; R < 8; ++R) *(f32x4*)(red + (cl * 2 + rr) * 10240 + (16 * R + r) * 80 + 16 * g) = acc[R];
  mid();
  WO_STEP(WR, 9, lane * 16);
  LBAR();
#pragma unroll
  for (int c2 = 0; c2 < 2; ++c2) {
    const char* r0 = red + (c2 * 2) * 10240 + (i >> 1) * 80 + (i & 1) * 32;
    const f32x4 p0 = *(const f32x4*)r0 + *(const f32x4*)(r0 + 10240), p1 = *(const f32x4*)(r0 + 16) + *(const f32x4*)(r0 + 10240 + 16);
    SBAR();
    if (c2) WO_STEP(WR, 12, lane * 16); else WO_STEP(WR, 10, lane * 16);
    SBAR();
    const float Dv = c2 ? bfhi(dpair) : bflo(dpair);
    float o[8];
#pragma unroll
    for (int e = 0; e < 8; ++e) {
      const float kx = (e & 1) ? bfhi(av[c2][e >> 1]) : bflo(av[c2][e >> 1]), qx = (e & 1) ? bfhi(qv[c2][e >> 1]) : bflo(qv[c2][e >> 1]);
      const float y2 = bfr(bfr(e < 4 ? p0[e & 3] : p1[e & 3]) + bfr(kx * Dv));
      o[e] = (e < 4 ? g0[e & 3] : g1[e & 3]) * bfr(qx * y2);
    }
    const u32x4 res = {cvtpk(o[0], o[1]), cvtpk(o[2], o[3]), cvtpk(o[4], o[5]), cvtpk(o[6], o[7])};
    SBAR();
    if (c2) WO_STEP(WR, 13, lane * 16); else WO_STEP(WR, 11, lane * 16);
    SBAR();
    asm volatile("global_store_dwordx4 %0, %1, off sc1\n\ts_nop 1" :: "v"(ws + WS_AT + ((size_t)(ch0 + c2) * SEQ + i * 8) * 2), "v"(res) : "memory");
  }
  WO_STEP(WR, 14, lane * 16);
}

constexpr int K2_LDS = 163840;
typedef __attribute__((address_space(1))) unsigned gu32;
DI void wait_count(gu32* cnt, unsigned want) {
  for (unsigned spins = 0; spins < (1u << 22); ++spins) {
    if (rfl(__hip_atomic_load(cnt, __ATOMIC_RELAXED, __HIP_MEMORY_SCOPE_AGENT)) >= want) break;
    __builtin_amdgcn_s_sleep(1);
  }
}
DI void attn_redo(const char* __restrict__ xin, const char* __restrict__ wq, char* __restrict__ ws, unsigned h, unsigned blk, unsigned wid) {
  const unsigned lane = lane_id(), r16 = lane & 15, g = lane >> 4;
  const unsigned row0 = blk * 128 + wid * 32;
  bf16x8 Q[2][4];
  {
    f32x4 acc[8][2];
#pragma unroll
    for (int nb = 0; nb < 8; ++nb) { acc[nb][0] = f32x4{}; acc[nb][1] = f32x4{}; }
    for (int ks = 0; ks < 16; ++ks) {
      bf16x8 xf[2];
#pragma unroll
      for (int qb = 0; qb < 2; ++qb) xf[qb] = *(const bf16x8*)(xin + (size_t)(row0 + 16 * qb + r16) * 1024 + ks * 64 + g * 16);
#pragma unroll
      for (int nb = 0; nb < 8; ++nb) {
        bf16x8 w;
#pragma unroll
        for (int e = 0; e < 8; ++e) w[e] = *(const short*)(wq + (size_t)(ks * 32 + g * 8 + e) * 8192 + h * 256 + (nb * 16 + r16) * 2);
        acc[nb][0] = MFMA16(w, xf[0], acc[nb][0]); acc[nb][1] = MFMA16(w, xf[1], acc[nb][1]);
      }
    }
#pragma unroll
    for (int qb = 0; qb < 2; ++qb)
#pragma unroll
      for (int ds = 0; ds < 4; ++ds) Q[qb][ds] = pack8(acc[2 * ds][qb] * QSCALE, acc[2 * ds + 1][qb] * QSCALE);
  }
  f32x4 O[8][2]; float m[2] = {-__builtin_inff(), -__builtin_inff()}, l[2] = {0.f, 0.f};
#pragma unroll
  for (int db = 0; db < 8; ++db) { O[db][0] = f32x4{}; O[db][1] = f32x4{}; }
  const char* Kh = ws + WS_KF + (size_t)h * (128 * 4096) + lane * 16;
  const char* Vh = ws + WS_VF + (size_t)h * (64 * 8192) + lane * 16;
  const unsigned nt = 2 * (blk + 1);
  for (unsigned t = 0; t < nt; ++t) {
    f32x4 S[4][2];
#pragma unroll
    for (int kb = 0; kb < 4; ++kb) {
      S[kb][0] = f32x4{}; S[kb][1] = f32x4{};
#pragma unroll
      for (int ds = 0; ds < 4; ++ds) {
        const bf16x8 k = *(const bf16x8*)(Kh + (size_t)t * 16384 + kb * 4096 + ds * 1024);
        S[kb][0] = MFMA16(k, Q[0][ds], S[kb][0]); S[kb][1] = MFMA16(k, Q[1][ds], S[kb][1]);
      }
    }
    bf16x8 P[2][2];
#pragma unroll
    for (int qb = 0; qb < 2; ++qb) {
      const int row = (int)(row0 + 16 * qb + r16);
      float mx = m[qb];
#pragma unroll
      for (int kb = 0; kb < 4; ++kb)
#pragma unroll
        for (int r = 0; r < 4; ++r) {
          if ((int)(t * 64 + kb * 16 + g * 4 + r) > row) S[kb][qb][r] = -__builtin_inff();
          mx = fmaxf(mx, S[kb][qb][r]);
        }
      mx = fmaxf(mx, __shfl_xor(mx, 16)); mx = fmaxf(mx, __shfl_xor(mx, 32));
      const float al = __builtin_amdgcn_exp2f(m[qb] - mx);
      m[qb] = mx;
      float s = 0.f;
#pragma unroll
      for (int kb = 0; kb < 4; ++kb)
#pragma unroll
        for (int r = 0; r < 4; ++r) { S[kb][qb][r] = __builtin_amdgcn_exp2f(S[kb][qb][r] - mx); s += S[kb][qb][r]; }
      s += __shfl_xor(s, 16); s += __shfl_xor(s, 32);
      l[qb] = l[qb] * al + s;
#pragma unroll
      for (int db = 0; db < 8; ++db) O[db][qb] *= al;
      P[0][qb] = pack8(S[0][qb], S[1][qb]); P[1][qb] = pack8(S[2][qb], S[3][qb]);
    }
#pragma unroll
    for (int ks = 0; ks < 2; ++ks)
#pragma unroll
      for (int db = 0; db < 8; ++db) {
        const bf16x8 vv = *(const bf16x8*)(Vh + (size_t)t * 16384 + ks * 8192 + db * 1024);
        O[db][0] = MFMA16(vv, P[ks][0], O[db][0]); O[db][1] = MFMA16(vv, P[ks][1], O[db][1]);
      }
  }
#pragma unroll
  for (int qb = 0; qb < 2; ++qb) {
    const float inv = 1.f / l[qb];
#pragma unroll
    for (int ds = 0; ds < 4; ++ds) {
      const bf16x8 f = pack8(O[2 * ds][qb] * inv, O[2 * ds + 1][qb] * inv);
      char* dst = ws + WS_CF + ((size_t)(blk * 8 + wid * 2 + qb) * NH + h) * 4096 + ds * 1024 + lane * 16;
      asm volatile("global_store_dwordx4 %0, %1, off sc1\n\ts_nop 1" :: "v"(dst), "v"(f) : "memory");
    }
  }
  asm volatile("s_waitcnt vmcnt(0)" ::: "memory");
}

__global__ __launch_bounds__(256, 1) void k2_attn(const char* __restrict__ xin, const char* __restrict__ wq, const unsigned short* __restrict__ ssk,
                                                  const unsigned short* __restrict__ ssd, char* __restrict__ ws, unsigned short* __restrict__ out) {
  extern __shared__ __attribute__((aligned(16))) char lds_[];
  const unsigned wid = rfl(threadIdx.x >> 6);
  const unsigned xcd = blockIdx.x & 7, j = blockIdx.x >> 3, h = xcd * 4 + (j & 3), x = j >> 2;
  asm volatile("" :: "s"(xin), "s"(wq), "s"(ssk), "s"(ssd), "s"(ws), "s"(out));
  unsigned flags, dvec;
  u32x4 sX = srd4(xin, SEQ * 1024), sW = srd4(wq + h * 256, 512 * 8192 - h * 256);
  const unsigned s88 = rfl(wid * 8192), s89 = rfl(wid * 2048), s90 = rfl(((15 - x) * 128 + wid * 32) * 1024), s91 = rfl((x * 128 + wid * 32) * 1024), s92 = rfl(wid * 16 * 8192);
  {
    const unsigned lane = lane_id(), rip = lane >> 3, pos = lane & 7;
    const u32x4 B = {rip * 1024 + 16 * (pos ^ ((rip >> 1) & 7)), rip * 1024 + 16 * (pos ^ ((4 + (rip >> 1)) & 7)),
                     rip * 8192 + 16 * (pos ^ (2 * ((rip >> 1) & 1))), rip * 8192 + 16 * (pos ^ (2 * (((rip >> 1) & 1) | 2)))};
    SBAR();
    asm volatile(
    "s_nop 7\n"
    "s_add_u32 m0, s88, 65536\n"
    "s_add_u32 s64, s90, 0\n"
    "buffer_load_dwordx4 v232, s[80:83], s64 offen lds\n"
    "s_add_u32 m0, s88, 66560\n"
    "s_add_u32 s64, s90, 8192\n"
    "buffer_load_dwordx4 v233, s[80:83], s64 offen lds\n"
    "s_add_u32 m0, s88, 67584\n"
    "s_add_u32 s64, s90, 16384\n"
    "buffer_load_dwordx4 v232, s[80:83], s64 offen lds\n"
    "s_add_u32 m0, s88, 68608\n"
    "s_add_u32 s64, s90, 24576\n"
    "buffer_load_dwordx4 v233, s[80:83], s64 offen lds\n"
    "s_add_u32 m0, s88, 69632\n"
    "s_add_u32 s64, s91, 0\n"
    "buffer_load_dwordx4 v232, s[80:83], s64 offen lds\n"
    "s_add_u32 m0, s88, 70656\n"
    "s_add_u32 s64, s91, 8192\n"
    "buffer_load_dwordx4 v233, s[80:83], s64 offen lds\n"
    "s_add_u32 m0, s88, 71680\n"
    "s_add_u32 s64, s91, 16384\n"
    "buffer_load_dwordx4 v232, s[80:83], s64 offen lds\n"
    "s_add_u32 m0, s88, 72704\n"
    "s_add_u32 s64, s91, 24576\n"
    "buffer_load_dwordx4 v233, s[80:83], s64 offen lds\n"
    "s_add_u32 m0, s89, 0\n"
    "s_add_u32 s64, s92, 0\n"
    "buffer_load_dwordx4 v234, s[84:87], s64 offen lds\n"
    "s_add_u32 m0, s89, 1024\n"
    "s_add_u32 s64, s92, 65536\n"
    "buffer_load_dwordx4 v235, s[84:87], s64 offen lds\n"
    "s_add_u32 m0, s89, 8192\n"
    "s_add_u32 s64, s92, 128\n"
    "buffer_load_dwordx4 v234, s[84:87], s64 offen lds\n"
    "s_add_u32 m0, s89, 9216\n"
    "s_add_u32 s64, s92, 65664\n"
    "buffer_load_dwordx4 v235, s[84:87], s64 offen lds\n"
    "s_add_u32 m0, s88, 98304\n"
    "s_add_u32 s64, s90, 128\n"
    "buffer_load_dwordx4 v232, s[80:83], s64 offen lds\n"
    "s_add_u32 m0, s88, 99328\n"
    "s_add_u32 s64, s90, 8320\n"
    "buffer_load_dwordx4 v233, s[80:83], s64 offen lds\n"
    "s_add_u32 m0, s88, 100352\n"
    "s_add_u32 s64, s90, 16512\n"
    "buffer_load_dwordx4 v232, s[80:83], s64 offen lds\n"
    "s_add_u32 m0, s88, 101376\n"
    "s_add_u32 s64, s90, 24704\n"
    "buffer_load_dwordx4 v233, s[80:83], s64 offen lds\n"
    "s_add_u32 m0, s88, 102400\n"
    "s_add_u32 s64, s91, 128\n"
    "buffer_load_dwordx4 v232, s[80:83], s64 offen lds\n"
    "s_add_u32 m0, s88, 103424\n"
    "s_add_u32 s64, s91, 8320\n"
    "buffer_load_dwordx4 v233, s[80:83], s64 offen lds\n"
    "s_add_u32 m0, s88, 104448\n"
    "s_add_u32 s64, s91, 16512\n"
    "buffer_load_dwordx4 v232, s[80:83], s64 offen lds\n"
    "s_add_u32 m0, s88, 105472\n"
    "s_add_u32 s64, s91, 24704\n"
    "buffer_load_dwordx4 v233, s[80:83], s64 offen lds\n"
    "s_add_u32 m0, s89, 16384\n"
    "s_add_u32 s64, s92, 524288\n"
    "buffer_load_dwordx4 v234, s[84:87], s64 offen lds\n"
    "s_add_u32 m0, s89, 17408\n"
    "s_add_u32 s64, s92, 589824\n"
    "buffer_load_dwordx4 v235, s[84:87], s64 offen lds\n"
    "s_add_u32 m0, s89, 24576\n"
    "s_add_u32 s64, s92, 524416\n"
    "buffer_load_dwordx4 v234, s[84:87], s64 offen lds\n"
    "s_add_u32 m0, s89, 25600\n"
    "s_add_u32 s64, s92, 589952\n"
    "buffer_load_dwordx4 v235, s[84:87], s64 offen lds\n"
    "s_add_u32 m0, s88, 131072\n"
    "s_add_u32 s64, s90, 256\n"
    "buffer_load_dwordx4 v232, s[80:83], s64 offen lds\n"
    "s_add_u32 m0, s88, 132096\n"
    "s_add_u32 s64, s90, 8448\n"
    "buffer_load_dwordx4 v233, s[80:83], s64 offen lds\n"
    "s_add_u32 m0, s88, 133120\n"
    "s_add_u32 s64, s90, 16640\n"
    "buffer_load_dwordx4 v232, s[80:83], s64 offen lds\n"
    "s_add_u32 m0, s88, 134144\n"
    "s_add_u32 s64, s90, 24832\n"
    "buffer_load_dwordx4 v233, s[80:83], s64 offen lds\n"
    "s_add_u32 m0, s88, 135168\n"
    "s_add_u32 s64, s91, 256\n"
    "buffer_load_dwordx4 v232, s[80:83], s64 offen lds\n"
    "s_add_u32 m0, s88, 136192\n"
    "s_add_u32 s64, s91, 8448\n"
    "buffer_load_dwordx4 v233, s[80:83], s64 offen lds\n"
    "s_add_u32 m0, s88, 137216\n"
    "s_add_u32 s64, s91, 16640\n"
    "buffer_load_dwordx4 v232, s[80:83], s64 offen lds\n"
    "s_add_u32 m0, s88, 138240\n"
    "s_add_u32 s64, s91, 24832\n"
    "buffer_load_dwordx4 v233, s[80:83], s64 offen lds\n"
    "s_add_u32 m0, s89, 32768\n"
    "s_add_u32 s64, s92, 1048576\n"
    "buffer_load_dwordx4 v234, s[84:87], s64 offen lds\n"
    "s_add_u32 m0, s89, 33792\n"
    "s_add_u32 s64, s92, 1114112\n"
    "buffer_load_dwordx4 v235, s[84:87], s64 offen lds\n"
    "s_add_u32 m0, s89, 40960\n"
    "s_add_u32 s64, s92, 1048704\n"
    "buffer_load_dwordx4 v234, s[84:87], s64 offen lds\n"
    "s_add_u32 m0, s89, 41984\n"
    "s_add_u32 s64, s92, 1114240\n"
    "buffer_load_dwordx4 v235, s[84:87], s64 offen lds\n"
      :
      : "{v[232:235]}"(B), "{s[80:83]}"(sX), "{s[84:87]}"(sW), "{s88}"(s88), "{s89}"(s89), "{s90}"(s90), "{s91}"(s91), "{s92}"(s92)
      : "s64", "scc", "memory");
    SBAR();
  }
  {
    const unsigned lane = lane_id();
    const unsigned rip = lane >> 3, pos = lane & 7, r = lane & 15, g = lane >> 4, q = r >> 2, p = lane & 3, sg = ((q >> 1) & 1) | ((g & 1) << 1);
    const unsigned b = (8 * g + q) * 128 + 16 * (p >> 1) + 8 * (p & 1);
    const unsigned a0 = 65536 + wid * 8192 + r * 128 + 16 * ((0 + g) ^ ((r >> 1) & 7)), a1 = 65536 + wid * 8192 + r * 128 + 16 * ((4 + g) ^ ((r >> 1) & 7));
    u32x8 A = {b + 32 * (0 ^ sg), b + 32 * (1 ^ sg), b + 32 * (2 ^ sg), b + 32 * (3 ^ sg), a0, a1, a0 + 65536, a1 + 65536};
    u32x8 B = {rip * 1024 + 16 * (pos ^ ((rip >> 1) & 7)), rip * 1024 + 16 * (pos ^ ((4 + (rip >> 1)) & 7)),
               rip * 8192 + 16 * (pos ^ (2 * ((rip >> 1) & 1))), rip * 8192 + 16 * (pos ^ (2 * (((rip >> 1) & 1) | 2))), lane * 16, 0u, 0u, 0u};
    const u32x4 C = {lane * 16, (unsigned)((int)(wid * 32 + r) - (int)(g * 4)), 0u, 0u};
    u32x4 sK = srd4(ws + WS_KF + (size_t)h * (128 * 4096), 128 * 4096), sV = srd4(ws + WS_VF + (size_t)h * (64 * 8192), 64 * 8192);
    const u32x4 sO = srd4(ws + WS_CF + (size_t)h * 4096, (16u << 20) - h * 4096);
    const unsigned ch = blockIdx.x * 2;
    const char* seg[2];
#pragma unroll
    for (int i = 0; i < 2; ++i) {
      const unsigned sgm = wid * 2 + i, c = ch + (sgm >= 3), k = sgm >= 3 ? sgm - 3 : sgm;
      seg[i] = sgm >= 6 ? ws + WS_G + (sgm - 6) * 4096 : (k == 0 ? (const char*)ssk + (size_t)c * 4096 : ws + (k == 1 ? WS_KVT : WS_QT) + (size_t)c * 4096);
    }
    const u32x4 sS = {rfl((unsigned)(uint64_t)seg[0]), rfl((unsigned)((uint64_t)seg[0] >> 32)), rfl((unsigned)(uint64_t)seg[1]), rfl((unsigned)((uint64_t)seg[1] >> 32))};
    SBAR();
    asm volatile(
    "gm_k2_code:\n"
    "s_nop 7\n"
    "s_waitcnt vmcnt(24)\n"
    "s_barrier\n"
    "ds_read_b128 v[128:131], v228 offset:0\n"
    "ds_read_b128 v[132:135], v228 offset:2048\n"
    "ds_read_b128 v[136:139], v228 offset:4096\n"
    "ds_read_b128 v[140:143], v228 offset:6144\n"
    "ds_read_b64_tr_b16 v[192:193], v224 offset:0\n"
    "ds_read_b64_tr_b16 v[194:195], v224 offset:512\n"
    "ds_read_b64_tr_b16 v[196:197], v225 offset:0\n"
    "ds_read_b64_tr_b16 v[198:199], v225 offset:512\n"
    "ds_read_b64_tr_b16 v[200:201], v226 offset:0\n"
    "ds_read_b64_tr_b16 v[202:203], v226 offset:512\n"
    "ds_read_b64_tr_b16 v[204:205], v227 offset:0\n"
    "ds_read_b64_tr_b16 v[206:207], v227 offset:512\n"
    "ds_read_b64_tr_b16 v[208:209], v224 offset:8192\n"
    "ds_read_b64_tr_b16 v[210:211], v224 offset:8704\n"
    "ds_read_b64_tr_b16 v[212:213], v225 offset:8192\n"
    "ds_read_b64_tr_b16 v[214:215], v225 offset:8704\n"
    "s_waitcnt lgkmcnt(10)\n"
    "v_mfma_f32_16x16x32_bf16 v[0:3], v[192:195], v[128:131], 0\n"
    "s_waitcnt lgkmcnt(8)\n"
    "ds_read_b64_tr_b16 v[216:217], v226 offset:8192\n"
    "v_mfma_f32_16x16x32_bf16 v[4:7], v[192:195], v[132:135], 0\n"
    "ds_read_b64_tr_b16 v[218:219], v226 offset:8704\n"
    "v_mfma_f32_16x16x32_bf16 v[8:11], v[192:195], v[136:139], 0\n"
    "v_mfma_f32_16x16x32_bf16 v[12:15], v[192:195], v[140:143], 0\n"
    "v_mfma_f32_16x16x32_bf16 v[16:19], v[196:199], v[128:131], 0\n"
    "s_waitcnt lgkmcnt(8)\n"
    "ds_read_b64_tr_b16 v[220:221], v227 offset:8192\n"
    "v_mfma_f32_16x16x32_bf16 v[20:23], v[196:199], v[132:135], 0\n"
    "ds_read_b64_tr_b16 v[222:223], v227 offset:8704\n"
    "v_mfma_f32_16x16x32_bf16 v[24:27], v[196:199], v[136:139], 0\n"
    "v_mfma_f32_16x16x32_bf16 v[28:31], v[196:199], v[140:143], 0\n"
    "v_mfma_f32_16x16x32_bf16 v[32:35], v[200:203], v[128:131], 0\n"
    "s_waitcnt lgkmcnt(8)\n"
    "ds_read_b64_tr_b16 v[192:193], v224 offset:4096\n"
    "v_mfma_f32_16x16x32_bf16 v[36:39], v[200:203], v[132:135], 0\n"
    "ds_read_b64_tr_b16 v[194:195], v224 offset:4608\n"
    "v_mfma_f32_16x16x32_bf16 v[40:43], v[200:203], v[136:139], 0\n"
    "ds_read_b128 v[144:147], v229 offset:0\n"
    "v_mfma_f32_16x16x32_bf16 v[44:47], v[200:203], v[140:143], 0\n"
    "ds_read_b128 v[148:151], v229 offset:2048\n"
    "v_mfma_f32_16x16x32_bf16 v[48:51], v[204:207], v[128:131], 0\n"
    "s_waitcnt lgkmcnt(10)\n"
    "ds_read_b64_tr_b16 v[196:197], v225 offset:4096\n"
    "v_mfma_f32_16x16x32_bf16 v[52:55], v[204:207], v[132:135], 0\n"
    "ds_read_b64_tr_b16 v[198:199], v225 offset:4608\n"
    "v_mfma_f32_16x16x32_bf16 v[56:59], v[204:207], v[136:139], 0\n"
    "ds_read_b128 v[152:155], v229 offset:4096\n"
    "v_mfma_f32_16x16x32_bf16 v[60:63], v[204:207], v[140:143], 0\n"
    "ds_read_b128 v[156:159], v229 offset:6144\n"
    "v_mfma_f32_16x16x32_bf16 v[64:67], v[208:211], v[128:131], 0\n"
    "s_waitcnt lgkmcnt(12)\n"
    "ds_read_b64_tr_b16 v[200:201], v226 offset:4096\n"
    "v_mfma_f32_16x16x32_bf16 v[68:71], v[208:211], v[132:135], 0\n"
    "ds_read_b64_tr_b16 v[202:203], v226 offset:4608\n"
    "v_mfma_f32_16x16x32_bf16 v[72:75], v[208:211], v[136:139], 0\n"
    "v_mfma_f32_16x16x32_bf16 v[76:79], v[208:211], v[140:143], 0\n"
    "v_mfma_f32_16x16x32_bf16 v[80:83], v[212:215], v[128:131], 0\n"
    "s_waitcnt lgkmcnt(12)\n"
    "ds_read_b64_tr_b16 v[204:205], v227 offset:4096\n"
    "v_mfma_f32_16x16x32_bf16 v[84:87], v[212:215], v[132:135], 0\n"
    "ds_read_b64_tr_b16 v[206:207], v227 offset:4608\n"
    "v_mfma_f32_16x16x32_bf16 v[88:91], v[212:215], v[136:139], 0\n"
    "v_mfma_f32_16x16x32_bf16 v[92:95], v[212:215], v[140:143], 0\n"
    "v_mfma_f32_16x16x32_bf16 v[96:99], v[216:219], v[128:131], 0\n"
    "s_waitcnt lgkmcnt(12)\n"
    "ds_read_b64_tr_b16 v[208:209], v224 offset:12288\n"
    "v_mfma_f32_16x16x32_bf16 v[100:103], v[216:219], v[132:135], 0\n"
    "ds_read_b64_tr_b16 v[210:211], v224 offset:12800\n"
    "v_mfma_f32_16x16x32_bf16 v[104:107], v[216:219], v[136:139], 0\n"
    "v_mfma_f32_16x16x32_bf16 v[108:111], v[216:219], v[140:143], 0\n"
    "v_mfma_f32_16x16x32_bf16 v[112:115], v[220:223], v[128:131], 0\n"
    "s_waitcnt lgkmcnt(6)\n"
    "ds_read_b64_tr_b16 v[212:213], v225 offset:12288\n"
    "v_mfma_f32_16x16x32_bf16 v[116:119], v[220:223], v[132:135], 0\n"
    "ds_read_b64_tr_b16 v[214:215], v225 offset:12800\n"
    "v_mfma_f32_16x16x32_bf16 v[120:123], v[220:223], v[136:139], 0\n"
    "v_mfma_f32_16x16x32_bf16 v[124:127], v[220:223], v[140:143], 0\n"
    "v_mfma_f32_16x16x32_bf16 v[0:3], v[192:195], v[144:147], v[0:3]\n"
    "ds_read_b64_tr_b16 v[216:217], v226 offset:12288\n"
    "v_mfma_f32_16x16x32_bf16 v[4:7], v[192:195], v[148:151], v[4:7]\n"
    "ds_read_b64_tr_b16 v[218:219], v226 offset:12800\n"
    "v_mfma_f32_16x16x32_bf16 v[8:11], v[192:195], v[152:155], v[8:11]\n"
    "v_mfma_f32_16x16x32_bf16 v[12:15], v[192:195], v[156:159], v[12:15]\n"
    "v_mfma_f32_16x16x32_bf16 v[16:19], v[196:199], v[144:147], v[16:19]\n"
    "s_waitcnt lgkmcnt(8)\n"
    "ds_read_b64_tr_b16 v[220:221], v227 offset:12288\n"
    "v_mfma_f32_16x16x32_bf16 v[20:23], v[196:199], v[148:151], v[20:23]\n"
    "ds_read_b64_tr_b16 v[222:223], v227 offset:12800\n"
    "v_mfma_f32_16x16x32_bf16 v[24:27], v[196:199], v[152:155], v[24:27]\n"
    "v_mfma_f32_16x16x32_bf16 v[28:31], v[196:199], v[156:159], v[28:31]\n"
    "v_mfma_f32_16x16x32_bf16 v[32:35], v[200:203], v[144:147], v[32:35]\n"
    "s_waitcnt vmcnt(12)\n"
    "s_barrier\n"
    "s_waitcnt lgkmcnt(8)\n"
    "ds_read_b64_tr_b16 v[192:193], v224 offset:16384\n"
    "v_mfma_f32_16x16x32_bf16 v[36:39], v[200:203], v[148:151], v[36:39]\n"
    "s_add_u32 m0, s89, 49152\n"
    "s_add_u32 s64, s92, 1572864\n"
    "ds_read_b64_tr_b16 v[194:195], v224 offset:16896\n"
    "v_mfma_f32_16x16x32_bf16 v[40:43], v[200:203], v[152:155], v[40:43]\n"
    "buffer_load_dwordx4 v234, s[84:87], s64 offen lds\n"
    "v_mfma_f32_16x16x32_bf16 v[44:47], v[200:203], v[156:159], v[44:47]\n"
    "ds_read_b128 v[160:163], v228 offset:32768\n"
    "v_mfma_f32_16x16x32_bf16 v[48:51], v[204:207], v[144:147], v[48:51]\n"
    "s_waitcnt lgkmcnt(9)\n"
    "ds_read_b64_tr_b16 v[196:197], v225 offset:16384\n"
    "v_mfma_f32_16x16x32_bf16 v[52:55], v[204:207], v[148:151], v[52:55]\n"
    "s_add_u32 m0, s89, 50176\n"
    "s_add_u32 s64, s92, 1638400\n"
    "ds_read_b64_tr_b16 v[198:199], v225 offset:16896\n"
    "v_mfma_f32_16x16x32_bf16 v[56:59], v[204:207], v[152:155], v[56:59]\n"
    "buffer_load_dwordx4 v235, s[84:87], s64 offen lds\n"
    "v_mfma_f32_16x16x32_bf16 v[60:63], v[204:207], v[156:159], v[60:63]\n"
    "ds_read_b128 v[164:167], v228 offset:34816\n"
    "v_mfma_f32_16x16x32_bf16 v[64:67], v[208:211], v[144:147], v[64:67]\n"
    "s_waitcnt lgkmcnt(10)\n"
    "ds_read_b64_tr_b16 v[200:201], v226 offset:16384\n"
    "v_mfma_f32_16x16x32_bf16 v[68:71], v[208:211], v[148:151], v[68:71]\n"
    "s_add_u32 m0, s89, 57344\n"
    "s_add_u32 s64, s92, 1572992\n"
    "ds_read_b64_tr_b16 v[202:203], v226 offset:16896\n"
    "v_mfma_f32_16x16x32_bf16 v[72:75], v[208:211], v[152:155], v[72:75]\n"
    "buffer_load_dwordx4 v234, s[84:87], s64 offen lds\n"
    "v_mfma_f32_16x16x32_bf16 v[76:79], v[208:211], v[156:159], v[76:79]\n"
    "ds_read_b128 v[168:171], v228 offset:36864\n"
    "v_mfma_f32_16x16x32_bf16 v[80:83], v[212:215], v[144:147], v[80:83]\n"
    "s_waitcnt lgkmcnt(11)\n"
    "ds_read_b64_tr_b16 v[204:205], v227 offset:16384\n"
    "v_mfma_f32_16x16x32_bf16 v[84:87], v[212:215], v[148:151], v[84:87]\n"
    "s_add_u32 m0, s89, 58368\n"
    "s_add_u32 s64, s92, 1638528\n"
    "ds_read_b64_tr_b16 v[206:207], v227 offset:16896\n"
    "v_mfma_f32_16x16x32_bf16 v[88:91], v[212:215], v[152:155], v[88:91]\n"
    "buffer_load_dwordx4 v235, s[84:87], s64 offen lds\n"
    "v_mfma_f32_16x16x32_bf16 v[92:95], v[212:215], v[156:159], v[92:95]\n"
    "ds_read_b128 v[172:175], v228 offset:38912\n"
    "v_mfma_f32_16x16x32_bf16 v[96:99], v[216:219], v[144:147], v[96:99]\n"
    "s_waitcnt lgkmcnt(12)\n"
    "ds_read_b64_tr_b16 v[208:209], v224 offset:24576\n"
    "v_mfma_f32_16x16x32_bf16 v[100:103], v[216:219], v[148:151], v[100:103]\n"
    "s_add_u32 m0, s88, 65536\n"
    "s_add_u32 s64, s90, 384\n"
    "ds_read_b64_tr_b16 v[210:211], v224 offset:25088\n"
    "v_mfma_f32_16x16x32_bf16 v[104:107], v[216:219], v[152:155], v[104:107]\n"
    "buffer_load_dwordx4 v232, s[80:83], s64 offen lds\n"
    "v_mfma_f32_16x16x32_bf16 v[108:111], v[216:219], v[156:159], v[108:111]\n"
    "v_mfma_f32_16x16x32_bf16 v[112:115], v[220:223], v[144:147], v[112:115]\n"
    "s_waitcnt lgkmcnt(2)\n"
    "ds_read_b64_tr_b16 v[212:213], v225 offset:24576\n"
    "v_mfma_f32_16x16x32_bf16 v[116:119], v[220:223], v[148:151], v[116:119]\n"
    "s_add_u32 m0, s88, 66560\n"
    "s_add_u32 s64, s90, 8576\n"
    "ds_read_b64_tr_b16 v[214:215], v225 offset:25088\n"
    "v_mfma_f32_16x16x32_bf16 v[120:123], v[220:223], v[152:155], v[120:123]\n"
    "buffer_load_dwordx4 v233, s[80:83], s64 offen lds\n"
    "v_mfma_f32_16x16x32_bf16 v[124:127], v[220:223], v[156:159], v[124:127]\n"
    "v_mfma_f32_16x16x32_bf16 v[0:3], v[192:195], v[160:163], v[0:3]\n"
    "ds_read_b64_tr_b16 v[216:217], v226 offset:24576\n"
    "v_mfma_f32_16x16x32_bf16 v[4:7], v[192:195], v[164:167], v[4:7]\n"
    "s_add_u32 m0, s88, 67584\n"
    "s_add_u32 s64, s90, 16768\n"
    "ds_read_b64_tr_b16 v[218:219], v226 offset:25088\n"
    "v_mfma_f32_16x16x32_bf16 v[8:11], v[192:195], v[168:171], v[8:11]\n"
    "buffer_load_dwordx4 v232, s[80:83], s64 offen lds\n"
    "v_mfma_f32_16x16x32_bf16 v[12:15], v[192:195], v[172:175], v[12:15]\n"
    "v_mfma_f32_16x16x32_bf16 v[16:19], v[196:199], v[160:163], v[16:19]\n"
    "ds_read_b64_tr_b16 v[220:221], v227 offset:24576\n"
    "v_mfma_f32_16x16x32_bf16 v[20:23], v[196:199], v[164:167], v[20:23]\n"
    "s_add_u32 m0, s88, 68608\n"
    "s_add_u32 s64, s90, 24960\n"
    "ds_read_b64_tr_b16 v[222:223], v227 offset:25088\n"
    "v_mfma_f32_16x16x32_bf16 v[24:27], v[196:199], v[168:171], v[24:27]\n"
    "buffer_load_dwordx4 v233, s[80:83], s64 offen lds\n"
    "v_mfma_f32_16x16x32_bf16 v[28:31], v[196:199], v[172:175], v[28:31]\n"
    "v_mfma_f32_16x16x32_bf16 v[32:35], v[200:203], v[160:163], v[32:35]\n"
    "ds_read_b64_tr_b16 v[192:193], v224 offset:20480\n"
    "v_mfma_f32_16x16x32_bf16 v[36:39], v[200:203], v[164:167], v[36:39]\n"
    "s_add_u32 m0, s88, 69632\n"
    "s_add_u32 s64, s91, 384\n"
    "ds_read_b64_tr_b16 v[194:195], v224 offset:20992\n"
    "v_mfma_f32_16x16x32_bf16 v[40:43], v[200:203], v[168:171], v[40:43]\n"
    "buffer_load_dwordx4 v232, s[80:83], s64 offen lds\n"
    "v_mfma_f32_16x16x32_bf16 v[44:47], v[200:203], v[172:175], v[44:47]\n"
    "ds_read_b128 v[176:179], v229 offset:32768\n"
    "v_mfma_f32_16x16x32_bf16 v[48:51], v[204:207], v[160:163], v[48:51]\n"
    "s_waitcnt lgkmcnt(9)\n"
    "ds_read_b64_tr_b16 v[196:197], v225 offset:20480\n"
    "v_mfma_f32_16x16x32_bf16 v[52:55], v[204:207], v[164:167], v[52:55]\n"
    "s_add_u32 m0, s88, 70656\n"
    "s_add_u32 s64, s91, 8576\n"
    "ds_read_b64_tr_b16 v[198:199], v225 offset:20992\n"
    "v_mfma_f32_16x16x32_bf16 v[56:59], v[204:207], v[168:171], v[56:59]\n"
    "buffer_load_dwordx4 v233, s[80:83], s64 offen lds\n"
    "v_mfma_f32_16x16x32_bf16 v[60:63], v[204:207], v[172:175], v[60:63]\n"
    "ds_read_b128 v[180:183], v229 offset:34816\n"
    "v_mfma_f32_16x16x32_bf16 v[64:67], v[208:211], v[160:163], v[64:67]\n"
    "s_waitcnt lgkmcnt(10)\n"
    "ds_read_b64_tr_b16 v[200:201], v226 offset:20480\n"
    "v_mfma_f32_16x16x32_bf16 v[68:71], v[208:211], v[164:167], v[68:71]\n"
    "s_add_u32 m0, s88, 71680\n"
    "s_add_u32 s64, s91, 16768\n"
    "ds_read_b64_tr_b16 v[202:203], v226 offset:20992\n"
    "v_mfma_f32_16x16x32_bf16 v[72:75], v[208:211], v[168:171], v[72:75]\n"
    "buffer_load_dwordx4 v232, s[80:83], s64 offen lds\n"
    "v_mfma_f32_16x16x32_bf16 v[76:79], v[208:211], v[172:175], v[76:79]\n"
    "ds_read_b128 v[184:187], v229 offset:36864\n"
    "v_mfma_f32_16x16x32_bf16 v[80:83], v[212:215], v[160:163], v[80:83]\n"
    "s_waitcnt lgkmcnt(11)\n"
    "ds_read_b64_tr_b16 v[204:205], v227 offset:20480\n"
    "v_mfma_f32_16x16x32_bf16 v[84:87], v[212:215], v[164:167], v[84:87]\n"
    "s_add_u32 m0, s88, 72704\n"
    "s_add_u32 s64, s91, 24960\n"
    "ds_read_b64_tr_b16 v[206:207], v227 offset:20992\n"
    "v_mfma_f32_16x16x32_bf16 v[88:91], v[212:215], v[168:171], v[88:91]\n"
    "buffer_load_dwordx4 v233, s[80:83], s64 offen lds\n"
    "v_mfma_f32_16x16x32_bf16 v[92:95], v[212:215], v[172:175], v[92:95]\n"
    "ds_read_b128 v[188:191], v229 offset:38912\n"
    "v_mfma_f32_16x16x32_bf16 v[96:99], v[216:219], v[160:163], v[96:99]\n"
    "s_waitcnt lgkmcnt(12)\n"
    "ds_read_b64_tr_b16 v[208:209], v224 offset:28672\n"
    "v_mfma_f32_16x16x32_bf16 v[100:103], v[216:219], v[164:167], v[100:103]\n"
    "ds_read_b64_tr_b16 v[210:211], v224 offset:29184\n"
    "v_mfma_f32_16x16x32_bf16 v[104:107], v[216:219], v[168:171], v[104:107]\n"
    "v_mfma_f32_16x16x32_bf16 v[108:111], v[216:219], v[172:175], v[108:111]\n"
    "v_mfma_f32_16x16x32_bf16 v[112:115], v[220:223], v[160:163], v[112:115]\n"
    "s_waitcnt lgkmcnt(2)\n"
    "ds_read_b64_tr_b16 v[212:213], v225 offset:28672\n"
    "v_mfma_f32_16x16x32_bf16 v[116:119], v[220:223], v[164:167], v[116:119]\n"
    "ds_read_b64_tr_b16 v[214:215], v225 offset:29184\n"
    "v_mfma_f32_16x16x32_bf16 v[120:123], v[220:223], v[168:171], v[120:123]\n"
    "v_mfma_f32_16x16x32_bf16 v[124:127], v[220:223], v[172:175], v[124:127]\n"
    "v_mfma_f32_16x16x32_bf16 v[0:3], v[192:195], v[176:179], v[0:3]\n"
    "ds_read_b64_tr_b16 v[216:217], v226 offset:28672\n"
    "v_mfma_f32_16x16x32_bf16 v[4:7], v[192:195], v[180:183], v[4:7]\n"
    "ds_read_b64_tr_b16 v[218:219], v226 offset:29184\n"
    "v_mfma_f32_16x16x32_bf16 v[8:11], v[192:195], v[184:187], v[8:11]\n"
    "v_mfma_f32_16x16x32_bf16 v[12:15], v[192:195], v[188:191], v[12:15]\n"
    "v_mfma_f32_16x16x32_bf16 v[16:19], v[196:199], v[176:179], v[16:19]\n"
    "ds_read_b64_tr_b16 v[220:221], v227 offset:28672\n"
    "v_mfma_f32_16x16x32_bf16 v[20:23], v[196:199], v[180:183], v[20:23]\n"
    "ds_read_b64_tr_b16 v[222:223], v227 offset:29184\n"
    "v_mfma_f32_16x16x32_bf16 v[24:27], v[196:199], v[184:187], v[24:27]\n"
    "v_mfma_f32_16x16x32_bf16 v[28:31], v[196:199], v[188:191], v[28:31]\n"
    "v_mfma_f32_16x16x32_bf16 v[32:35], v[200:203], v[176:179], v[32:35]\n"
    "s_waitcnt vmcnt(12)\n"
    "s_barrier\n"
    "ds_read_b64_tr_b16 v[192:193], v224 offset:32768\n"
    "v_mfma_f32_16x16x32_bf16 v[36:39], v[200:203], v[180:183], v[36:39]\n"
    "s_add_u32 m0, s89, 0\n"
    "s_add_u32 s64, s92, 2097152\n"
    "ds_read_b64_tr_b16 v[194:195], v224 offset:33280\n"
    "v_mfma_f32_16x16x32_bf16 v[40:43], v[200:203], v[184:187], v[40:43]\n"
    "buffer_load_dwordx4 v234, s[84:87], s64 offen lds\n"
    "v_mfma_f32_16x16x32_bf16 v[44:47], v[200:203], v[188:191], v[44:47]\n"
    "ds_read_b128 v[128:131], v230 offset:0\n"
    "v_mfma_f32_16x16x32_bf16 v[48:51], v[204:207], v[176:179], v[48:51]\n"
    "s_waitcnt lgkmcnt(9)\n"
    "ds_read_b64_tr_b16 v[196:197], v225 offset:32768\n"
    "v_mfma_f32_16x16x32_bf16 v[52:55], v[204:207], v[180:183], v[52:55]\n"
    "s_add_u32 m0, s89, 1024\n"
    "s_add_u32 s64, s92, 2162688\n"
    "ds_read_b64_tr_b16 v[198:199], v225 offset:33280\n"
    "v_mfma_f32_16x16x32_bf16 v[56:59], v[204:207], v[184:187], v[56:59]\n"
    "buffer_load_dwordx4 v235, s[84:87], s64 offen lds\n"
    "v_mfma_f32_16x16x32_bf16 v[60:63], v[204:207], v[188:191], v[60:63]\n"
    "ds_read_b128 v[132:135], v230 offset:2048\n"
    "v_mfma_f32_16x16x32_bf16 v[64:67], v[208:211], v[176:179], v[64:67]\n"
    "s_waitcnt lgkmcnt(10)\n"
    "ds_read_b64_tr_b16 v[200:201], v226 offset:32768\n"
    "v_mfma_f32_16x16x32_bf16 v[68:71], v[208:211], v[180:183], v[68:71]\n"
    "s_add_u32 m0, s89, 8192\n"
    "s_add_u32 s64, s92, 2097280\n"
    "ds_read_b64_tr_b16 v[202:203], v226 offset:33280\n"
    "v_mfma_f32_16x16x32_bf16 v[72:75], v[208:211], v[184:187], v[72:75]\n"
    "buffer_load_dwordx4 v234, s[84:87], s64 offen lds\n"
    "v_mfma_f32_16x16x32_bf16 v[76:79], v[208:211], v[188:191], v[76:79]\n"
    "ds_read_b128 v[136:139], v230 offset:4096\n"
    "v_mfma_f32_16x16x32_bf16 v[80:83], v[212:215], v[176:179], v[80:83]\n"
    "s_waitcnt lgkmcnt(11)\n"
    "ds_read_b64_tr_b16 v[204:205], v227 offset:32768\n"
    "v_mfma_f32_16x16x32_bf16 v[84:87], v[212:215], v[180:183], v[84:87]\n"
    "s_add_u32 m0, s89, 9216\n"
    "s_add_u32 s64, s92, 2162816\n"
    "ds_read_b64_tr_b16 v[206:207], v227 offset:33280\n"
    "v_mfma_f32_16x16x32_bf16 v[88:91], v[212:215], v[184:187], v[88:91]\n"
    "buffer_load_dwordx4 v235, s[84:87], s64 offen lds\n"
    "v_mfma_f32_16x16x32_bf16 v[92:95], v[212:215], v[188:191], v[92:95]\n"
    "ds_read_b128 v[140:143], v230 offset:6144\n"
    "v_mfma_f32_16x16x32_bf16 v[96:99], v[216:219], v[176:179], v[96:99]\n"
    "s_waitcnt lgkmcnt(12)\n"
    "ds_read_b64_tr_b16 v[208:209], v224 offset:40960\n"
    "v_mfma_f32_16x16x32_bf16 v[100:103], v[216:219], v[180:183], v[100:103]\n"
    "s_add_u32 m0, s88, 98304\n"
    "s_add_u32 s64, s90, 512\n"
    "ds_read_b64_tr_b16 v[210:211], v224 offset:41472\n"
    "v_mfma_f32_16x16x32_bf16 v[104:107], v[216:219], v[184:187], v[104:107]\n"
    "buffer_load_dwordx4 v232, s[80:83], s64 offen lds\n"
    "v_mfma_f32_16x16x32_bf16 v[108:111], v[216:219], v[188:191], v[108:111]\n"
    "v_mfma_f32_16x16x32_bf16 v[112:115], v[220:223], v[176:179], v[112:115]\n"
    "s_waitcnt lgkmcnt(2)\n"
    "ds_read_b64_tr_b16 v[212:213], v225 offset:40960\n"
    "v_mfma_f32_16x16x32_bf16 v[116:119], v[220:223], v[180:183], v[116:119]\n"
    "s_add_u32 m0, s88, 99328\n"
    "s_add_u32 s64, s90, 8704\n"
    "ds_read_b64_tr_b16 v[214:215], v225 offset:41472\n"
    "v_mfma_f32_16x16x32_bf16 v[120:123], v[220:223], v[184:187], v[120:123]\n"
    "buffer_load_dwordx4 v233, s[80:83], s64 offen lds\n"
    "v_mfma_f32_16x16x32_bf16 v[124:127], v[220:223], v[188:191], v[124:127]\n"
    "v_mfma_f32_16x16x32_bf16 v[0:3], v[192:195], v[128:131], v[0:3]\n"
    "ds_read_b64_tr_b16 v[216:217], v226 offset:40960\n"
    "v_mfma_f32_16x16x32_bf16 v[4:7], v[192:195], v[132:135], v[4:7]\n"
    "s_add_u32 m0, s88, 100352\n"
    "s_add_u32 s64, s90, 16896\n"
    "ds_read_b64_tr_b16 v[218:219], v226 offset:41472\n"
    "v_mfma_f32_16x16x32_bf16 v[8:11], v[192:195], v[136:139], v[8:11]\n"
    "buffer_load_dwordx4 v232, s[80:83], s64 offen lds\n"
    "v_mfma_f32_16x16x32_bf16 v[12:15], v[192:195], v[140:143], v[12:15]\n"
    "v_mfma_f32_16x16x32_bf16 v[16:19], v[196:199], v[128:131], v[16:19]\n"
    "ds_read_b64_tr_b16 v[220:221], v227 offset:40960\n"
    "v_mfma_f32_16x16x32_bf16 v[20:23], v[196:199], v[132:135], v[20:23]\n"
    "s_add_u32 m0, s88, 101376\n"
    "s_add_u32 s64, s90, 25088\n"
    "ds_read_b64_tr_b16 v[222:223], v227 offset:41472\n"
    "v_mfma_f32_16x16x32_bf16 v[24:27], v[196:199], v[136:139], v[24:27]\n"
    "buffer_load_dwordx4 v233, s[80:83], s64 offen lds\n"
    "v_mfma_f32_16x16x32_bf16 v[28:31], v[196:199], v[140:143], v[28:31]\n"
    "v_mfma_f32_16x16x32_bf16 v[32:35], v[200:203], v[128:131], v[32:35]\n"
    "ds_read_b64_tr_b16 v[192:193], v224 offset:36864\n"
    "v_mfma_f32_16x16x32_bf16 v[36:39], v[200:203], v[132:135], v[36:39]\n"
    "s_add_u32 m0, s88, 102400\n"
    "s_add_u32 s64, s91, 512\n"
    "ds_read_b64_tr_b16 v[194:195], v224 offset:37376\n"
    "v_mfma_f32_16x16x32_bf16 v[40:43], v[200:203], v[136:139], v[40:43]\n"
    "buffer_load_dwordx4 v232, s[80:83], s64 offen lds\n"
    "v_mfma_f32_16x16x32_bf16 v[44:47], v[200:203], v[140:143], v[44:47]\n"
    "ds_read_b128 v[144:147], v231 offset:0\n"
    "v_mfma_f32_16x16x32_bf16 v[48:51], v[204:207], v[128:131], v[48:51]\n"
    "s_waitcnt lgkmcnt(9)\n"
    "ds_read_b64_tr_b16 v[196:197], v225 offset:36864\n"
    "v_mfma_f32_16x16x32_bf16 v[52:55], v[204:207], v[132:135], v[52:55]\n"
    "s_add_u32 m0, s88, 103424\n"
    "s_add_u32 s64, s91, 8704\n"
    "ds_read_b64_tr_b16 v[198:199], v225 offset:37376\n"
    "v_mfma_f32_16x16x32_bf16 v[56:59], v[204:207], v[136:139], v[56:59]\n"
    "buffer_load_dwordx4 v233, s[80:83], s64 offen lds\n"
    "v_mfma_f32_16x16x32_bf16 v[60:63], v[204:207], v[140:143], v[60:63]\n"
    "ds_read_b128 v[148:151], v231 offset:2048\n"
    "v_mfma_f32_16x16x32_bf16 v[64:67], v[208:211], v[128:131], v[64:67]\n"
    "s_waitcnt lgkmcnt(10)\n"
    "ds_read_b64_tr_b16 v[200:201], v226 offset:36864\n"
    "v_mfma_f32_16x16x32_bf16 v[68:71], v[208:211], v[132:135], v[68:71]\n"
    "s_add_u32 m0, s88, 104448\n"
    "s_add_u32 s64, s91, 16896\n"
    "ds_read_b64_tr_b16 v[202:203], v226 offset:37376\n"
    "v_mfma_f32_16x16x32_bf16 v[72:75], v[208:211], v[136:139], v[72:75]\n"
    "buffer_load_dwordx4 v232, s[80:83], s64 offen lds\n"
    "v_mfma_f32_16x16x32_bf16 v[76:79], v[208:211], v[140:143], v[76:79]\n"
    "ds_read_b128 v[152:155], v231 offset:4096\n"
    "v_mfma_f32_16x16x32_bf16 v[80:83], v[212:215], v[128:131], v[80:83]\n"
    "s_waitcnt lgkmcnt(11)\n"
    "ds_read_b64_tr_b16 v[204:205], v227 offset:36864\n"
    "v_mfma_f32_16x16x32_bf16 v[84:87], v[212:215], v[132:135], v[84:87]\n"
    "s_add_u32 m0, s88, 105472\n"
    "s_add_u32 s64, s91, 25088\n"
    "ds_read_b64_tr_b16 v[206:207], v227 offset:37376\n"
    "v_mfma_f32_16x16x32_bf16 v[88:91], v[212:215], v[136:139], v[88:91]\n"
    "buffer_load_dwordx4 v233, s[80:83], s64 offen lds\n"
    "v_mfma_f32_16x16x32_bf16 v[92:95], v[212:215], v[140:143], v[92:95]\n"
    "ds_read_b128 v[156:159], v231 offset:6144\n"
    "v_mfma_f32_16x16x32_bf16 v[96:99], v[216:219], v[128:131], v[96:99]\n"
    "s_waitcnt lgkmcnt(12)\n"
    "ds_read_b64_tr_b16 v[208:209], v224 offset:45056\n"
    "v_mfma_f32_16x16x32_bf16 v[100:103], v[216:219], v[132:135], v[100:103]\n"
    "ds_read_b64_tr_b16 v[210:211], v224 offset:45568\n"
    "v_mfma_f32_16x16x32_bf16 v[104:107], v[216:219], v[136:139], v[104:107]\n"
    "v_mfma_f32_16x16x32_bf16 v[108:111], v[216:219], v[140:143], v[108:111]\n"
    "v_mfma_f32_16x16x32_bf16 v[112:115], v[220:223], v[128:131], v[112:115]\n"
    "s_waitcnt lgkmcnt(2)\n"
    "ds_read_b64_tr_b16 v[212:213], v225 offset:45056\n"
    "v_mfma_f32_16x16x32_bf16 v[116:119], v[220:223], v[132:135], v[116:119]\n"
    "ds_read_b64_tr_b16 v[214:215], v225 offset:45568\n"
    "v_mfma_f32_16x16x32_bf16 v[120:123], v[220:223], v[136:139], v[120:123]\n"
    "v_mfma_f32_16x16x32_bf16 v[124:127], v[220:223], v[140:143], v[124:127]\n"
    "v_mfma_f32_16x16x32_bf16 v[0:3], v[192:195], v[144:147], v[0:3]\n"
    "ds_read_b64_tr_b16 v[216:217], v226 offset:45056\n"
    "v_mfma_f32_16x16x32_bf16 v[4:7], v[192:195], v[148:151], v[4:7]\n"
    "ds_read_b64_tr_b16 v[218:219], v226 offset:45568\n"
    "v_mfma_f32_16x16x32_bf16 v[8:11], v[192:195], v[152:155], v[8:11]\n"
    "v_mfma_f32_16x16x32_bf16 v[12:15], v[192:195], v[156:159], v[12:15]\n"
    "v_mfma_f32_16x16x32_bf16 v[16:19], v[196:199], v[144:147], v[16:19]\n"
    "ds_read_b64_tr_b16 v[220:221], v227 offset:45056\n"
    "v_mfma_f32_16x16x32_bf16 v[20:23], v[196:199], v[148:151], v[20:23]\n"
    "ds_read_b64_tr_b16 v[222:223], v227 offset:45568\n"
    "v_mfma_f32_16x16x32_bf16 v[24:27], v[196:199], v[152:155], v[24:27]\n"
    "v_mfma_f32_16x16x32_bf16 v[28:31], v[196:199], v[156:159], v[28:31]\n"
    "v_mfma_f32_16x16x32_bf16 v[32:35], v[200:203], v[144:147], v[32:35]\n"
    "s_waitcnt vmcnt(12)\n"
    "s_barrier\n"
    "ds_read_b64_tr_b16 v[192:193], v224 offset:49152\n"
    "v_mfma_f32_16x16x32_bf16 v[36:39], v[200:203], v[148:151], v[36:39]\n"
    "s_add_u32 m0, s89, 16384\n"
    "s_add_u32 s64, s92, 2621440\n"
    "ds_read_b64_tr_b16 v[194:195], v224 offset:49664\n"
    "v_mfma_f32_16x16x32_bf16 v[40:43], v[200:203], v[152:155], v[40:43]\n"
    "buffer_load_dwordx4 v234, s[84:87], s64 offen lds\n"
    "v_mfma_f32_16x16x32_bf16 v[44:47], v[200:203], v[156:159], v[44:47]\n"
    "ds_read_b128 v[160:163], v228 offset:0\n"
    "v_mfma_f32_16x16x32_bf16 v[48:51], v[204:207], v[144:147], v[48:51]\n"
    "s_waitcnt lgkmcnt(9)\n"
    "ds_read_b64_tr_b16 v[196:197], v225 offset:49152\n"
    "v_mfma_f32_16x16x32_bf16 v[52:55], v[204:207], v[148:151], v[52:55]\n"
    "s_add_u32 m0, s89, 17408\n"
    "s_add_u32 s64, s92, 2686976\n"
    "ds_read_b64_tr_b16 v[198:199], v225 offset:49664\n"
    "v_mfma_f32_16x16x32_bf16 v[56:59], v[204:207], v[152:155], v[56:59]\n"
    "buffer_load_dwordx4 v235, s[84:87], s64 offen lds\n"
    "v_mfma_f32_16x16x32_bf16 v[60:63], v[204:207], v[156:159], v[60:63]\n"
    "ds_read_b128 v[164:167], v228 offset:2048\n"
    "v_mfma_f32_16x16x32_bf16 v[64:67], v[208:211], v[144:147], v[64:67]\n"
    "s_waitcnt lgkmcnt(10)\n"
    "ds_read_b64_tr_b16 v[200:201], v226 offset:49152\n"
    "v_mfma_f32_16x16x32_bf16 v[68:71], v[208:211], v[148:151], v[68:71]\n"
    "s_add_u32 m0, s89, 24576\n"
    "s_add_u32 s64, s92, 2621568\n"
    "ds_read_b64_tr_b16 v[202:203], v226 offset:49664\n"
    "v_mfma_f32_16x16x32_bf16 v[72:75], v[208:211], v[152:155], v[72:75]\n"
    "buffer_load_dwordx4 v234, s[84:87], s64 offen lds\n"
    "v_mfma_f32_16x16x32_bf16 v[76:79], v[208:211], v[156:159], v[76:79]\n"
    "ds_read_b128 v[168:171], v228 offset:4096\n"
    "v_mfma_f32_16x16x32_bf16 v[80:83], v[212:215], v[144:147], v[80:83]\n"
    "s_waitcnt lgkmcnt(11)\n"
    "ds_read_b64_tr_b16 v[204:205], v227 offset:49152\n"
    "v_mfma_f32_16x16x32_bf16 v[84:87], v[212:215], v[148:151], v[84:87]\n"
    "s_add_u32 m0, s89, 25600\n"
    "s_add_u32 s64, s92, 2687104\n"
    "ds_read_b64_tr_b16 v[206:207], v227 offset:49664\n"
    "v_mfma_f32_16x16x32_bf16 v[88:91], v[212:215], v[152:155], v[88:91]\n"
    "buffer_load_dwordx4 v235, s[84:87], s64 offen lds\n"
    "v_mfma_f32_16x16x32_bf16 v[92:95], v[212:215], v[156:159], v[92:95]\n"
    "ds_read_b128 v[172:175], v228 offset:6144\n"
    "v_mfma_f32_16x16x32_bf16 v[96:99], v[216:219], v[144:147], v[96:99]\n"
    "s_waitcnt lgkmcnt(12)\n"
    "ds_read_b64_tr_b16 v[208:209], v224 offset:57344\n"
    "v_mfma_f32_16x16x32_bf16 v[100:103], v[216:219], v[148:151], v[100:103]\n"
    "s_add_u32 m0, s88, 131072\n"
    "s_add_u32 s64, s90, 640\n"
    "ds_read_b64_tr_b16 v[210:211], v224 offset:57856\n"
    "v_mfma_f32_16x16x32_bf16 v[104:107], v[216:219], v[152:155], v[104:107]\n"
    "buffer_load_dwordx4 v232, s[80:83], s64 offen lds\n"
    "v_mfma_f32_16x16x32_bf16 v[108:111], v[216:219], v[156:159], v[108:111]\n"
    "v_mfma_f32_16x16x32_bf16 v[112:115], v[220:223], v[144:147], v[112:115]\n"
    "s_waitcnt lgkmcnt(2)\n"
    "ds_read_b64_tr_b16 v[212:213], v225 offset:57344\n"
    "v_mfma_f32_16x16x32_bf16 v[116:119], v[220:223], v[148:151], v[116:119]\n"
    "s_add_u32 m0, s88, 132096\n"
    "s_add_u32 s64, s90, 8832\n"
    "ds_read_b64_tr_b16 v[214:215], v225 offset:57856\n"
    "v_mfma_f32_16x16x32_bf16 v[120:123], v[220:223], v[152:155], v[120:123]\n"
    "buffer_load_dwordx4 v233, s[80:83], s64 offen lds\n"
    "v_mfma_f32_16x16x32_bf16 v[124:127], v[220:223], v[156:159], v[124:127]\n"
    "v_mfma_f32_16x16x32_bf16 v[0:3], v[192:195], v[160:163], v[0:3]\n"
    "ds_read_b64_tr_b16 v[216:217], v226 offset:57344\n"
    "v_mfma_f32_16x16x32_bf16 v[4:7], v[192:195], v[164:167], v[4:7]\n"
    "s_add_u32 m0, s88, 133120\n"
    "s_add_u32 s64, s90, 17024\n"
    "ds_read_b64_tr_b16 v[218:219], v226 offset:57856\n"
    "v_mfma_f32_16x16x32_bf16 v[8:11], v[192:195], v[168:171], v[8:11]\n"
    "buffer_load_dwordx4 v232, s[80:83], s64 offen lds\n"
    "v_mfma_f32_16x16x32_bf16 v[12:15], v[192:195], v[172:175], v[12:15]\n"
    "v_mfma_f32_16x16x32_bf16 v[16:19], v[196:199], v[160:163], v[16:19]\n"
    "ds_read_b64_tr_b16 v[220:221], v227 offset:57344\n"
    "v_mfma_f32_16x16x32_bf16 v[20:23], v[196:199], v[164:167], v[20:23]\n"
    "s_add_u32 m0, s88, 134144\n"
    "s_add_u32 s64, s90, 25216\n"
    "ds_read_b64_tr_b16 v[222:223], v227 offset:57856\n"
    "v_mfma_f32_16x16x32_bf16 v[24:27], v[196:199], v[168:171], v[24:27]\n"
    "buffer_load_dwordx4 v233, s[80:83], s64 offen lds\n"
    "v_mfma_f32_16x16x32_bf16 v[28:31], v[196:199], v[172:175], v[28:31]\n"
    "v_mfma_f32_16x16x32_bf16 v[32:35], v[200:203], v[160:163], v[32:35]\n"
    "ds_read_b64_tr_b16 v[192:193], v224 offset:53248\n"
    "v_mfma_f32_16x16x32_bf16 v[36:39], v[200:203], v[164:167], v[36:39]\n"
    "s_add_u32 m0, s88, 135168\n"
    "s_add_u32 s64, s91, 640\n"
    "ds_read_b64_tr_b16 v[194:195], v224 offset:53760\n"
    "v_mfma_f32_16x16x32_bf16 v[40:43], v[200:203], v[168:171], v[40:43]\n"
    "buffer_load_dwordx4 v232, s[80:83], s64 offen lds\n"
    "v_mfma_f32_16x16x32_bf16 v[44:47], v[200:203], v[172:175], v[44:47]\n"
    "ds_read_b128 v[176:179], v229 offset:0\n"
    "v_mfma_f32_16x16x32_bf16 v[48:51], v[204:207], v[160:163], v[48:51]\n"
    "s_waitcnt lgkmcnt(9)\n"
    "ds_read_b64_tr_b16 v[196:197], v225 offset:53248\n"
    "v_mfma_f32_16x16x32_bf16 v[52:55], v[204:207], v[164:167], v[52:55]\n"
    "s_add_u32 m0, s88, 136192\n"
    "s_add_u32 s64, s91, 8832\n"
    "ds_read_b64_tr_b16 v[198:199], v225 offset:53760\n"
    "v_mfma_f32_16x16x32_bf16 v[56:59], v[204:207], v[168:171], v[56:59]\n"
    "buffer_load_dwordx4 v233, s[80:83], s64 offen lds\n"
    "v_mfma_f32_16x16x32_bf16 v[60:63], v[204:207], v[172:175], v[60:63]\n"
    "ds_read_b128 v[180:183], v229 offset:2048\n"
    "v_mfma_f32_16x16x32_bf16 v[64:67], v[208:211], v[160:163], v[64:67]\n"
    "s_waitcnt lgkmcnt(10)\n"
    "ds_read_b64_tr_b16 v[200:201], v226 offset:53248\n"
    "v_mfma_f32_16x16x32_bf16 v[68:71], v[208:211], v[164:167], v[68:71]\n"
    "s_add_u32 m0, s88, 137216\n"
    "s_add_u32 s64, s91, 17024\n"
    "ds_read_b64_tr_b16 v[202:203], v226 offset:53760\n"
    "v_mfma_f32_16x16x32_bf16 v[72:75], v[208:211], v[168:171], v[72:75]\n"
    "buffer_load_dwordx4 v232, s[80:83], s64 offen lds\n"
    "v_mfma_f32_16x16x32_bf16 v[76:79], v[208:211], v[172:175], v[76:79]\n"
    "ds_read_b128 v[184:187], v229 offset:4096\n"
    "v_mfma_f32_16x16x32_bf16 v[80:83], v[212:215], v[160:163], v[80:83]\n"
    "s_waitcnt lgkmcnt(11)\n"
    "ds_read_b64_tr_b16 v[204:205], v227 offset:53248\n"
    "v_mfma_f32_16x16x32_bf16 v[84:87], v[212:215], v[164:167], v[84:87]\n"
    "s_add_u32 m0, s88, 138240\n"
    "s_add_u32 s64, s91, 25216\n"
    "ds_read_b64_tr_b16 v[206:207], v227 offset:53760\n"
    "v_mfma_f32_16x16x32_bf16 v[88:91], v[212:215], v[168:171], v[88:91]\n"
    "buffer_load_dwordx4 v233, s[80:83], s64 offen lds\n"
    "v_mfma_f32_16x16x32_bf16 v[92:95], v[212:215], v[172:175], v[92:95]\n"
    "ds_read_b128 v[188:191], v229 offset:6144\n"
    "v_mfma_f32_16x16x32_bf16 v[96:99], v[216:219], v[160:163], v[96:99]\n"
    "s_waitcnt lgkmcnt(12)\n"
    "ds_read_b64_tr_b16 v[208:209], v224 offset:61440\n"
    "v_mfma_f32_16x16x32_bf16 v[100:103], v[216:219], v[164:167], v[100:103]\n"
    "ds_read_b64_tr_b16 v[210:211], v224 offset:61952\n"
    "v_mfma_f32_16x16x32_bf16 v[104:107], v[216:219], v[168:171], v[104:107]\n"
    "v_mfma_f32_16x16x32_bf16 v[108:111], v[216:219], v[172:175], v[108:111]\n"
    "v_mfma_f32_16x16x32_bf16 v[112:115], v[220:223], v[160:163], v[112:115]\n"
    "s_waitcnt lgkmcnt(2)\n"
    "ds_read_b64_tr_b16 v[212:213], v225 offset:61440\n"
    "v_mfma_f32_16x16x32_bf16 v[116:119], v[220:223], v[164:167], v[116:119]\n"
    "ds_read_b64_tr_b16 v[214:215], v225 offset:61952\n"
    "v_mfma_f32_16x16x32_bf16 v[120:123], v[220:223], v[168:171], v[120:123]\n"
    "v_mfma_f32_16x16x32_bf16 v[124:127], v[220:223], v[172:175], v[124:127]\n"
    "v_mfma_f32_16x16x32_bf16 v[0:3], v[192:195], v[176:179], v[0:3]\n"
    "ds_read_b64_tr_b16 v[216:217], v226 offset:61440\n"
    "v_mfma_f32_16x16x32_bf16 v[4:7], v[192:195], v[180:183], v[4:7]\n"
    "ds_read_b64_tr_b16 v[218:219], v226 offset:61952\n"
    "v_mfma_f32_16x16x32_bf16 v[8:11], v[192:195], v[184:187], v[8:11]\n"
    "v_mfma_f32_16x16x32_bf16 v[12:15], v[192:195], v[188:191], v[12:15]\n"
    "v_mfma_f32_16x16x32_bf16 v[16:19], v[196:199], v[176:179], v[16:19]\n"
    "ds_read_b64_tr_b16 v[220:221], v227 offset:61440\n"
    "v_mfma_f32_16x16x32_bf16 v[20:23], v[196:199], v[180:183], v[20:23]\n"
    "ds_read_b64_tr_b16 v[222:223], v227 offset:61952\n"
    "v_mfma_f32_16x16x32_bf16 v[24:27], v[196:199], v[184:187], v[24:27]\n"
    "v_mfma_f32_16x16x32_bf16 v[28:31], v[196:199], v[188:191], v[28:31]\n"
    "v_mfma_f32_16x16x32_bf16 v[32:35], v[200:203], v[176:179], v[32:35]\n"
    "s_waitcnt vmcnt(12)\n"
    "s_barrier\n"
    "ds_read_b64_tr_b16 v[192:193], v224 offset:0\n"
    "v_mfma_f32_16x16x32_bf16 v[36:39], v[200:203], v[180:183], v[36:39]\n"
    "s_add_u32 m0, s89, 32768\n"
    "s_add_u32 s64, s92, 3145728\n"
    "ds_read_b64_tr_b16 v[194:195], v224 offset:512\n"
    "v_mfma_f32_16x16x32_bf16 v[40:43], v[200:203], v[184:187], v[40:43]\n"
    "buffer_load_dwordx4 v234, s[84:87], s64 offen lds\n"
    "v_mfma_f32_16x16x32_bf16 v[44:47], v[200:203], v[188:191], v[44:47]\n"
    "ds_read_b128 v[128:131], v228 offset:32768\n"
    "v_mfma_f32_16x16x32_bf16 v[48:51], v[204:207], v[176:179], v[48:51]\n"
    "s_waitcnt lgkmcnt(9)\n"
    "ds_read_b64_tr_b16 v[196:197], v225 offset:0\n"
    "v_mfma_f32_16x16x32_bf16 v[52:55], v[204:207], v[180:183], v[52:55]\n"
    "s_add_u32 m0, s89, 33792\n"
    "s_add_u32 s64, s92, 3211264\n"
    "ds_read_b64_tr_b16 v[198:199], v225 offset:512\n"
    "v_mfma_f32_16x16x32_bf16 v[56:59], v[204:207], v[184:187], v[56:59]\n"
    "buffer_load_dwordx4 v235, s[84:87], s64 offen lds\n"
    "v_mfma_f32_16x16x32_bf16 v[60:63], v[204:207], v[188:191], v[60:63]\n"
    "ds_read_b128 v[132:135], v228 offset:34816\n"
    "v_mfma_f32_16x16x32_bf16 v[64:67], v[208:211], v[176:179], v[64:67]\n"
    "s_waitcnt lgkmcnt(10)\n"
    "ds_read_b64_tr_b16 v[200:201], v226 offset:0\n"
    "v_mfma_f32_16x16x32_bf16 v[68:71], v[208:211], v[180:183], v[68:71]\n"
    "s_add_u32 m0, s89, 40960\n"
    "s_add_u32 s64, s92, 3145856\n"
    "ds_read_b64_tr_b16 v[202:203], v226 offset:512\n"
    "v_mfma_f32_16x16x32_bf16 v[72:75], v[208:211], v[184:187], v[72:75]\n"
    "buffer_load_dwordx4 v234, s[84:87], s64 offen lds\n"
    "v_mfma_f32_16x16x32_bf16 v[76:79], v[208:211], v[188:191], v[76:79]\n"
    "ds_read_b128 v[136:139], v228 offset:36864\n"
    "v_mfma_f32_16x16x32_bf16 v[80:83], v[212:215], v[176:179], v[80:83]\n"
    "s_waitcnt lgkmcnt(11)\n"
    "ds_read_b64_tr_b16 v[204:205], v227 offset:0\n"
    "v_mfma_f32_16x16x32_bf16 v[84:87], v[212:215], v[180:183], v[84:87]\n"
    "s_add_u32 m0, s89, 41984\n"
    "s_add_u32 s64, s92, 3211392\n"
    "ds_read_b64_tr_b16 v[206:207], v227 offset:512\n"
    "v_mfma_f32_16x16x32_bf16 v[88:91], v[212:215], v[184:187], v[88:91]\n"
    "buffer_load_dwordx4 v235, s[84:87], s64 offen lds\n"
    "v_mfma_f32_16x16x32_bf16 v[92:95], v[212:215], v[188:191], v[92:95]\n"
    "ds_read_b128 v[140:143], v228 offset:38912\n"
    "v_mfma_f32_16x16x32_bf16 v[96:99], v[216:219], v[176:179], v[96:99]\n"
    "s_waitcnt lgkmcnt(12)\n"
    "ds_read_b64_tr_b16 v[208:209], v224 offset:8192\n"
    "v_mfma_f32_16x16x32_bf16 v[100:103], v[216:219], v[180:183], v[100:103]\n"
    "s_add_u32 m0, s88, 65536\n"
    "s_add_u32 s64, s90, 768\n"
    "ds_read_b64_tr_b16 v[210:211], v224 offset:8704\n"
    "v_mfma_f32_16x16x32_bf16 v[104:107], v[216:219], v[184:187], v[104:107]\n"
    "buffer_load_dwordx4 v232, s[80:83], s64 offen lds\n"
    "v_mfma_f32_16x16x32_bf16 v[108:111], v[216:219], v[188:191], v[108:111]\n"
    "v_mfma_f32_16x16x32_bf16 v[112:115], v[220:223], v[176:179], v[112:115]\n"
    "s_waitcnt lgkmcnt(2)\n"
    "ds_read_b64_tr_b16 v[212:213], v225 offset:8192\n"
    "v_mfma_f32_16x16x32_bf16 v[116:119], v[220:223], v[180:183], v[116:119]\n"
    "s_add_u32 m0, s88, 66560\n"
    "s_add_u32 s64, s90, 8960\n"
    "ds_read_b64_tr_b16 v[214:215], v225 offset:8704\n"
    "v_mfma_f32_16x16x32_bf16 v[120:123], v[220:223], v[184:187], v[120:123]\n"
    "buffer_load_dwordx4 v233, s[80:83], s64 offen lds\n"
    "v_mfma_f32_16x16x32_bf16 v[124:127], v[220:223], v[188:191], v[124:127]\n"
    "v_mfma_f32_16x16x32_bf16 v[0:3], v[192:195], v[128:131], v[0:3]\n"
    "ds_read_b64_tr_b16 v[216:217], v226 offset:8192\n"
    "v_mfma_f32_16x16x32_bf16 v[4:7], v[192:195], v[132:135], v[4:7]\n"
    "s_add_u32 m0, s88, 67584\n"
    "s_add_u32 s64, s90, 17152\n"
    "ds_read_b64_tr_b16 v[218:219], v226 offset:8704\n"
    "v_mfma_f32_16x16x32_bf16 v[8:11], v[192:195], v[136:139], v[8:11]\n"
    "buffer_load_dwordx4 v232, s[80:83], s64 offen lds\n"
    "v_mfma_f32_16x16x32_bf16 v[12:15], v[192:195], v[140:143], v[12:15]\n"
    "v_mfma_f32_16x16x32_bf16 v[16:19], v[196:199], v[128:131], v[16:19]\n"
    "ds_read_b64_tr_b16 v[220:221], v227 offset:8192\n"
    "v_mfma_f32_16x16x32_bf16 v[20:23], v[196:199], v[132:135], v[20:23]\n"
    "s_add_u32 m0, s88, 68608\n"
    "s_add_u32 s64, s90, 25344\n"
    "ds_read_b64_tr_b16 v[222:223], v227 offset:8704\n"
    "v_mfma_f32_16x16x32_bf16 v[24:27], v[196:199], v[136:139], v[24:27]\n"
    "buffer_load_dwordx4 v233, s[80:83], s64 offen lds\n"
    "v_mfma_f32_16x16x32_bf16 v[28:31], v[196:199], v[140:143], v[28:31]\n"
    "v_mfma_f32_16x16x32_bf16 v[32:35], v[200:203], v[128:131], v[32:35]\n"
    "ds_read_b64_tr_b16 v[192:193], v224 offset:4096\n"
    "v_mfma_f32_16x16x32_bf16 v[36:39], v[200:203], v[132:135], v[36:39]\n"
    "s_add_u32 m0, s88, 69632\n"
    "s_add_u32 s64, s91, 768\n"
    "ds_read_b64_tr_b16 v[194:195], v224 offset:4608\n"
    "v_mfma_f32_16x16x32_bf16 v[40:43], v[200:203], v[136:139], v[40:43]\n"
    "buffer_load_dwordx4 v232, s[80:83], s64 offen lds\n"
    "v_mfma_f32_16x16x32_bf16 v[44:47], v[200:203], v[140:143], v[44:47]\n"
    "ds_read_b128 v[144:147], v229 offset:32768\n"
    "v_mfma_f32_16x16x32_bf16 v[48:51], v[204:207], v[128:131], v[48:51]\n"
    "s_waitcnt lgkmcnt(9)\n"
    "ds_read_b64_tr_b16 v[196:197], v225 offset:4096\n"
    "v_mfma_f32_16x16x32_bf16 v[52:55], v[204:207], v[132:135], v[52:55]\n"
    "s_add_u32 m0, s88, 70656\n"
    "s_add_u32 s64, s91, 8960\n"
    "ds_read_b64_tr_b16 v[198:199], v225 offset:4608\n"
    "v_mfma_f32_16x16x32_bf16 v[56:59], v[204:207], v[136:139], v[56:59]\n"
    "buffer_load_dwordx4 v233, s[80:83], s64 offen lds\n"
    "v_mfma_f32_16x16x32_bf16 v[60:63], v[204:207], v[140:143], v[60:63]\n"
    "ds_read_b128 v[148:151], v229 offset:34816\n"
    "v_mfma_f32_16x16x32_bf16 v[64:67], v[208:211], v[128:131], v[64:67]\n"
    "s_waitcnt lgkmcnt(10)\n"
    "ds_read_b64_tr_b16 v[200:201], v226 offset:4096\n"
    "v_mfma_f32_16x16x32_bf16 v[68:71], v[208:211], v[132:135], v[68:71]\n"
    "s_add_u32 m0, s88, 71680\n"
    "s_add_u32 s64, s91, 17152\n"
    "ds_read_b64_tr_b16 v[202:203], v226 offset:4608\n"
    "v_mfma_f32_16x16x32_bf16 v[72:75], v[208:211], v[136:139], v[72:75]\n"
    "buffer_load_dwordx4 v232, s[80:83], s64 offen lds\n"
    "v_mfma_f32_16x16x32_bf16 v[76:79], v[208:211], v[140:143], v[76:79]\n"
    "ds_read_b128 v[152:155], v229 offset:36864\n"
    "v_mfma_f32_16x16x32_bf16 v[80:83], v[212:215], v[128:131], v[80:83]\n"
    "s_waitcnt lgkmcnt(11)\n"
    "ds_read_b64_tr_b16 v[204:205], v227 offset:4096\n"
    "v_mfma_f32_16x16x32_bf16 v[84:87], v[212:215], v[132:135], v[84:87]\n"
    "s_add_u32 m0, s88, 72704\n"
    "s_add_u32 s64, s91, 25344\n"
    "ds_read_b64_tr_b16 v[206:207], v227 offset:4608\n"
    "v_mfma_f32_16x16x32_bf16 v[88:91], v[212:215], v[136:139], v[88:91]\n"
    "buffer_load_dwordx4 v233, s[80:83], s64 offen lds\n"
    "v_mfma_f32_16x16x32_bf16 v[92:95], v[212:215], v[140:143], v[92:95]\n"
    "ds_read_b128 v[156:159], v229 offset:38912\n"
    "v_mfma_f32_16x16x32_bf16 v[96:99], v[216:219], v[128:131], v[96:99]\n"
    "s_waitcnt lgkmcnt(12)\n"
    "ds_read_b64_tr_b16 v[208:209], v224 offset:12288\n"
    "v_mfma_f32_16x16x32_bf16 v[100:103], v[216:219], v[132:135], v[100:103]\n"
    "ds_read_b64_tr_b16 v[210:211], v224 offset:12800\n"
    "v_mfma_f32_16x16x32_bf16 v[104:107], v[216:219], v[136:139], v[104:107]\n"
    "v_mfma_f32_16x16x32_bf16 v[108:111], v[216:219], v[140:143], v[108:111]\n"
    "v_mfma_f32_16x16x32_bf16 v[112:115], v[220:223], v[128:131], v[112:115]\n"
    "s_waitcnt lgkmcnt(2)\n"
    "ds_read_b64_tr_b16 v[212:213], v225 offset:12288\n"
    "v_mfma_f32_16x16x32_bf16 v[116:119], v[220:223], v[132:135], v[116:119]\n"
    "ds_read_b64_tr_b16 v[214:215], v225 offset:12800\n"
    "v_mfma_f32_16x16x32_bf16 v[120:123], v[220:223], v[136:139], v[120:123]\n"
    "v_mfma_f32_16x16x32_bf16 v[124:127], v[220:223], v[140:143], v[124:127]\n"
    "v_mfma_f32_16x16x32_bf16 v[0:3], v[192:195], v[144:147], v[0:3]\n"
    "ds_read_b64_tr_b16 v[216:217], v226 offset:12288\n"
    "v_mfma_f32_16x16x32_bf16 v[4:7], v[192:195], v[148:151], v[4:7]\n"
    "ds_read_b64_tr_b16 v[218:219], v226 offset:12800\n"
    "v_mfma_f32_16x16x32_bf16 v[8:11], v[192:195], v[152:155], v[8:11]\n"
    "v_mfma_f32_16x16x32_bf16 v[12:15], v[192:195], v[156:159], v[12:15]\n"
    "v_mfma_f32_16x16x32_bf16 v[16:19], v[196:199], v[144:147], v[16:19]\n"
    "ds_read_b64_tr_b16 v[220:221], v227 offset:12288\n"
    "v_mfma_f32_16x16x32_bf16 v[20:23], v[196:199], v[148:151], v[20:23]\n"
    "ds_read_b64_tr_b16 v[222:223], v227 offset:12800\n"
    "v_mfma_f32_16x16x32_bf16 v[24:27], v[196:199], v[152:155], v[24:27]\n"
    "v_mfma_f32_16x16x32_bf16 v[28:31], v[196:199], v[156:159], v[28:31]\n"
    "v_mfma_f32_16x16x32_bf16 v[32:35], v[200:203], v[144:147], v[32:35]\n"
    "s_waitcnt vmcnt(12)\n"
    "s_barrier\n"
    "ds_read_b64_tr_b16 v[192:193], v224 offset:16384\n"
    "v_mfma_f32_16x16x32_bf16 v[36:39], v[200:203], v[148:151], v[36:39]\n"
    "s_add_u32 m0, s89, 49152\n"
    "s_add_u32 s64, s92, 3670016\n"
    "ds_read_b64_tr_b16 v[194:195], v224 offset:16896\n"
    "v_mfma_f32_16x16x32_bf16 v[40:43], v[200:203], v[152:155], v[40:43]\n"
    "buffer_load_dwordx4 v234, s[84:87], s64 offen lds\n"
    "v_mfma_f32_16x16x32_bf16 v[44:47], v[200:203], v[156:159], v[44:47]\n"
    "ds_read_b128 v[160:163], v230 offset:0\n"
    "v_mfma_f32_16x16x32_bf16 v[48:51], v[204:207], v[144:147], v[48:51]\n"
    "s_waitcnt lgkmcnt(9)\n"
    "ds_read_b64_tr_b16 v[196:197], v225 offset:16384\n"
    "v_mfma_f32_16x16x32_bf16 v[52:55], v[204:207], v[148:151], v[52:55]\n"
    "s_add_u32 m0, s89, 50176\n"
    "s_add_u32 s64, s92, 3735552\n"
    "ds_read_b64_tr_b16 v[198:199], v225 offset:16896\n"
    "v_mfma_f32_16x16x32_bf16 v[56:59], v[204:207], v[152:155], v[56:59]\n"
    "buffer_load_dwordx4 v235, s[84:87], s64 offen lds\n"
    "v_mfma_f32_16x16x32_bf16 v[60:63], v[204:207], v[156:159], v[60:63]\n"
    "ds_read_b128 v[164:167], v230 offset:2048\n"
    "v_mfma_f32_16x16x32_bf16 v[64:67], v[208:211], v[144:147], v[64:67]\n"
    "s_waitcnt lgkmcnt(10)\n"
    "ds_read_b64_tr_b16 v[200:201], v226 offset:16384\n"
    "v_mfma_f32_16x16x32_bf16 v[68:71], v[208:211], v[148:151], v[68:71]\n"
    "s_add_u32 m0, s89, 57344\n"
    "s_add_u32 s64, s92, 3670144\n"
    "ds_read_b64_tr_b16 v[202:203], v226 offset:16896\n"
    "v_mfma_f32_16x16x32_bf16 v[72:75], v[208:211], v[152:155], v[72:75]\n"
    "buffer_load_dwordx4 v234, s[84:87], s64 offen lds\n"
    "v_mfma_f32_16x16x32_bf16 v[76:79], v[208:211], v[156:159], v[76:79]\n"
    "ds_read_b128 v[168:171], v230 offset:4096\n"
    "v_mfma_f32_16x16x32_bf16 v[80:83], v[212:215], v[144:147], v[80:83]\n"
    "s_waitcnt lgkmcnt(11)\n"
    "ds_read_b64_tr_b16 v[204:205], v227 offset:16384\n"
    "v_mfma_f32_16x16x32_bf16 v[84:87], v[212:215], v[148:151], v[84:87]\n"
    "s_add_u32 m0, s89, 58368\n"
    "s_add_u32 s64, s92, 3735680\n"
    "ds_read_b64_tr_b16 v[206:207], v227 offset:16896\n"
    "v_mfma_f32_16x16x32_bf16 v[88:91], v[212:215], v[152:155], v[88:91]\n"
    "buffer_load_dwordx4 v235, s[84:87], s64 offen lds\n"
    "v_mfma_f32_16x16x32_bf16 v[92:95], v[212:215], v[156:159], v[92:95]\n"
    "ds_read_b128 v[172:175], v230 offset:6144\n"
    "v_mfma_f32_16x16x32_bf16 v[96:99], v[216:219], v[144:147], v[96:99]\n"
    "s_waitcnt lgkmcnt(12)\n"
    "ds_read_b64_tr_b16 v[208:209], v224 offset:24576\n"
    "v_mfma_f32_16x16x32_bf16 v[100:103], v[216:219], v[148:151], v[100:103]\n"
    "s_add_u32 m0, s88, 98304\n"
    "s_add_u32 s64, s90, 896\n"
    "ds_read_b64_tr_b16 v[210:211], v224 offset:25088\n"
    "v_mfma_f32_16x16x32_bf16 v[104:107], v[216:219], v[152:155], v[104:107]\n"
    "buffer_load_dwordx4 v232, s[80:83], s64 offen lds\n"
    "v_mfma_f32_16x16x32_bf16 v[108:111], v[216:219], v[156:159], v[108:111]\n"
    "v_mfma_f32_16x16x32_bf16 v[112:115], v[220:223], v[144:147], v[112:115]\n"
    "s_waitcnt lgkmcnt(2)\n"
    "ds_read_b64_tr_b16 v[212:213], v225 offset:24576\n"
    "v_mfma_f32_16x16x32_bf16 v[116:119], v[220:223], v[148:151], v[116:119]\n"
    "s_add_u32 m0, s88, 99328\n"
    "s_add_u32 s64, s90, 9088\n"
    "ds_read_b64_tr_b16 v[214:215], v225 offset:25088\n"
    "v_mfma_f32_16x16x32_bf16 v[120:123], v[220:223], v[152:155], v[120:123]\n"
    "buffer_load_dwordx4 v233, s[80:83], s64 offen lds\n"
    "v_mfma_f32_16x16x32_bf16 v[124:127], v[220:223], v[156:159], v[124:127]\n"
    "v_mfma_f32_16x16x32_bf16 v[0:3], v[192:195], v[160:163], v[0:3]\n"
    "ds_read_b64_tr_b16 v[216:217], v226 offset:24576\n"
    "v_mfma_f32_16x16x32_bf16 v[4:7], v[192:195], v[164:167], v[4:7]\n"
    "s_add_u32 m0, s88, 100352\n"
    "s_add_u32 s64, s90, 17280\n"
    "ds_read_b64_tr_b16 v[218:219], v226 offset:25088\n"
    "v_mfma_f32_16x16x32_bf16 v[8:11], v[192:195], v[168:171], v[8:11]\n"
    "buffer_load_dwordx4 v232, s[80:83], s64 offen lds\n"
    "v_mfma_f32_16x16x32_bf16 v[12:15], v[192:195], v[172:175], v[12:15]\n"
    "v_mfma_f32_16x16x32_bf16 v[16:19], v[196:199], v[160:163], v[16:19]\n"
    "ds_read_b64_tr_b16 v[220:221], v227 offset:24576\n"
    "v_mfma_f32_16x16x32_bf16 v[20:23], v[196:199], v[164:167], v[20:23]\n"
    "s_add_u32 m0, s88, 101376\n"
    "s_add_u32 s64, s90, 25472\n"
    "ds_read_b64_tr_b16 v[222:223], v227 offset:25088\n"
    "v_mfma_f32_16x16x32_bf16 v[24:27], v[196:199], v[168:171], v[24:27]\n"
    "buffer_load_dwordx4 v233, s[80:83], s64 offen lds\n"
    "v_mfma_f32_16x16x32_bf16 v[28:31], v[196:199], v[172:175], v[28:31]\n"
    "v_mfma_f32_16x16x32_bf16 v[32:35], v[200:203], v[160:163], v[32:35]\n"
    "ds_read_b64_tr_b16 v[192:193], v224 offset:20480\n"
    "v_mfma_f32_16x16x32_bf16 v[36:39], v[200:203], v[164:167], v[36:39]\n"
    "s_add_u32 m0, s88, 102400\n"
    "s_add_u32 s64, s91, 896\n"
    "ds_read_b64_tr_b16 v[194:195], v224 offset:20992\n"
    "v_mfma_f32_16x16x32_bf16 v[40:43], v[200:203], v[168:171], v[40:43]\n"
    "buffer_load_dwordx4 v232, s[80:83], s64 offen lds\n"
    "v_mfma_f32_16x16x32_bf16 v[44:47], v[200:203], v[172:175], v[44:47]\n"
    "ds_read_b128 v[176:179], v231 offset:0\n"
    "v_mfma_f32_16x16x32_bf16 v[48:51], v[204:207], v[160:163], v[48:51]\n"
    "s_waitcnt lgkmcnt(9)\n"
    "ds_read_b64_tr_b16 v[196:197], v225 offset:20480\n"
    "v_mfma_f32_16x16x32_bf16 v[52:55], v[204:207], v[164:167], v[52:55]\n"
    "s_add_u32 m0, s88, 103424\n"
    "s_add_u32 s64, s91, 9088\n"
    "ds_read_b64_tr_b16 v[198:199], v225 offset:20992\n"
    "v_mfma_f32_16x16x32_bf16 v[56:59], v[204:207], v[168:171], v[56:59]\n"
    "buffer_load_dwordx4 v233, s[80:83], s64 offen lds\n"
    "v_mfma_f32_16x16x32_bf16 v[60:63], v[204:207], v[172:175], v[60:63]\n"
    "ds_read_b128 v[180:183], v231 offset:2048\n"
    "v_mfma_f32_16x16x32_bf16 v[64:67], v[208:211], v[160:163], v[64:67]\n"
    "s_waitcnt lgkmcnt(10)\n"
    "ds_read_b64_tr_b16 v[200:201], v226 offset:20480\n"
    "v_mfma_f32_16x16x32_bf16 v[68:71], v[208:211], v[164:167], v[68:71]\n"
    "s_add_u32 m0, s88, 104448\n"
    "s_add_u32 s64, s91, 17280\n"
    "ds_read_b64_tr_b16 v[202:203], v226 offset:20992\n"
    "v_mfma_f32_16x16x32_bf16 v[72:75], v[208:211], v[168:171], v[72:75]\n"
    "buffer_load_dwordx4 v232, s[80:83], s64 offen lds\n"
    "v_mfma_f32_16x16x32_bf16 v[76:79], v[208:211], v[172:175], v[76:79]\n"
    "ds_read_b128 v[184:187], v231 offset:4096\n"
    "v_mfma_f32_16x16x32_bf16 v[80:83], v[212:215], v[160:163], v[80:83]\n"
    "s_waitcnt lgkmcnt(11)\n"
    "ds_read_b64_tr_b16 v[204:205], v227 offset:20480\n"
    "v_mfma_f32_16x16x32_bf16 v[84:87], v[212:215], v[164:167], v[84:87]\n"
    "s_add_u32 m0, s88, 105472\n"
    "s_add_u32 s64, s91, 25472\n"
    "ds_read_b64_tr_b16 v[206:207], v227 offset:20992\n"
    "v_mfma_f32_16x16x32_bf16 v[88:91], v[212:215], v[168:171], v[88:91]\n"
    "buffer_load_dwordx4 v233, s[80:83], s64 offen lds\n"
    "v_mfma_f32_16x16x32_bf16 v[92:95], v[212:215], v[172:175], v[92:95]\n"
    "ds_read_b128 v[188:191], v231 offset:6144\n"
    "v_mfma_f32_16x16x32_bf16 v[96:99], v[216:219], v[160:163], v[96:99]\n"
    "s_waitcnt lgkmcnt(12)\n"
    "ds_read_b64_tr_b16 v[208:209], v224 offset:28672\n"
    "v_mfma_f32_16x16x32_bf16 v[100:103], v[216:219], v[164:167], v[100:103]\n"
    "ds_read_b64_tr_b16 v[210:211], v224 offset:29184\n"
    "v_mfma_f32_16x16x32_bf16 v[104:107], v[216:219], v[168:171], v[104:107]\n"
    "v_mfma_f32_16x16x32_bf16 v[108:111], v[216:219], v[172:175], v[108:111]\n"
    "v_mfma_f32_16x16x32_bf16 v[112:115], v[220:223], v[160:163], v[112:115]\n"
    "s_waitcnt lgkmcnt(2)\n"
    "ds_read_b64_tr_b16 v[212:213], v225 offset:28672\n"
    "v_mfma_f32_16x16x32_bf16 v[116:119], v[220:223], v[164:167], v[116:119]\n"
    "ds_read_b64_tr_b16 v[214:215], v225 offset:29184\n"
    "v_mfma_f32_16x16x32_bf16 v[120:123], v[220:223], v[168:171], v[120:123]\n"
    "v_mfma_f32_16x16x32_bf16 v[124:127], v[220:223], v[172:175], v[124:127]\n"
    "v_mfma_f32_16x16x32_bf16 v[0:3], v[192:195], v[176:179], v[0:3]\n"
    "ds_read_b64_tr_b16 v[216:217], v226 offset:28672\n"
    "v_mfma_f32_16x16x32_bf16 v[4:7], v[192:195], v[180:183], v[4:7]\n"
    "ds_read_b64_tr_b16 v[218:219], v226 offset:29184\n"
    "v_mfma_f32_16x16x32_bf16 v[8:11], v[192:195], v[184:187], v[8:11]\n"
    "v_mfma_f32_16x16x32_bf16 v[12:15], v[192:195], v[188:191], v[12:15]\n"
    "v_mfma_f32_16x16x32_bf16 v[16:19], v[196:199], v[176:179], v[16:19]\n"
    "ds_read_b64_tr_b16 v[220:221], v227 offset:28672\n"
    "v_mfma_f32_16x16x32_bf16 v[20:23], v[196:199], v[180:183], v[20:23]\n"
    "ds_read_b64_tr_b16 v[222:223], v227 offset:29184\n"
    "v_mfma_f32_16x16x32_bf16 v[24:27], v[196:199], v[184:187], v[24:27]\n"
    "v_mfma_f32_16x16x32_bf16 v[28:31], v[196:199], v[188:191], v[28:31]\n"
    "v_mfma_f32_16x16x32_bf16 v[32:35], v[200:203], v[176:179], v[32:35]\n"
    "s_waitcnt vmcnt(12)\n"
    "s_barrier\n"
    "ds_read_b64_tr_b16 v[192:193], v224 offset:32768\n"
    "v_mfma_f32_16x16x32_bf16 v[36:39], v[200:203], v[180:183], v[36:39]\n"
    "ds_read_b64_tr_b16 v[194:195], v224 offset:33280\n"
    "v_mfma_f32_16x16x32_bf16 v[40:43], v[200:203], v[184:187], v[40:43]\n"
    "ds_read_b128 v[128:131], v228 offset:0\n"
    "v_mfma_f32_16x16x32_bf16 v[44:47], v[200:203], v[188:191], v[44:47]\n"
    "ds_read_b128 v[132:135], v228 offset:2048\n"
    "v_mfma_f32_16x16x32_bf16 v[48:51], v[204:207], v[176:179], v[48:51]\n"
    "s_waitcnt lgkmcnt(10)\n"
    "ds_read_b64_tr_b16 v[196:197], v225 offset:32768\n"
    "v_mfma_f32_16x16x32_bf16 v[52:55], v[204:207], v[180:183], v[52:55]\n"
    "ds_read_b64_tr_b16 v[198:199], v225 offset:33280\n"
    "v_mfma_f32_16x16x32_bf16 v[56:59], v[204:207], v[184:187], v[56:59]\n"
    "ds_read_b128 v[136:139], v228 offset:4096\n"
    "v_mfma_f32_16x16x32_bf16 v[60:63], v[204:207], v[188:191], v[60:63]\n"
    "ds_read_b128 v[140:143], v228 offset:6144\n"
    "v_mfma_f32_16x16x32_bf16 v[64:67], v[208:211], v[176:179], v[64:67]\n"
    "s_waitcnt lgkmcnt(12)\n"
    "ds_read_b64_tr_b16 v[200:201], v226 offset:32768\n"
    "v_mfma_f32_16x16x32_bf16 v[68:71], v[208:211], v[180:183], v[68:71]\n"
    "ds_read_b64_tr_b16 v[202:203], v226 offset:33280\n"
    "v_mfma_f32_16x16x32_bf16 v[72:75], v[208:211], v[184:187], v[72:75]\n"
    "v_mfma_f32_16x16x32_bf16 v[76:79], v[208:211], v[188:191], v[76:79]\n"
    "v_mfma_f32_16x16x32_bf16 v[80:83], v[212:215], v[176:179], v[80:83]\n"
    "s_waitcnt lgkmcnt(12)\n"
    "ds_read_b64_tr_b16 v[204:205], v227 offset:32768\n"
    "v_mfma_f32_16x16x32_bf16 v[84:87], v[212:215], v[180:183], v[84:87]\n"
    "ds_read_b64_tr_b16 v[206:207], v227 offset:33280\n"
    "v_mfma_f32_16x16x32_bf16 v[88:91], v[212:215], v[184:187], v[88:91]\n"
    "v_mfma_f32_16x16x32_bf16 v[92:95], v[212:215], v[188:191], v[92:95]\n"
    "v_mfma_f32_16x16x32_bf16 v[96:99], v[216:219], v[176:179], v[96:99]\n"
    "s_waitcnt lgkmcnt(12)\n"
    "ds_read_b64_tr_b16 v[208:209], v224 offset:40960\n"
    "v_mfma_f32_16x16x32_bf16 v[100:103], v[216:219], v[180:183], v[100:103]\n"
    "ds_read_b64_tr_b16 v[210:211], v224 offset:41472\n"
    "v_mfma_f32_16x16x32_bf16 v[104:107], v[216:219], v[184:187], v[104:107]\n"
    "v_mfma_f32_16x16x32_bf16 v[108:111], v[216:219], v[188:191], v[108:111]\n"
    "v_mfma_f32_16x16x32_bf16 v[112:115], v[220:223], v[176:179], v[112:115]\n"
    "s_waitcnt lgkmcnt(6)\n"
    "ds_read_b64_tr_b16 v[212:213], v225 offset:40960\n"
    "v_mfma_f32_16x16x32_bf16 v[116:119], v[220:223], v[180:183], v[116:119]\n"
    "ds_read_b64_tr_b16 v[214:215], v225 offset:41472\n"
    "v_mfma_f32_16x16x32_bf16 v[120:123], v[220:223], v[184:187], v[120:123]\n"
    "v_mfma_f32_16x16x32_bf16 v[124:127], v[220:223], v[188:191], v[124:127]\n"
    "v_mfma_f32_16x16x32_bf16 v[0:3], v[192:195], v[128:131], v[0:3]\n"
    "ds_read_b64_tr_b16 v[216:217], v226 offset:40960\n"
    "v_mfma_f32_16x16x32_bf16 v[4:7], v[192:195], v[132:135], v[4:7]\n"
    "ds_read_b64_tr_b16 v[218:219], v226 offset:41472\n"
    "v_mfma_f32_16x16x32_bf16 v[8:11], v[192:195], v[136:139], v[8:11]\n"
    "v_mfma_f32_16x16x32_bf16 v[12:15], v[192:195], v[140:143], v[12:15]\n"
    "v_mfma_f32_16x16x32_bf16 v[16:19], v[196:199], v[128:131], v[16:19]\n"
    "s_waitcnt lgkmcnt(8)\n"
    "ds_read_b64_tr_b16 v[220:221], v227 offset:40960\n"
    "v_mfma_f32_16x16x32_bf16 v[20:23], v[196:199], v[132:135], v[20:23]\n"
    "ds_read_b64_tr_b16 v[222:223], v227 offset:41472\n"
    "v_mfma_f32_16x16x32_bf16 v[24:27], v[196:199], v[136:139], v[24:27]\n"
    "v_mfma_f32_16x16x32_bf16 v[28:31], v[196:199], v[140:143], v[28:31]\n"
    "v_mfma_f32_16x16x32_bf16 v[32:35], v[200:203], v[128:131], v[32:35]\n"
    "s_waitcnt lgkmcnt(8)\n"
    "ds_read_b64_tr_b16 v[192:193], v224 offset:36864\n"
    "v_mfma_f32_16x16x32_bf16 v[36:39], v[200:203], v[132:135], v[36:39]\n"
    "ds_read_b64_tr_b16 v[194:195], v224 offset:37376\n"
    "v_mfma_f32_16x16x32_bf16 v[40:43], v[200:203], v[136:139], v[40:43]\n"
    "ds_read_b128 v[144:147], v229 offset:0\n"
    "v_mfma_f32_16x16x32_bf16 v[44:47], v[200:203], v[140:143], v[44:47]\n"
    "ds_read_b128 v[148:151], v229 offset:2048\n"
    "v_mfma_f32_16x16x32_bf16 v[48:51], v[204:207], v[128:131], v[48:51]\n"
    "s_waitcnt lgkmcnt(10)\n"
    "ds_read_b64_tr_b16 v[196:197], v225 offset:36864\n"
    "v_mfma_f32_16x16x32_bf16 v[52:55], v[204:207], v[132:135], v[52:55]\n"
    "ds_read_b64_tr_b16 v[198:199], v225 offset:37376\n"
    "v_mfma_f32_16x16x32_bf16 v[56:59], v[204:207], v[136:139], v[56:59]\n"
    "ds_read_b128 v[152:155], v229 offset:4096\n"
    "v_mfma_f32_16x16x32_bf16 v[60:63], v[204:207], v[140:143], v[60:63]\n"
    "ds_read_b128 v[156:159], v229 offset:6144\n"
    "v_mfma_f32_16x16x32_bf16 v[64:67], v[208:211], v[128:131], v[64:67]\n"
    "s_waitcnt lgkmcnt(12)\n"
    "ds_read_b64_tr_b16 v[200:201], v226 offset:36864\n"
    "v_mfma_f32_16x16x32_bf16 v[68:71], v[208:211], v[132:135], v[68:71]\n"
    "ds_read_b64_tr_b16 v[202:203], v226 offset:37376\n"
    "v_mfma_f32_16x16x32_bf16 v[72:75], v[208:211], v[136:139], v[72:75]\n"
    "v_mfma_f32_16x16x32_bf16 v[76:79], v[208:211], v[140:143], v[76:79]\n"
    "v_mfma_f32_16x16x32_bf16 v[80:83], v[212:215], v[128:131], v[80:83]\n"
    "s_waitcnt lgkmcnt(12)\n"
    "ds_read_b64_tr_b16 v[204:205], v227 offset:36864\n"
    "v_mfma_f32_16x16x32_bf16 v[84:87], v[212:215], v[132:135], v[84:87]\n"
    "ds_read_b64_tr_b16 v[206:207], v227 offset:37376\n"
    "v_mfma_f32_16x16x32_bf16 v[88:91], v[212:215], v[136:139], v[88:91]\n"
    "v_mfma_f32_16x16x32_bf16 v[92:95], v[212:215], v[140:143], v[92:95]\n"
    "v_mfma_f32_16x16x32_bf16 v[96:99], v[216:219], v[128:131], v[96:99]\n"
    "s_waitcnt lgkmcnt(12)\n"
    "ds_read_b64_tr_b16 v[208:209], v224 offset:45056\n"
    "v_mfma_f32_16x16x32_bf16 v[100:103], v[216:219], v[132:135], v[100:103]\n"
    "ds_read_b64_tr_b16 v[210:211], v224 offset:45568\n"
    "v_mfma_f32_16x16x32_bf16 v[104:107], v[216:219], v[136:139], v[104:107]\n"
    "v_mfma_f32_16x16x32_bf16 v[108:111], v[216:219], v[140:143], v[108:111]\n"
    "v_mfma_f32_16x16x32_bf16 v[112:115], v[220:223], v[128:131], v[112:115]\n"
    "s_waitcnt lgkmcnt(6)\n"
    "ds_read_b64_tr_b16 v[212:213], v225 offset:45056\n"
    "v_mfma_f32_16x16x32_bf16 v[116:119], v[220:223], v[132:135], v[116:119]\n"
    "ds_read_b64_tr_b16 v[214:215], v225 offset:45568\n"
    "v_mfma_f32_16x16x32_bf16 v[120:123], v[220:223], v[136:139], v[120:123]\n"
    "v_mfma_f32_16x16x32_bf16 v[124:127], v[220:223], v[140:143], v[124:127]\n"
    "v_mfma_f32_16x16x32_bf16 v[0:3], v[192:195], v[144:147], v[0:3]\n"
    "ds_read_b64_tr_b16 v[216:217], v226 offset:45056\n"
    "v_mfma_f32_16x16x32_bf16 v[4:7], v[192:195], v[148:151], v[4:7]\n"
    "ds_read_b64_tr_b16 v[218:219], v226 offset:45568\n"
    "v_mfma_f32_16x16x32_bf16 v[8:11], v[192:195], v[152:155], v[8:11]\n"
    "v_mfma_f32_16x16x32_bf16 v[12:15], v[192:195], v[156:159], v[12:15]\n"
    "v_mfma_f32_16x16x32_bf16 v[16:19], v[196:199], v[144:147], v[16:19]\n"
    "s_waitcnt lgkmcnt(8)\n"
    "ds_read_b64_tr_b16 v[220:221], v227 offset:45056\n"
    "v_mfma_f32_16x16x32_bf16 v[20:23], v[196:199], v[148:151], v[20:23]\n"
    "ds_read_b64_tr_b16 v[222:223], v227 offset:45568\n"
    "v_mfma_f32_16x16x32_bf16 v[24:27], v[196:199], v[152:155], v[24:27]\n"
    "v_mfma_f32_16x16x32_bf16 v[28:31], v[196:199], v[156:159], v[28:31]\n"
    "v_mfma_f32_16x16x32_bf16 v[32:35], v[200:203], v[144:147], v[32:35]\n"
    "s_waitcnt vmcnt(0)\n"
    "s_barrier\n"
    "s_waitcnt lgkmcnt(8)\n"
    "ds_read_b64_tr_b16 v[192:193], v224 offset:49152\n"
    "v_mfma_f32_16x16x32_bf16 v[36:39], v[200:203], v[148:151], v[36:39]\n"
    "s_add_u32 m0, s54, 0\n"
    "s_add_u32 s64, s51, 0\n"
    "ds_read_b64_tr_b16 v[194:195], v224 offset:49664\n"
    "v_mfma_f32_16x16x32_bf16 v[40:43], v[200:203], v[152:155], v[40:43]\n"
    "buffer_load_dwordx4 v236, s[40:43], s64 offen offset:0 lds\n"
    "v_mfma_f32_16x16x32_bf16 v[44:47], v[200:203], v[156:159], v[44:47]\n"
    "ds_read_b128 v[160:163], v228 offset:32768\n"
    "v_mfma_f32_16x16x32_bf16 v[48:51], v[204:207], v[144:147], v[48:51]\n"
    "s_waitcnt lgkmcnt(9)\n"
    "ds_read_b64_tr_b16 v[196:197], v225 offset:49152\n"
    "v_mfma_f32_16x16x32_bf16 v[52:55], v[204:207], v[148:151], v[52:55]\n"
    "ds_read_b64_tr_b16 v[198:199], v225 offset:49664\n"
    "v_mfma_f32_16x16x32_bf16 v[56:59], v[204:207], v[152:155], v[56:59]\n"
    "buffer_load_dwordx4 v236, s[40:43], s64 offen offset:1024 lds\n"
    "v_mfma_f32_16x16x32_bf16 v[60:63], v[204:207], v[156:159], v[60:63]\n"
    "ds_read_b128 v[164:167], v228 offset:34816\n"
    "v_mfma_f32_16x16x32_bf16 v[64:67], v[208:211], v[144:147], v[64:67]\n"
    "s_waitcnt lgkmcnt(10)\n"
    "ds_read_b64_tr_b16 v[200:201], v226 offset:49152\n"
    "v_mfma_f32_16x16x32_bf16 v[68:71], v[208:211], v[148:151], v[68:71]\n"
    "ds_read_b64_tr_b16 v[202:203], v226 offset:49664\n"
    "v_mfma_f32_16x16x32_bf16 v[72:75], v[208:211], v[152:155], v[72:75]\n"
    "buffer_load_dwordx4 v236, s[40:43], s64 offen offset:2048 lds\n"
    "v_mfma_f32_16x16x32_bf16 v[76:79], v[208:211], v[156:159], v[76:79]\n"
    "ds_read_b128 v[168:171], v228 offset:36864\n"
    "v_mfma_f32_16x16x32_bf16 v[80:83], v[212:215], v[144:147], v[80:83]\n"
    "s_waitcnt lgkmcnt(11)\n"
    "ds_read_b64_tr_b16 v[204:205], v227 offset:49152\n"
    "v_mfma_f32_16x16x32_bf16 v[84:87], v[212:215], v[148:151], v[84:87]\n"
    "ds_read_b64_tr_b16 v[206:207], v227 offset:49664\n"
    "v_mfma_f32_16x16x32_bf16 v[88:91], v[212:215], v[152:155], v[88:91]\n"
    "buffer_load_dwordx4 v236, s[40:43], s64 offen offset:3072 lds\n"
    "v_mfma_f32_16x16x32_bf16 v[92:95], v[212:215], v[156:159], v[92:95]\n"
    "ds_read_b128 v[172:175], v228 offset:38912\n"
    "v_mfma_f32_16x16x32_bf16 v[96:99], v[216:219], v[144:147], v[96:99]\n"
    "s_waitcnt lgkmcnt(12)\n"
    "ds_read_b64_tr_b16 v[208:209], v224 offset:57344\n"
    "v_mfma_f32_16x16x32_bf16 v[100:103], v[216:219], v[148:151], v[100:103]\n"
    "s_add_u32 m0, s54, 65536\n"
    "s_add_u32 s64, s51, 0\n"
    "ds_read_b64_tr_b16 v[210:211], v224 offset:57856\n"
    "v_mfma_f32_16x16x32_bf16 v[104:107], v[216:219], v[152:155], v[104:107]\n"
    "buffer_load_dwordx4 v236, s[44:47], s64 offen offset:0 lds\n"
    "v_mfma_f32_16x16x32_bf16 v[108:111], v[216:219], v[156:159], v[108:111]\n"
    "v_mfma_f32_16x16x32_bf16 v[112:115], v[220:223], v[144:147], v[112:115]\n"
    "s_waitcnt lgkmcnt(2)\n"
    "ds_read_b64_tr_b16 v[212:213], v225 offset:57344\n"
    "v_mfma_f32_16x16x32_bf16 v[116:119], v[220:223], v[148:151], v[116:119]\n"
    "ds_read_b64_tr_b16 v[214:215], v225 offset:57856\n"
    "v_mfma_f32_16x16x32_bf16 v[120:123], v[220:223], v[152:155], v[120:123]\n"
    "buffer_load_dwordx4 v236, s[44:47], s64 offen offset:1024 lds\n"
    "v_mfma_f32_16x16x32_bf16 v[124:127], v[220:223], v[156:159], v[124:127]\n"
    "v_mfma_f32_16x16x32_bf16 v[0:3], v[192:195], v[160:163], v[0:3]\n"
    "ds_read_b64_tr_b16 v[216:217], v226 offset:57344\n"
    "v_mfma_f32_16x16x32_bf16 v[4:7], v[192:195], v[164:167], v[4:7]\n"
    "ds_read_b64_tr_b16 v[218:219], v226 offset:57856\n"
    "v_mfma_f32_16x16x32_bf16 v[8:11], v[192:195], v[168:171], v[8:11]\n"
    "buffer_load_dwordx4 v236, s[44:47], s64 offen offset:2048 lds\n"
    "v_mfma_f32_16x16x32_bf16 v[12:15], v[192:195], v[172:175], v[12:15]\n"
    "v_mfma_f32_16x16x32_bf16 v[16:19], v[196:199], v[160:163], v[16:19]\n"
    "ds_read_b64_tr_b16 v[220:221], v227 offset:57344\n"
    "v_mfma_f32_16x16x32_bf16 v[20:23], v[196:199], v[164:167], v[20:23]\n"
    "ds_read_b64_tr_b16 v[222:223], v227 offset:57856\n"
    "v_mfma_f32_16x16x32_bf16 v[24:27], v[196:199], v[168:171], v[24:27]\n"
    "buffer_load_dwordx4 v236, s[44:47], s64 offen offset:3072 lds\n"
    "v_mfma_f32_16x16x32_bf16 v[28:31], v[196:199], v[172:175], v[28:31]\n"
    "v_mfma_f32_16x16x32_bf16 v[32:35], v[200:203], v[160:163], v[32:35]\n"
    "ds_read_b64_tr_b16 v[192:193], v224 offset:53248\n"
    "v_mfma_f32_16x16x32_bf16 v[36:39], v[200:203], v[164:167], v[36:39]\n"
    "s_add_u32 m0, s54, 16384\n"
    "s_add_u32 s64, s51, 16384\n"
    "ds_read_b64_tr_b16 v[194:195], v224 offset:53760\n"
    "v_mfma_f32_16x16x32_bf16 v[40:43], v[200:203], v[168:171], v[40:43]\n"
    "buffer_load_dwordx4 v236, s[40:43], s64 offen offset:0 lds\n"
    "v_mfma_f32_16x16x32_bf16 v[44:47], v[200:203], v[172:175], v[44:47]\n"
    "ds_read_b128 v[176:179], v229 offset:32768\n"
    "v_mfma_f32_16x16x32_bf16 v[48:51], v[204:207], v[160:163], v[48:51]\n"
    "s_waitcnt lgkmcnt(9)\n"
    "ds_read_b64_tr_b16 v[196:197], v225 offset:53248\n"
    "v_mfma_f32_16x16x32_bf16 v[52:55], v[204:207], v[164:167], v[52:55]\n"
    "ds_read_b64_tr_b16 v[198:199], v225 offset:53760\n"
    "v_mfma_f32_16x16x32_bf16 v[56:59], v[204:207], v[168:171], v[56:59]\n"
    "buffer_load_dwordx4 v236, s[40:43], s64 offen offset:1024 lds\n"
    "v_mfma_f32_16x16x32_bf16 v[60:63], v[204:207], v[172:175], v[60:63]\n"
    "ds_read_b128 v[180:183], v229 offset:34816\n"
    "v_mfma_f32_16x16x32_bf16 v[64:67], v[208:211], v[160:163], v[64:67]\n"
    "s_waitcnt lgkmcnt(10)\n"
    "ds_read_b64_tr_b16 v[200:201], v226 offset:53248\n"
    "v_mfma_f32_16x16x32_bf16 v[68:71], v[208:211], v[164:167], v[68:71]\n"
    "ds_read_b64_tr_b16 v[202:203], v226 offset:53760\n"
    "v_mfma_f32_16x16x32_bf16 v[72:75], v[208:211], v[168:171], v[72:75]\n"
    "buffer_load_dwordx4 v236, s[40:43], s64 offen offset:2048 lds\n"
    "v_mfma_f32_16x16x32_bf16 v[76:79], v[208:211], v[172:175], v[76:79]\n"
    "ds_read_b128 v[184:187], v229 offset:36864\n"
    "v_mfma_f32_16x16x32_bf16 v[80:83], v[212:215], v[160:163], v[80:83]\n"
    "s_waitcnt lgkmcnt(11)\n"
    "ds_read_b64_tr_b16 v[204:205], v227 offset:53248\n"
    "v_mfma_f32_16x16x32_bf16 v[84:87], v[212:215], v[164:167], v[84:87]\n"
    "ds_read_b64_tr_b16 v[206:207], v227 offset:53760\n"
    "v_mfma_f32_16x16x32_bf16 v[88:91], v[212:215], v[168:171], v[88:91]\n"
    "buffer_load_dwordx4 v236, s[40:43], s64 offen offset:3072 lds\n"
    "v_mfma_f32_16x16x32_bf16 v[92:95], v[212:215], v[172:175], v[92:95]\n"
    "ds_read_b128 v[188:191], v229 offset:38912\n"
    "v_mfma_f32_16x16x32_bf16 v[96:99], v[216:219], v[160:163], v[96:99]\n"
    "s_waitcnt lgkmcnt(12)\n"
    "ds_read_b64_tr_b16 v[208:209], v224 offset:61440\n"
    "v_mfma_f32_16x16x32_bf16 v[100:103], v[216:219], v[164:167], v[100:103]\n"
    "ds_read_b64_tr_b16 v[210:211], v224 offset:61952\n"
    "v_mfma_f32_16x16x32_bf16 v[104:107], v[216:219], v[168:171], v[104:107]\n"
    "v_mfma_f32_16x16x32_bf16 v[108:111], v[216:219], v[172:175], v[108:111]\n"
    "v_mfma_f32_16x16x32_bf16 v[112:115], v[220:223], v[160:163], v[112:115]\n"
    "s_waitcnt lgkmcnt(2)\n"
    "ds_read_b64_tr_b16 v[212:213], v225 offset:61440\n"
    "v_mfma_f32_16x16x32_bf16 v[116:119], v[220:223], v[164:167], v[116:119]\n"
    "ds_read_b64_tr_b16 v[214:215], v225 offset:61952\n"
    "v_mfma_f32_16x16x32_bf16 v[120:123], v[220:223], v[168:171], v[120:123]\n"
    "v_mfma_f32_16x16x32_bf16 v[124:127], v[220:223], v[172:175], v[124:127]\n"
    "v_mfma_f32_16x16x32_bf16 v[0:3], v[192:195], v[176:179], v[0:3]\n"
    "ds_read_b64_tr_b16 v[216:217], v226 offset:61440\n"
    "v_mfma_f32_16x16x32_bf16 v[4:7], v[192:195], v[180:183], v[4:7]\n"
    "ds_read_b64_tr_b16 v[218:219], v226 offset:61952\n"
    "v_mfma_f32_16x16x32_bf16 v[8:11], v[192:195], v[184:187], v[8:11]\n"
    "v_mfma_f32_16x16x32_bf16 v[12:15], v[192:195], v[188:191], v[12:15]\n"
    "v_mfma_f32_16x16x32_bf16 v[16:19], v[196:199], v[176:179], v[16:19]\n"
    "ds_read_b64_tr_b16 v[220:221], v227 offset:61440\n"
    "v_mfma_f32_16x16x32_bf16 v[20:23], v[196:199], v[180:183], v[20:23]\n"
    "ds_read_b64_tr_b16 v[222:223], v227 offset:61952\n"
    "v_mfma_f32_16x16x32_bf16 v[24:27], v[196:199], v[184:187], v[24:27]\n"
    "v_mfma_f32_16x16x32_bf16 v[28:31], v[196:199], v[188:191], v[28:31]\n"
    "v_mfma_f32_16x16x32_bf16 v[32:35], v[200:203], v[176:179], v[32:35]\n"
    "v_mfma_f32_16x16x32_bf16 v[36:39], v[200:203], v[180:183], v[36:39]\n"
    "v_mfma_f32_16x16x32_bf16 v[40:43], v[200:203], v[184:187], v[40:43]\n"
    "v_mfma_f32_16x16x32_bf16 v[44:47], v[200:203], v[188:191], v[44:47]\n"
    "v_mfma_f32_16x16x32_bf16 v[48:51], v[204:207], v[176:179], v[48:51]\n"
    "s_waitcnt lgkmcnt(6)\n"
    "v_mfma_f32_16x16x32_bf16 v[52:55], v[204:207], v[180:183], v[52:55]\n"
    "v_mfma_f32_16x16x32_bf16 v[56:59], v[204:207], v[184:187], v[56:59]\n"
    "v_mfma_f32_16x16x32_bf16 v[60:63], v[204:207], v[188:191], v[60:63]\n"
    "v_mfma_f32_16x16x32_bf16 v[64:67], v[208:211], v[176:179], v[64:67]\n"
    "s_waitcnt lgkmcnt(4)\n"
    "v_mfma_f32_16x16x32_bf16 v[68:71], v[208:211], v[180:183], v[68:71]\n"
    "v_mfma_f32_16x16x32_bf16 v[72:75], v[208:211], v[184:187], v[72:75]\n"
    "v_mfma_f32_16x16x32_bf16 v[76:79], v[208:211], v[188:191], v[76:79]\n"
    "v_mfma_f32_16x16x32_bf16 v[80:83], v[212:215], v[176:179], v[80:83]\n"
    "s_waitcnt lgkmcnt(2)\n"
    "v_mfma_f32_16x16x32_bf16 v[84:87], v[212:215], v[180:183], v[84:87]\n"
    "v_mfma_f32_16x16x32_bf16 v[88:91], v[212:215], v[184:187], v[88:91]\n"
    "v_mfma_f32_16x16x32_bf16 v[92:95], v[212:215], v[188:191], v[92:95]\n"
    "v_mfma_f32_16x16x32_bf16 v[96:99], v[216:219], v[176:179], v[96:99]\n"
    "s_waitcnt lgkmcnt(0)\n"
    "v_mfma_f32_16x16x32_bf16 v[100:103], v[216:219], v[180:183], v[100:103]\n"
    "v_mfma_f32_16x16x32_bf16 v[104:107], v[216:219], v[184:187], v[104:107]\n"
    "v_mfma_f32_16x16x32_bf16 v[108:111], v[216:219], v[188:191], v[108:111]\n"
    "v_mfma_f32_16x16x32_bf16 v[112:115], v[220:223], v[176:179], v[112:115]\n"
    "v_mfma_f32_16x16x32_bf16 v[116:119], v[220:223], v[180:183], v[116:119]\n"
    "v_mfma_f32_16x16x32_bf16 v[120:123], v[220:223], v[184:187], v[120:123]\n"
    "v_mfma_f32_16x16x32_bf16 v[124:127], v[220:223], v[188:191], v[124:127]\n"
    "s_barrier\n"
    "s_add_u32 m0, s54, 32768\n"
    "s_add_u32 s64, s51, 32768\n"
    "buffer_load_dwordx4 v236, s[40:43], s64 offen offset:0 lds\n"
    "buffer_load_dwordx4 v236, s[40:43], s64 offen offset:1024 lds\n"
    "buffer_load_dwordx4 v236, s[40:43], s64 offen offset:2048 lds\n"
    "buffer_load_dwordx4 v236, s[40:43], s64 offen offset:3072 lds\n"
    "s_add_u32 m0, s54, 81920\n"
    "s_add_u32 s64, s51, 16384\n"
    "buffer_load_dwordx4 v236, s[44:47], s64 offen offset:0 lds\n"
    "buffer_load_dwordx4 v236, s[44:47], s64 offen offset:1024 lds\n"
    "buffer_load_dwordx4 v236, s[44:47], s64 offen offset:2048 lds\n"
    "buffer_load_dwordx4 v236, s[44:47], s64 offen offset:3072 lds\n"
    "s_mov_b32 s80, s96\n"
    "s_and_b32 s81, s97, 0xffff\n"
    "s_movk_i32 s82, 0x1000\n"
    "s_mov_b32 s83, 0x20000\n"
    "s_lshl_b32 s58, s51, 1\n"
    "s_add_u32 m0, s58, 131072\n"
    "s_nop 0\n"
    "buffer_load_dwordx4 v236, s[80:83], 0 offen offset:0 lds\n"
    "buffer_load_dwordx4 v236, s[80:83], 0 offen offset:1024 lds\n"
    "buffer_load_dwordx4 v236, s[80:83], 0 offen offset:2048 lds\n"
    "buffer_load_dwordx4 v236, s[80:83], 0 offen offset:3072 lds\n"
    "s_mov_b32 s80, s98\n"
    "s_and_b32 s81, s99, 0xffff\n"
    "s_movk_i32 s82, 0x1000\n"
    "s_mov_b32 s83, 0x20000\n"
    "s_lshl_b32 s58, s51, 1\n"
    "s_add_u32 m0, s58, 135168\n"
    "s_nop 0\n"
    "buffer_load_dwordx4 v236, s[80:83], 0 offen offset:0 lds\n"
    "buffer_load_dwordx4 v236, s[80:83], 0 offen offset:1024 lds\n"
    "buffer_load_dwordx4 v236, s[80:83], 0 offen offset:2048 lds\n"
    "buffer_load_dwordx4 v236, s[80:83], 0 offen offset:3072 lds\n"
    "v_mov_b32 v242, 0\n"
    "global_load_dword v242, v242, s[78:79]\n"
    "v_mov_b32 v128, 0x3e0293ee\n"
    "v_mov_b32 v129, 0x3e0293ee\n"
    "v_pk_mul_f32 v[0:1], v[0:1], v[128:129]\n"
    "v_pk_mul_f32 v[2:3], v[2:3], v[128:129]\n"
    "v_cvt_pk_bf16_f32 v192, v0, v1\n"
    "v_cvt_pk_bf16_f32 v193, v2, v3\n"
    "v_pk_mul_f32 v[16:17], v[16:17], v[128:129]\n"
    "v_pk_mul_f32 v[18:19], v[18:19], v[128:129]\n"
    "v_cvt_pk_bf16_f32 v194, v16, v17\n"
    "v_cvt_pk_bf16_f32 v195, v18, v19\n"
    "v_accvgpr_write_b32 a32, v192\n"
    "v_accvgpr_write_b32 a33, v193\n"
    "v_accvgpr_write_b32 a34, v194\n"
    "v_accvgpr_write_b32 a35, v195\n"
    "v_pk_mul_f32 v[32:33], v[32:33], v[128:129]\n"
    "v_pk_mul_f32 v[34:35], v[34:35], v[128:129]\n"
    "v_cvt_pk_bf16_f32 v196, v32, v33\n"
    "v_cvt_pk_bf16_f32 v197, v34, v35\n"
    "v_pk_mul_f32 v[48:49], v[48:49], v[128:129]\n"
    "v_pk_mul_f32 v[50:51], v[50:51], v[128:129]\n"
    "v_cvt_pk_bf16_f32 v198, v48, v49\n"
    "v_cvt_pk_bf16_f32 v199, v50, v51\n"
    "v_accvgpr_write_b32 a36, v196\n"
    "v_accvgpr_write_b32 a37, v197\n"
    "v_accvgpr_write_b32 a38, v198\n"
    "v_accvgpr_write_b32 a39, v199\n"
    "v_pk_mul_f32 v[64:65], v[64:65], v[128:129]\n"
    "v_pk_mul_f32 v[66:67], v[66:67], v[128:129]\n"
    "v_cvt_pk_bf16_f32 v200, v64, v65\n"
    "v_cvt_pk_bf16_f32 v201, v66, v67\n"
    "v_pk_mul_f32 v[80:81], v[80:81], v[128:129]\n"
    "v_pk_mul_f32 v[82:83], v[82:83], v[128:129]\n"
    "v_cvt_pk_bf16_f32 v202, v80, v81\n"
    "v_cvt_pk_bf16_f32 v203, v82, v83\n"
    "v_accvgpr_write_b32 a40, v200\n"
    "v_accvgpr_write_b32 a41, v201\n"
    "v_accvgpr_write_b32 a42, v202\n"
    "v_accvgpr_write_b32 a43, v203\n"
    "v_pk_mul_f32 v[96:97], v[96:97], v[128:129]\n"
    "v_pk_mul_f32 v[98:99], v[98:99], v[128:129]\n"
    "v_cvt_pk_bf16_f32 v204, v96, v97\n"
    "v_cvt_pk_bf16_f32 v205, v98, v99\n"
    "v_pk_mul_f32 v[112:113], v[112:113], v[128:129]\n"
    "v_pk_mul_f32 v[114:115], v[114:115], v[128:129]\n"
    "v_cvt_pk_bf16_f32 v206, v112, v113\n"
    "v_cvt_pk_bf16_f32 v207, v114, v115\n"
    "v_accvgpr_write_b32 a44, v204\n"
    "v_accvgpr_write_b32 a45, v205\n"
    "v_accvgpr_write_b32 a46, v206\n"
    "v_accvgpr_write_b32 a47, v207\n"
    "v_pk_mul_f32 v[4:5], v[4:5], v[128:129]\n"
    "v_pk_mul_f32 v[6:7], v[6:7], v[128:129]\n"
    "v_cvt_pk_bf16_f32 v208, v4, v5\n"
    "v_cvt_pk_bf16_f32 v209, v6, v7\n"
    "v_pk_mul_f32 v[20:21], v[20:21], v[128:129]\n"
    "v_pk_mul_f32 v[22:23], v[22:23], v[128:129]\n"
    "v_cvt_pk_bf16_f32 v210, v20, v21\n"
    "v_cvt_pk_bf16_f32 v211, v22, v23\n"
    "v_accvgpr_write_b32 a48, v208\n"
    "v_accvgpr_write_b32 a49, v209\n"
    "v_accvgpr_write_b32 a50, v210\n"
    "v_accvgpr_write_b32 a51, v211\n"
    "v_pk_mul_f32 v[36:37], v[36:37], v[128:129]\n"
    "v_pk_mul_f32 v[38:39], v[38:39], v[128:129]\n"
    "v_cvt_pk_bf16_f32 v212, v36, v37\n"
    "v_cvt_pk_bf16_f32 v213, v38, v39\n"
    "v_pk_mul_f32 v[52:53], v[52:53], v[128:129]\n"
    "v_pk_mul_f32 v[54:55], v[54:55], v[128:129]\n"
    "v_cvt_pk_bf16_f32 v214, v52, v53\n"
    "v_cvt_pk_bf16_f32 v215, v54, v55\n"
    "v_accvgpr_write_b32 a52, v212\n"
    "v_accvgpr_write_b32 a53, v213\n"
    "v_accvgpr_write_b32 a54, v214\n"
    "v_accvgpr_write_b32 a55, v215\n"
    "v_pk_mul_f32 v[68:69], v[68:69], v[128:129]\n"
    "v_pk_mul_f32 v[70:71], v[70:71], v[128:129]\n"
    "v_cvt_pk_bf16_f32 v216, v68, v69\n"
    "v_cvt_pk_bf16_f32 v217, v70, v71\n"
    "v_pk_mul_f32 v[84:85], v[84:85], v[128:129]\n"
    "v_pk_mul_f32 v[86:87], v[86:87], v[128:129]\n"
    "v_cvt_pk_bf16_f32 v218, v84, v85\n"
    "v_cvt_pk_bf16_f32 v219, v86, v87\n"
    "v_accvgpr_write_b32 a56, v216\n"
    "v_accvgpr_write_b32 a57, v217\n"
    "v_accvgpr_write_b32 a58, v218\n"
    "v_accvgpr_write_b32 a59, v219\n"
    "v_pk_mul_f32 v[100:101], v[100:101], v[128:129]\n"
    "v_pk_mul_f32 v[102:103], v[102:103], v[128:129]\n"
    "v_cvt_pk_bf16_f32 v220, v100, v101\n"
    "v_cvt_pk_bf16_f32 v221, v102, v103\n"
    "v_pk_mul_f32 v[116:117], v[116:117], v[128:129]\n"
    "v_pk_mul_f32 v[118:119], v[118:119], v[128:129]\n"
    "v_cvt_pk_bf16_f32 v222, v116, v117\n"
    "v_cvt_pk_bf16_f32 v223, v118, v119\n"
    "v_accvgpr_write_b32 a60, v220\n"
    "v_accvgpr_write_b32 a61, v221\n"
    "v_accvgpr_write_b32 a62, v222\n"
    "v_accvgpr_write_b32 a63, v223\n"
    "v_pk_mul_f32 v[8:9], v[8:9], v[128:129]\n"
    "v_pk_mul_f32 v[10:11], v[10:11], v[128:129]\n"
    "v_cvt_pk_bf16_f32 v192, v8, v9\n"
    "v_cvt_pk_bf16_f32 v193, v10, v11\n"
    "v_pk_mul_f32 v[24:25], v[24:25], v[128:129]\n"
    "v_pk_mul_f32 v[26:27], v[26:27], v[128:129]\n"
    "v_cvt_pk_bf16_f32 v194, v24, v25\n"
    "v_cvt_pk_bf16_f32 v195, v26, v27\n"
    "v_accvgpr_write_b32 a0, v192\n"
    "v_accvgpr_write_b32 a1, v193\n"
    "v_accvgpr_write_b32 a2, v194\n"
    "v_accvgpr_write_b32 a3, v195\n"
    "v_pk_mul_f32 v[40:41], v[40:41], v[128:129]\n"
    "v_pk_mul_f32 v[42:43], v[42:43], v[128:129]\n"
    "v_cvt_pk_bf16_f32 v196, v40, v41\n"
    "v_cvt_pk_bf16_f32 v197, v42, v43\n"
    "v_pk_mul_f32 v[56:57], v[56:57], v[128:129]\n"
    "v_pk_mul_f32 v[58:59], v[58:59], v[128:129]\n"
    "v_cvt_pk_bf16_f32 v198, v56, v57\n"
    "v_cvt_pk_bf16_f32 v199, v58, v59\n"
    "v_accvgpr_write_b32 a4, v196\n"
    "v_accvgpr_write_b32 a5, v197\n"
    "v_accvgpr_write_b32 a6, v198\n"
    "v_accvgpr_write_b32 a7, v199\n"
    "v_pk_mul_f32 v[72:73], v[72:73], v[128:129]\n"
    "v_pk_mul_f32 v[74:75], v[74:75], v[128:129]\n"
    "v_cvt_pk_bf16_f32 v200, v72, v73\n"
    "v_cvt_pk_bf16_f32 v201, v74, v75\n"
    "v_pk_mul_f32 v[88:89], v[88:89], v[128:129]\n"
    "v_pk_mul_f32 v[90:91], v[90:91], v[128:129]\n"
    "v_cvt_pk_bf16_f32 v202, v88, v89\n"
    "v_cvt_pk_bf16_f32 v203, v90, v91\n"
    "v_accvgpr_write_b32 a8, v200\n"
    "v_accvgpr_write_b32 a9, v201\n"
    "v_accvgpr_write_b32 a10, v202\n"
    "v_accvgpr_write_b32 a11, v203\n"
    "v_pk_mul_f32 v[104:105], v[104:105], v[128:129]\n"
    "v_pk_mul_f32 v[106:107], v[106:107], v[128:129]\n"
    "v_cvt_pk_bf16_f32 v204, v104, v105\n"
    "v_cvt_pk_bf16_f32 v205, v106, v107\n"
    "v_pk_mul_f32 v[120:121], v[120:121], v[128:129]\n"
    "v_pk_mul_f32 v[122:123], v[122:123], v[128:129]\n"
    "v_cvt_pk_bf16_f32 v206, v120, v121\n"
    "v_cvt_pk_bf16_f32 v207, v122, v123\n"
    "v_accvgpr_write_b32 a12, v204\n"
    "v_accvgpr_write_b32 a13, v205\n"
    "v_accvgpr_write_b32 a14, v206\n"
    "v_accvgpr_write_b32 a15, v207\n"
    "v_pk_mul_f32 v[12:13], v[12:13], v[128:129]\n"
    "v_pk_mul_f32 v[14:15], v[14:15], v[128:129]\n"
    "v_cvt_pk_bf16_f32 v208, v12, v13\n"
    "v_cvt_pk_bf16_f32 v209, v14, v15\n"
    "v_pk_mul_f32 v[28:29], v[28:29], v[128:129]\n"
    "v_pk_mul_f32 v[30:31], v[30:31], v[128:129]\n"
    "v_cvt_pk_bf16_f32 v210, v28, v29\n"
    "v_cvt_pk_bf16_f32 v211, v30, v31\n"
    "v_accvgpr_write_b32 a16, v208\n"
    "v_accvgpr_write_b32 a17, v209\n"
    "v_accvgpr_write_b32 a18, v210\n"
    "v_accvgpr_write_b32 a19, v211\n"
    "v_pk_mul_f32 v[44:45], v[44:45], v[128:129]\n"
    "v_pk_mul_f32 v[46:47], v[46:47], v[128:129]\n"
    "v_cvt_pk_bf16_f32 v212, v44, v45\n"
    "v_cvt_pk_bf16_f32 v213, v46, v47\n"
    "v_pk_mul_f32 v[60:61], v[60:61], v[128:129]\n"
    "v_pk_mul_f32 v[62:63], v[62:63], v[128:129]\n"
    "v_cvt_pk_bf16_f32 v214, v60, v61\n"
    "v_cvt_pk_bf16_f32 v215, v62, v63\n"
    "v_accvgpr_write_b32 a20, v212\n"
    "v_accvgpr_write_b32 a21, v213\n"
    "v_accvgpr_write_b32 a22, v214\n"
    "v_accvgpr_write_b32 a23, v215\n"
    "v_pk_mul_f32 v[76:77], v[76:77], v[128:129]\n"
    "v_pk_mul_f32 v[78:79], v[78:79], v[128:129]\n"
    "v_cvt_pk_bf16_f32 v216, v76, v77\n"
    "v_cvt_pk_bf16_f32 v217, v78, v79\n"
    "v_pk_mul_f32 v[92:93], v[92:93], v[128:129]\n"
    "v_pk_mul_f32 v[94:95], v[94:95], v[128:129]\n"
    "v_cvt_pk_bf16_f32 v218, v92, v93\n"
    "v_cvt_pk_bf16_f32 v219, v94, v95\n"
    "v_accvgpr_write_b32 a24, v216\n"
    "v_accvgpr_write_b32 a25, v217\n"
    "v_accvgpr_write_b32 a26, v218\n"
    "v_accvgpr_write_b32 a27, v219\n"
    "v_pk_mul_f32 v[108:109], v[108:109], v[128:129]\n"
    "v_pk_mul_f32 v[110:111], v[110:111], v[128:129]\n"
    "v_cvt_pk_bf16_f32 v220, v108, v109\n"
    "v_cvt_pk_bf16_f32 v221, v110, v111\n"
    "v_pk_mul_f32 v[124:125], v[124:125], v[128:129]\n"
    "v_pk_mul_f32 v[126:127], v[126:127], v[128:129]\n"
    "v_cvt_pk_bf16_f32 v222, v124, v125\n"
    "v_cvt_pk_bf16_f32 v223, v126, v127\n"
    "v_accvgpr_write_b32 a28, v220\n"
    "v_accvgpr_write_b32 a29, v221\n"
    "v_accvgpr_write_b32 a30, v222\n"
    "v_accvgpr_write_b32 a31, v223\n"
    "v_mov_b32 v228, v244\n"
    "v_add_u32 v229, 0x4000, v244\n"
    "v_add_u32 v230, 0x1c000, v244\n"
    "v_mov_b32 v231, v244\n"
    "v_mov_b32 v232, v245\n"
    "v_mov_b32 v80, 0x3f803f80\n"
    "v_mov_b32 v81, 0x3f803f80\n"
    "v_mov_b32 v82, 0x3f803f80\n"
    "v_mov_b32 v83, 0x3f803f80\n"
    "v_mov_b32 v233, 0xff800000\n"
    "v_mov_b32 v240, 0x4f800000\n"
    "v_add_u32 v241, -64, v245\n"
    "v_mov_b32 v236, 0xffff\n"
    "v_mov_b32 v237, 0x10000\n"
    "s_mov_b32 s55, 0\n"
    "s_mov_b32 s76, 0\n"
    "s_mov_b32 s50, s49\n"
    "s_mov_b32 s62, s93\n"
    "s_add_u32 s56, s51, 49152\n"
    "s_add_u32 s57, s51, 32768\n"
    "s_add_u32 s60, s51, 49152\n"
    "s_add_u32 s61, s51, 32768\n"
    "s_lshl_b32 s58, s49, 14\n"
    "s_add_u32 s58, s58, s51\n"
    "s_lshl_b32 s48, s77, 14\n"
    "v_accvgpr_read_b32 v84, a32\n"
    "v_accvgpr_read_b32 v85, a33\n"
    "v_accvgpr_read_b32 v86, a34\n"
    "v_accvgpr_read_b32 v87, a35\n"
    "v_accvgpr_read_b32 v88, a36\n"
    "v_accvgpr_read_b32 v89, a37\n"
    "v_accvgpr_read_b32 v90, a38\n"
    "v_accvgpr_read_b32 v91, a39\n"
    "v_accvgpr_read_b32 v92, a40\n"
    "v_accvgpr_read_b32 v93, a41\n"
    "v_accvgpr_read_b32 v94, a42\n"
    "v_accvgpr_read_b32 v95, a43\n"
    "v_accvgpr_read_b32 v96, a44\n"
    "v_accvgpr_read_b32 v97, a45\n"
    "v_accvgpr_read_b32 v98, a46\n"
    "v_accvgpr_read_b32 v99, a47\n"
    "v_accvgpr_read_b32 v100, a48\n"
    "v_accvgpr_read_b32 v101, a49\n"
    "v_accvgpr_read_b32 v102, a50\n"
    "v_accvgpr_read_b32 v103, a51\n"
    "v_accvgpr_read_b32 v104, a52\n"
    "v_accvgpr_read_b32 v105, a53\n"
    "v_accvgpr_read_b32 v106, a54\n"
    "v_accvgpr_read_b32 v107, a55\n"
    "v_accvgpr_read_b32 v108, a56\n"
    "v_accvgpr_read_b32 v109, a57\n"
    "v_accvgpr_read_b32 v110, a58\n"
    "v_accvgpr_read_b32 v111, a59\n"
    "v_accvgpr_read_b32 v112, a60\n"
    "v_accvgpr_read_b32 v113, a61\n"
    "v_accvgpr_read_b32 v114, a62\n"
    "v_accvgpr_read_b32 v115, a63\n"
    "s_waitcnt vmcnt(25)\n"
    "s_barrier\n"
    "LJ_JOB%=:\n"
    "ds_read_b128 v[196:199], v228 offset:0\n"
    "ds_read_b128 v[200:203], v228 offset:4096\n"
    "ds_read_b128 v[204:207], v228 offset:8192\n"
    "ds_read_b128 v[208:211], v228 offset:12288\n"
    "ds_read_b128 v[212:215], v228 offset:1024\n"
    "ds_read_b128 v[216:219], v228 offset:5120\n"
    "s_waitcnt lgkmcnt(5)\n"
    "v_mfma_f32_16x16x32_bf16 v[116:119], v[196:199], v[84:87], 0\n"
    "s_mov_b32 m0, s60\n"
    "s_nop 0\n"
    "buffer_load_dwordx4 v231, s[40:43], s56 offen lds\n"
    "ds_read_b128 v[220:223], v228 offset:9216\n"
    "v_mfma_f32_16x16x32_bf16 v[120:123], v[196:199], v[100:103], 0\n"
    "v_mov_b32 v0, 0\n"
    "v_mov_b32 v1, 0\n"
    "s_waitcnt lgkmcnt(5)\n"
    "v_mfma_f32_16x16x32_bf16 v[124:127], v[200:203], v[84:87], 0\n"
    "buffer_load_dwordx4 v231, s[40:43], s56 offen offset:1024 lds\n"
    "ds_read_b128 v[224:227], v228 offset:13312\n"
    "v_mfma_f32_16x16x32_bf16 v[128:131], v[200:203], v[100:103], 0\n"
    "v_mov_b32 v2, 0\n"
    "v_mov_b32 v3, 0\n"
    "s_waitcnt lgkmcnt(5)\n"
    "v_mfma_f32_16x16x32_bf16 v[132:135], v[204:207], v[84:87], 0\n"
    "buffer_load_dwordx4 v231, s[40:43], s56 offen offset:2048 lds\n"
    "ds_read_b128 v[196:199], v228 offset:2048\n"
    "v_mfma_f32_16x16x32_bf16 v[136:139], v[204:207], v[100:103], 0\n"
    "v_mov_b32 v4, 0\n"
    "v_mov_b32 v5, 0\n"
    "s_waitcnt lgkmcnt(5)\n"
    "v_mfma_f32_16x16x32_bf16 v[140:143], v[208:211], v[84:87], 0\n"
    "buffer_load_dwordx4 v231, s[40:43], s56 offen offset:3072 lds\n"
    "ds_read_b128 v[200:203], v228 offset:6144\n"
    "v_mfma_f32_16x16x32_bf16 v[144:147], v[208:211], v[100:103], 0\n"
    "v_mov_b32 v6, 0\n"
    "v_mov_b32 v7, 0\n"
    "s_waitcnt lgkmcnt(5)\n"
    "v_mfma_f32_16x16x32_bf16 v[116:119], v[212:215], v[88:91], v[116:119]\n"
    "s_or_b32 m0, s61, 0x10000\n"
    "s_nop 0\n"
    "buffer_load_dwordx4 v231, s[44:47], s57 offen lds\n"
    "ds_read_b128 v[204:207], v228 offset:10240\n"
    "v_mfma_f32_16x16x32_bf16 v[120:123], v[212:215], v[104:107], v[120:123]\n"
    "v_mov_b32 v8, 0\n"
    "v_mov_b32 v9, 0\n"
    "s_waitcnt lgkmcnt(5)\n"
    "v_mfma_f32_16x16x32_bf16 v[124:127], v[216:219], v[88:91], v[124:127]\n"
    "buffer_load_dwordx4 v231, s[44:47], s57 offen offset:1024 lds\n"
    "ds_read_b128 v[208:211], v228 offset:14336\n"
    "v_mfma_f32_16x16x32_bf16 v[128:131], v[216:219], v[104:107], v[128:131]\n"
    "v_mov_b32 v10, 0\n"
    "v_mov_b32 v11, 0\n"
    "s_waitcnt lgkmcnt(5)\n"
    "v_mfma_f32_16x16x32_bf16 v[132:135], v[220:223], v[88:91], v[132:135]\n"
    "buffer_load_dwordx4 v231, s[44:47], s57 offen offset:2048 lds\n"
    "ds_read_b128 v[212:215], v228 offset:3072\n"
    "v_mfma_f32_16x16x32_bf16 v[136:139], v[220:223], v[104:107], v[136:139]\n"
    "v_mov_b32 v12, 0\n"
    "v_mov_b32 v13, 0\n"
    "s_waitcnt lgkmcnt(5)\n"
    "v_mfma_f32_16x16x32_bf16 v[140:143], v[224:227], v[88:91], v[140:143]\n"
    "buffer_load_dwordx4 v231, s[44:47], s57 offen offset:3072 lds\n"
    "ds_read_b128 v[216:219], v228 offset:7168\n"
    "v_mfma_f32_16x16x32_bf16 v[144:147], v[224:227], v[104:107], v[144:147]\n"
    "v_mov_b32 v14, 0\n"
    "v_mov_b32 v15, 0\n"
    "s_waitcnt lgkmcnt(5)\n"
    "v_mfma_f32_16x16x32_bf16 v[116:119], v[196:199], v[92:95], v[116:119]\n"
    "ds_read_b128 v[220:223], v228 offset:11264\n"
    "v_mov_b32 v16, 0\n"
    "v_mfma_f32_16x16x32_bf16 v[120:123], v[196:199], v[108:111], v[120:123]\n"
    "v_mov_b32 v17, 0\n"
    "v_mov_b32 v18, 0\n"
    "s_waitcnt lgkmcnt(5)\n"
    "v_mfma_f32_16x16x32_bf16 v[124:127], v[200:203], v[92:95], v[124:127]\n"
    "ds_read_b128 v[224:227], v228 offset:15360\n"
    "v_mov_b32 v19, 0\n"
    "v_mfma_f32_16x16x32_bf16 v[128:131], v[200:203], v[108:111], v[128:131]\n"
    "v_mov_b32 v20, 0\n"
    "v_mov_b32 v21, 0\n"
    "s_waitcnt lgkmcnt(5)\n"
    "v_mfma_f32_16x16x32_bf16 v[132:135], v[204:207], v[92:95], v[132:135]\n"
    "v_mov_b32 v22, 0\n"
    "v_mfma_f32_16x16x32_bf16 v[136:139], v[204:207], v[108:111], v[136:139]\n"
    "v_mov_b32 v23, 0\n"
    "v_mov_b32 v24, 0\n"
    "s_waitcnt lgkmcnt(4)\n"
    "v_mfma_f32_16x16x32_bf16 v[140:143], v[208:211], v[92:95], v[140:143]\n"
    "v_mov_b32 v25, 0\n"
    "v_mfma_f32_16x16x32_bf16 v[144:147], v[208:211], v[108:111], v[144:147]\n"
    "v_mov_b32 v26, 0\n"
    "v_mov_b32 v27, 0\n"
    "s_waitcnt lgkmcnt(3)\n"
    "v_mfma_f32_16x16x32_bf16 v[116:119], v[212:215], v[96:99], v[116:119]\n"
    "v_mov_b32 v28, 0\n"
    "v_mfma_f32_16x16x32_bf16 v[120:123], v[212:215], v[112:115], v[120:123]\n"
    "v_mov_b32 v29, 0\n"
    "v_mov_b32 v30, 0\n"
    "s_waitcnt lgkmcnt(2)\n"
    "v_mfma_f32_16x16x32_bf16 v[124:127], v[216:219], v[96:99], v[124:127]\n"
    "v_mov_b32 v31, 0\n"
    "v_mfma_f32_16x16x32_bf16 v[128:131], v[216:219], v[112:115], v[128:131]\n"
    "v_mov_b32 v32, 0\n"
    "v_mov_b32 v33, 0\n"
    "s_waitcnt lgkmcnt(1)\n"
    "v_mfma_f32_16x16x32_bf16 v[132:135], v[220:223], v[96:99], v[132:135]\n"
    "v_mov_b32 v34, 0\n"
    "v_mfma_f32_16x16x32_bf16 v[136:139], v[220:223], v[112:115], v[136:139]\n"
    "v_mov_b32 v35, 0\n"
    "v_mov_b32 v36, 0\n"
    "s_waitcnt lgkmcnt(0)\n"
    "v_mfma_f32_16x16x32_bf16 v[140:143], v[224:227], v[96:99], v[140:143]\n"
    "v_mov_b32 v37, 0\n"
    "v_mfma_f32_16x16x32_bf16 v[144:147], v[224:227], v[112:115], v[144:147]\n"
    "v_mov_b32 v38, 0\n"
    "v_mov_b32 v39, 0\n"
    "v_mov_b32 v40, 0\n"
    "v_mov_b32 v41, 0\n"
    "v_mov_b32 v42, 0\n"
    "v_mov_b32 v43, 0\n"
    "v_mov_b32 v44, 0\n"
    "v_mov_b32 v45, 0\n"
    "v_mov_b32 v46, 0\n"
    "v_mov_b32 v47, 0\n"
    "v_mov_b32 v48, 0\n"
    "v_mov_b32 v49, 0\n"
    "v_mov_b32 v50, 0\n"
    "v_mov_b32 v51, 0\n"
    "v_mov_b32 v52, 0\n"
    "v_mov_b32 v53, 0\n"
    "v_mov_b32 v54, 0\n"
    "v_mov_b32 v55, 0\n"
    "v_mov_b32 v56, 0\n"
    "v_mov_b32 v57, 0\n"
    "v_mov_b32 v58, 0\n"
    "v_mov_b32 v59, 0\n"
    "v_mov_b32 v60, 0\n"
    "v_mov_b32 v61, 0\n"
    "v_mov_b32 v62, 0\n"
    "v_mov_b32 v63, 0\n"
    "v_mov_b32 v64, 0\n"
    "v_mov_b32 v65, 0\n"
    "v_mov_b32 v66, 0\n"
    "v_mov_b32 v67, 0\n"
    "v_mov_b32 v68, 0\n"
    "v_mov_b32 v69, 0\n"
    "v_mov_b32 v70, 0\n"
    "v_mov_b32 v71, 0\n"
    "s_add_u32 s56, s56, 16384\n"
    "s_cmp_eq_u32 s56, s58\n"
    "s_cselect_b32 s56, s51, s56\n"
    "s_cselect_b32 s42, s48, s42\n"
    "s_add_u32 s60, s60, 16384\n"
    "s_and_b32 s60, s60, 0xffff\n"
    "s_add_u32 s57, s57, 16384\n"
    "s_cmp_eq_u32 s57, s58\n"
    "s_cselect_b32 s57, s51, s57\n"
    "s_cselect_b32 s46, s48, s46\n"
    "s_add_u32 s61, s61, 16384\n"
    "s_and_b32 s61, s61, 0xffff\n"
    "s_cmp_eq_u32 s50, 2\n"
    "s_cbranch_scc0 LJ_FMASK%=\n"
    "s_nop 7\n"
    "v_cmp_gt_i32_e64 s[64:65], 0, v232\n"
    "v_cmp_gt_i32_e64 s[66:67], 1, v232\n"
    "v_cmp_gt_i32_e64 s[68:69], 2, v232\n"
    "v_cmp_gt_i32_e64 s[70:71], 3, v232\n"
    "v_cndmask_b32_e64 v116, v116, v233, s[64:65]\n"
    "v_cndmask_b32_e64 v117, v117, v233, s[66:67]\n"
    "v_cndmask_b32_e64 v118, v118, v233, s[68:69]\n"
    "v_cndmask_b32_e64 v119, v119, v233, s[70:71]\n"
    "v_cmp_gt_i32_e64 s[64:65], -16, v232\n"
    "v_cmp_gt_i32_e64 s[66:67], -15, v232\n"
    "v_cmp_gt_i32_e64 s[68:69], -14, v232\n"
    "v_cmp_gt_i32_e64 s[70:71], -13, v232\n"
    "v_cndmask_b32_e64 v120, v120, v233, s[64:65]\n"
    "v_cndmask_b32_e64 v121, v121, v233, s[66:67]\n"
    "v_cndmask_b32_e64 v122, v122, v233, s[68:69]\n"
    "v_cndmask_b32_e64 v123, v123, v233, s[70:71]\n"
    "v_cmp_gt_i32_e64 s[64:65], 16, v232\n"
    "v_cmp_gt_i32_e64 s[66:67], 17, v232\n"
    "v_cmp_gt_i32_e64 s[68:69], 18, v232\n"
    "v_cmp_gt_i32_e64 s[70:71], 19, v232\n"
    "v_cndmask_b32_e64 v124, v124, v233, s[64:65]\n"
    "v_cndmask_b32_e64 v125, v125, v233, s[66:67]\n"
    "v_cndmask_b32_e64 v126, v126, v233, s[68:69]\n"
    "v_cndmask_b32_e64 v127, v127, v233, s[70:71]\n"
    "v_cmp_gt_i32_e64 s[64:65], 0, v232\n"
    "v_cmp_gt_i32_e64 s[66:67], 1, v232\n"
    "v_cmp_gt_i32_e64 s[68:69], 2, v232\n"
    "v_cmp_gt_i32_e64 s[70:71], 3, v232\n"
    "v_cndmask_b32_e64 v128, v128, v233, s[64:65]\n"
    "v_cndmask_b32_e64 v129, v129, v233, s[66:67]\n"
    "v_cndmask_b32_e64 v130, v130, v233, s[68:69]\n"
    "v_cndmask_b32_e64 v131, v131, v233, s[70:71]\n"
    "v_cmp_gt_i32_e64 s[64:65], 32, v232\n"
    "v_cmp_gt_i32_e64 s[66:67], 33, v232\n"
    "v_cmp_gt_i32_e64 s[68:69], 34, v232\n"
    "v_cmp_gt_i32_e64 s[70:71], 35, v232\n"
    "v_cndmask_b32_e64 v132, v132, v233, s[64:65]\n"
    "v_cndmask_b32_e64 v133, v133, v233, s[66:67]\n"
    "v_cndmask_b32_e64 v134, v134, v233, s[68:69]\n"
    "v_cndmask_b32_e64 v135, v135, v233, s[70:71]\n"
    "v_cmp_gt_i32_e64 s[64:65], 16, v232\n"
    "v_cmp_gt_i32_e64 s[66:67], 17, v232\n"
    "v_cmp_gt_i32_e64 s[68:69], 18, v232\n"
    "v_cmp_gt_i32_e64 s[70:71], 19, v232\n"
    "v_cndmask_b32_e64 v136, v136, v233, s[64:65]\n"
    "v_cndmask_b32_e64 v137, v137, v233, s[66:67]\n"
    "v_cndmask_b32_e64 v138, v138, v233, s[68:69]\n"
    "v_cndmask_b32_e64 v139, v139, v233, s[70:71]\n"
    "v_cmp_gt_i32_e64 s[64:65], 48, v232\n"
    "v_cmp_gt_i32_e64 s[66:67], 49, v232\n"
    "v_cmp_gt_i32_e64 s[68:69], 50, v232\n"
    "v_cmp_gt_i32_e64 s[70:71], 51, v232\n"
    "v_cndmask_b32_e64 v140, v140, v233, s[64:65]\n"
    "v_cndmask_b32_e64 v141, v141, v233, s[66:67]\n"
    "v_cndmask_b32_e64 v142, v142, v233, s[68:69]\n"
    "v_cndmask_b32_e64 v143, v143, v233, s[70:71]\n"
    "v_cmp_gt_i32_e64 s[64:65], 32, v232\n"
    "v_cmp_gt_i32_e64 s[66:67], 33, v232\n"
    "v_cmp_gt_i32_e64 s[68:69], 34, v232\n"
    "v_cmp_gt_i32_e64 s[70:71], 35, v232\n"
    "v_cndmask_b32_e64 v144, v144, v233, s[64:65]\n"
    "v_cndmask_b32_e64 v145, v145, v233, s[66:67]\n"
    "v_cndmask_b32_e64 v146, v146, v233, s[68:69]\n"
    "v_cndmask_b32_e64 v147, v147, v233, s[70:71]\n"
    "LJ_FMASK%=:\n"
    "v_max3_f32 v238, v116, v117, v118\n"
    "v_max3_f32 v238, v238, v119, v124\n"
    "v_max3_f32 v238, v238, v125, v126\n"
    "v_max3_f32 v238, v238, v127, v132\n"
    "v_max3_f32 v238, v238, v133, v134\n"
    "v_max3_f32 v238, v238, v135, v140\n"
    "v_max3_f32 v238, v238, v141, v142\n"
    "v_max_f32 v238, v238, v143\n"
    "v_max3_f32 v239, v120, v121, v122\n"
    "v_max3_f32 v239, v239, v123, v128\n"
    "v_max3_f32 v239, v239, v129, v130\n"
    "v_max3_f32 v239, v239, v131, v136\n"
    "v_max3_f32 v239, v239, v137, v138\n"
    "v_max3_f32 v239, v239, v139, v144\n"
    "v_max3_f32 v239, v239, v145, v146\n"
    "v_max_f32 v239, v239, v147\n"
    "v_add_u32 v228, 0x4000, v229\n"
    "v_and_b32 v228, 0xffff, v228\n"
    "v_add_u32 v230, 0x4000, v230\n"
    "v_and_or_b32 v230, v230, v236, v237\n"
    "v_mov_b32 v234, v238\n"
    "v_mov_b32 v235, v239\n"
    "s_nop 0\n"
    "v_permlane16_swap_b32 v234, v238\n"
    "v_permlane16_swap_b32 v235, v239\n"
    "v_max_f32 v238, v238, v234\n"
    "v_max_f32 v239, v239, v235\n"
    "v_mov_b32 v234, v238\n"
    "v_mov_b32 v235, v239\n"
    "s_nop 0\n"
    "v_permlane32_swap_b32 v234, v238\n"
    "v_permlane32_swap_b32 v235, v239\n"
    "v_max_f32 v238, v238, v234\n"
    "v_max_f32 v239, v239, v235\n"
    "v_swap_b32 v228, v229\n"
    "s_waitcnt vmcnt(12)\n"
    "s_barrier\n"
    "s_and_b32 s59, s76, 1\n"
    "s_xor_b32 s59, s59, 1\n"
    "s_and_b32 s59, s59, s55\n"
    "s_cmp_eq_u32 s59, 1\n"
    "s_cbranch_scc0 LJ_PUB%=\n"
    "s_nop 7\n"
    "s_mov_b64 exec, 1\n"
    "v_mov_b32 v235, 1\n"
    "v_mov_b32 v234, s95\n"
    "global_atomic_add v234, v235, s[100:101]\n"
    "s_mov_b64 exec, -1\n"
    "LJ_PUB%=:\n"
    "ds_read_b128 v[196:199], v228 offset:0\n"
    "ds_read_b128 v[200:203], v228 offset:4096\n"
    "ds_read_b128 v[204:207], v228 offset:8192\n"
    "ds_read_b128 v[208:211], v228 offset:12288\n"
    "ds_read_b128 v[212:215], v228 offset:1024\n"
    "ds_read_b128 v[216:219], v228 offset:5120\n"
    "v_sub_f32 v72, 0, v238\n"
    "v_sub_f32 v73, 0, v238\n"
    "v_sub_f32 v74, 0, v238\n"
    "v_sub_f32 v75, 0, v238\n"
    "v_sub_f32 v116, v116, v238\n"
    "v_sub_f32 v117, v117, v238\n"
    "v_sub_f32 v118, v118, v238\n"
    "v_sub_f32 v119, v119, v238\n"
    "v_sub_f32 v124, v124, v238\n"
    "v_sub_f32 v125, v125, v238\n"
    "v_sub_f32 v126, v126, v238\n"
    "v_sub_f32 v127, v127, v238\n"
    "v_sub_f32 v132, v132, v238\n"
    "v_sub_f32 v133, v133, v238\n"
    "v_sub_f32 v134, v134, v238\n"
    "v_sub_f32 v135, v135, v238\n"
    "v_sub_f32 v140, v140, v238\n"
    "v_sub_f32 v141, v141, v238\n"
    "v_sub_f32 v142, v142, v238\n"
    "v_sub_f32 v143, v143, v238\n"
    "v_exp_f32 v116, v116\n"
    "v_exp_f32 v117, v117\n"
    "v_exp_f32 v118, v118\n"
    "v_exp_f32 v119, v119\n"
    "v_exp_f32 v124, v124\n"
    "v_exp_f32 v125, v125\n"
    "v_exp_f32 v126, v126\n"
    "v_exp_f32 v127, v127\n"
    "v_sub_f32 v76, 0, v239\n"
    "v_sub_f32 v77, 0, v239\n"
    "v_sub_f32 v78, 0, v239\n"
    "v_sub_f32 v79, 0, v239\n"
    "v_sub_f32 v120, v120, v239\n"
    "v_sub_f32 v121, v121, v239\n"
    "v_sub_f32 v122, v122, v239\n"
    "v_sub_f32 v123, v123, v239\n"
    "v_sub_f32 v128, v128, v239\n"
    "v_sub_f32 v129, v129, v239\n"
    "v_sub_f32 v130, v130, v239\n"
    "v_sub_f32 v131, v131, v239\n"
    "v_sub_f32 v136, v136, v239\n"
    "v_sub_f32 v137, v137, v239\n"
    "v_sub_f32 v138, v138, v239\n"
    "v_sub_f32 v139, v139, v239\n"
    "v_sub_f32 v144, v144, v239\n"
    "v_sub_f32 v145, v145, v239\n"
    "v_sub_f32 v146, v146, v239\n"
    "v_sub_f32 v147, v147, v239\n"
    "v_exp_f32 v120, v120\n"
    "v_exp_f32 v121, v121\n"
    "v_exp_f32 v122, v122\n"
    "v_exp_f32 v123, v123\n"
    "v_exp_f32 v128, v128\n"
    "v_exp_f32 v129, v129\n"
    "v_exp_f32 v130, v130\n"
    "v_exp_f32 v131, v131\n"
    "s_cmp_eq_u32 s50, 2\n"
    "s_cbranch_scc1 LJ_SHORT%=\n"
    "s_waitcnt lgkmcnt(5)\n"
    "v_mfma_f32_16x16x32_bf16 v[148:151], v[196:199], v[84:87], v[72:75]\n"
    "s_waitcnt lgkmcnt(3)\n"
    "ds_read_b128 v[220:223], v228 offset:9216\n"
    "v_cvt_pk_bf16_f32 v180, v116, v117\n"
    "v_mfma_f32_16x16x32_bf16 v[152:155], v[196:199], v[100:103], v[76:79]\n"
    "s_mov_b32 m0, s60\n"
    "v_exp_f32 v132, v132\n"
    "v_mfma_f32_16x16x32_bf16 v[156:159], v[200:203], v[84:87], v[72:75]\n"
    "buffer_load_dwordx4 v231, s[40:43], s56 offen lds\n"
    "ds_read_b128 v[224:227], v228 offset:13312\n"
    "v_mfma_f32_16x16x32_bf16 v[160:163], v[200:203], v[100:103], v[76:79]\n"
    "v_exp_f32 v133, v133\n"
    "v_mfma_f32_16x16x32_bf16 v[164:167], v[204:207], v[84:87], v[72:75]\n"
    "s_waitcnt lgkmcnt(3)\n"
    "ds_read_b128 v[196:199], v228 offset:2048\n"
    "v_cvt_pk_bf16_f32 v181, v118, v119\n"
    "v_mfma_f32_16x16x32_bf16 v[168:171], v[204:207], v[100:103], v[76:79]\n"
    "v_exp_f32 v134, v134\n"
    "v_mfma_f32_16x16x32_bf16 v[172:175], v[208:211], v[84:87], v[72:75]\n"
    "buffer_load_dwordx4 v231, s[40:43], s56 offen offset:1024 lds\n"
    "ds_read_b128 v[200:203], v228 offset:6144\n"
    "v_mfma_f32_16x16x32_bf16 v[176:179], v[208:211], v[100:103], v[76:79]\n"
    "v_exp_f32 v135, v135\n"
    "v_mfma_f32_16x16x32_bf16 v[148:151], v[212:215], v[88:91], v[148:151]\n"
    "s_waitcnt lgkmcnt(3)\n"
    "ds_read_b128 v[204:207], v228 offset:10240\n"
    "v_cvt_pk_bf16_f32 v182, v124, v125\n"
    "v_mfma_f32_16x16x32_bf16 v[152:155], v[212:215], v[104:107], v[152:155]\n"
    "v_exp_f32 v140, v140\n"
    "v_mfma_f32_16x16x32_bf16 v[156:159], v[216:219], v[88:91], v[156:159]\n"
    "buffer_load_dwordx4 v231, s[40:43], s56 offen offset:2048 lds\n"
    "ds_read_b128 v[208:211], v228 offset:14336\n"
    "v_mfma_f32_16x16x32_bf16 v[160:163], v[216:219], v[104:107], v[160:163]\n"
    "v_exp_f32 v141, v141\n"
    "v_mfma_f32_16x16x32_bf16 v[164:167], v[220:223], v[88:91], v[164:167]\n"
    "s_waitcnt lgkmcnt(3)\n"
    "ds_read_b128 v[212:215], v228 offset:3072\n"
    "v_cvt_pk_bf16_f32 v183, v126, v127\n"
    "v_mfma_f32_16x16x32_bf16 v[168:171], v[220:223], v[104:107], v[168:171]\n"
    "v_exp_f32 v142, v142\n"
    "v_mfma_f32_16x16x32_bf16 v[172:175], v[224:227], v[88:91], v[172:175]\n"
    "buffer_load_dwordx4 v231, s[40:43], s56 offen offset:3072 lds\n"
    "ds_read_b128 v[216:219], v228 offset:7168\n"
    "v_mfma_f32_16x16x32_bf16 v[176:179], v[224:227], v[104:107], v[176:179]\n"
    "v_exp_f32 v143, v143\n"
    "v_mfma_f32_16x16x32_bf16 v[148:151], v[196:199], v[92:95], v[148:151]\n"
    "s_waitcnt lgkmcnt(3)\n"
    "ds_read_b128 v[220:223], v228 offset:11264\n"
    "v_cvt_pk_bf16_f32 v184, v120, v121\n"
    "v_mfma_f32_16x16x32_bf16 v[152:155], v[196:199], v[108:111], v[152:155]\n"
    "s_add_u32 s56, s56, 16384\n"
    "v_exp_f32 v136, v136\n"
    "v_mfma_f32_16x16x32_bf16 v[156:159], v[200:203], v[92:95], v[156:159]\n"
    "ds_read_b128 v[224:227], v228 offset:15360\n"
    "v_cvt_pk_bf16_f32 v185, v122, v123\n"
    "v_mfma_f32_16x16x32_bf16 v[160:163], v[200:203], v[108:111], v[160:163]\n"
    "s_cmp_eq_u32 s56, s58\n"
    "v_exp_f32 v137, v137\n"
    "v_mfma_f32_16x16x32_bf16 v[164:167], v[204:207], v[92:95], v[164:167]\n"
    "s_waitcnt lgkmcnt(3)\n"
    "ds_read_b128 v[196:199], v230 offset:0\n"
    "v_cvt_pk_bf16_f32 v186, v128, v129\n"
    "v_mfma_f32_16x16x32_bf16 v[168:171], v[204:207], v[108:111], v[168:171]\n"
    "s_cselect_b32 s56, s51, s56\n"
    "v_exp_f32 v138, v138\n"
    "v_mfma_f32_16x16x32_bf16 v[172:175], v[208:211], v[92:95], v[172:175]\n"
    "ds_read_b128 v[200:203], v230 offset:1024\n"
    "v_cvt_pk_bf16_f32 v187, v130, v131\n"
    "v_mfma_f32_16x16x32_bf16 v[176:179], v[208:211], v[108:111], v[176:179]\n"
    "s_cselect_b32 s42, s48, s42\n"
    "v_exp_f32 v139, v139\n"
    "v_mfma_f32_16x16x32_bf16 v[148:151], v[212:215], v[96:99], v[148:151]\n"
    "s_waitcnt lgkmcnt(3)\n"
    "ds_read_b128 v[204:207], v230 offset:2048\n"
    "v_cvt_pk_bf16_f32 v188, v132, v133\n"
    "v_mfma_f32_16x16x32_bf16 v[152:155], v[212:215], v[112:115], v[152:155]\n"
    "s_add_u32 s60, s60, 16384\n"
    "v_exp_f32 v144, v144\n"
    "v_mfma_f32_16x16x32_bf16 v[156:159], v[216:219], v[96:99], v[156:159]\n"
    "ds_read_b128 v[208:211], v230 offset:3072\n"
    "v_cvt_pk_bf16_f32 v189, v134, v135\n"
    "v_mfma_f32_16x16x32_bf16 v[160:163], v[216:219], v[112:115], v[160:163]\n"
    "s_and_b32 s60, s60, 0xffff\n"
    "v_exp_f32 v145, v145\n"
    "v_mfma_f32_16x16x32_bf16 v[164:167], v[220:223], v[96:99], v[164:167]\n"
    "s_waitcnt lgkmcnt(3)\n"
    "ds_read_b128 v[212:215], v230 offset:4096\n"
    "v_cvt_pk_bf16_f32 v190, v140, v141\n"
    "v_mfma_f32_16x16x32_bf16 v[168:171], v[220:223], v[112:115], v[168:171]\n"
    "v_exp_f32 v146, v146\n"
    "v_mfma_f32_16x16x32_bf16 v[172:175], v[224:227], v[96:99], v[172:175]\n"
    "ds_read_b128 v[216:219], v230 offset:5120\n"
    "v_cvt_pk_bf16_f32 v191, v142, v143\n"
    "v_mfma_f32_16x16x32_bf16 v[176:179], v[224:227], v[112:115], v[176:179]\n"
    "s_or_b32 m0, s61, 0x10000\n"
    "v_exp_f32 v147, v147\n"
    "v_mfma_f32_16x16x32_bf16 v[0:3], v[196:199], v[180:183], v[0:3]\n"
    "s_waitcnt lgkmcnt(3)\n"
    "ds_read_b128 v[220:223], v230 offset:6144\n"
    "v_cvt_pk_bf16_f32 v192, v136, v137\n"
    "v_mfma_f32_16x16x32_bf16 v[4:7], v[196:199], v[184:187], v[4:7]\n"
    "v_exp_f32 v148, v148\n"
    "v_mfma_f32_16x16x32_bf16 v[8:11], v[200:203], v[180:183], v[8:11]\n"
    "buffer_load_dwordx4 v231, s[44:47], s57 offen lds\n"
    "ds_read_b128 v[224:227], v230 offset:7168\n"
    "v_mfma_f32_16x16x32_bf16 v[12:15], v[200:203], v[184:187], v[12:15]\n"
    "v_exp_f32 v149, v149\n"
    "v_mfma_f32_16x16x32_bf16 v[16:19], v[204:207], v[180:183], v[16:19]\n"
    "s_waitcnt lgkmcnt(3)\n"
    "ds_read_b128 v[196:199], v230 offset:8192\n"
    "v_cvt_pk_bf16_f32 v193, v138, v139\n"
    "v_mfma_f32_16x16x32_bf16 v[20:23], v[204:207], v[184:187], v[20:23]\n"
    "v_exp_f32 v150, v150\n"
    "v_mfma_f32_16x16x32_bf16 v[64:67], v[80:83], v[180:183], v[64:67]\n"
    "v_add_u32 v228, 0x4000, v229\n"
    "v_and_b32 v228, 0xffff, v228\n"
    "v_mfma_f32_16x16x32_bf16 v[68:71], v[80:83], v[184:187], v[68:71]\n"
    "v_mfma_f32_16x16x32_bf16 v[24:27], v[208:211], v[180:183], v[24:27]\n"
    "buffer_load_dwordx4 v231, s[44:47], s57 offen offset:1024 lds\n"
    "ds_read_b128 v[200:203], v230 offset:9216\n"
    "v_mfma_f32_16x16x32_bf16 v[28:31], v[208:211], v[184:187], v[28:31]\n"
    "v_exp_f32 v151, v151\n"
    "v_mfma_f32_16x16x32_bf16 v[32:35], v[212:215], v[180:183], v[32:35]\n"
    "s_waitcnt lgkmcnt(3)\n"
    "ds_read_b128 v[204:207], v230 offset:10240\n"
    "v_cvt_pk_bf16_f32 v194, v144, v145\n"
    "v_mfma_f32_16x16x32_bf16 v[36:39], v[212:215], v[184:187], v[36:39]\n"
    "v_exp_f32 v156, v156\n"
    "v_mfma_f32_16x16x32_bf16 v[40:43], v[216:219], v[180:183], v[40:43]\n"
    "buffer_load_dwordx4 v231, s[44:47], s57 offen offset:2048 lds\n"
    "ds_read_b128 v[208:211], v230 offset:11264\n"
    "v_mfma_f32_16x16x32_bf16 v[44:47], v[216:219], v[184:187], v[44:47]\n"
    "v_exp_f32 v157, v157\n"
    "v_mfma_f32_16x16x32_bf16 v[48:51], v[220:223], v[180:183], v[48:51]\n"
    "s_waitcnt lgkmcnt(3)\n"
    "ds_read_b128 v[212:215], v230 offset:12288\n"
    "v_cvt_pk_bf16_f32 v195, v146, v147\n"
    "v_mfma_f32_16x16x32_bf16 v[52:55], v[220:223], v[184:187], v[52:55]\n"
    "v_exp_f32 v158, v158\n"
    "v_mfma_f32_16x16x32_bf16 v[56:59], v[224:227], v[180:183], v[56:59]\n"
    "buffer_load_dwordx4 v231, s[44:47], s57 offen offset:3072 lds\n"
    "ds_read_b128 v[216:219], v230 offset:13312\n"
    "v_mfma_f32_16x16x32_bf16 v[60:63], v[224:227], v[184:187], v[60:63]\n"
    "v_exp_f32 v159, v159\n"
    "v_mfma_f32_16x16x32_bf16 v[0:3], v[196:199], v[188:191], v[0:3]\n"
    "s_waitcnt lgkmcnt(3)\n"
    "ds_read_b128 v[220:223], v230 offset:14336\n"
    "v_mfma_f32_16x16x32_bf16 v[4:7], v[196:199], v[192:195], v[4:7]\n"
    "s_add_u32 s57, s57, 16384\n"
    "v_exp_f32 v152, v152\n"
    "v_mfma_f32_16x16x32_bf16 v[8:11], v[200:203], v[188:191], v[8:11]\n"
    "ds_read_b128 v[224:227], v230 offset:15360\n"
    "v_mfma_f32_16x16x32_bf16 v[12:15], v[200:203], v[192:195], v[12:15]\n"
    "s_cmp_eq_u32 s57, s58\n"
    "v_exp_f32 v153, v153\n"
    "v_mfma_f32_16x16x32_bf16 v[16:19], v[204:207], v[188:191], v[16:19]\n"
    "s_waitcnt lgkmcnt(3)\n"
    "ds_read_b128 v[196:199], v229 offset:0\n"
    "v_mfma_f32_16x16x32_bf16 v[20:23], v[204:207], v[192:195], v[20:23]\n"
    "s_cselect_b32 s57, s51, s57\n"
    "v_exp_f32 v154, v154\n"
    "v_mfma_f32_16x16x32_bf16 v[24:27], v[208:211], v[188:191], v[24:27]\n"
    "ds_read_b128 v[200:203], v229 offset:4096\n"
    "v_mfma_f32_16x16x32_bf16 v[28:31], v[208:211], v[192:195], v[28:31]\n"
    "s_cselect_b32 s46, s48, s46\n"
    "v_exp_f32 v155, v155\n"
    "v_mfma_f32_16x16x32_bf16 v[32:35], v[212:215], v[188:191], v[32:35]\n"
    "s_waitcnt lgkmcnt(3)\n"
    "ds_read_b128 v[204:207], v229 offset:8192\n"
    "v_mfma_f32_16x16x32_bf16 v[36:39], v[212:215], v[192:195], v[36:39]\n"
    "s_add_u32 s61, s61, 16384\n"
    "v_exp_f32 v160, v160\n"
    "v_mfma_f32_16x16x32_bf16 v[40:43], v[216:219], v[188:191], v[40:43]\n"
    "ds_read_b128 v[208:211], v229 offset:12288\n"
    "v_mfma_f32_16x16x32_bf16 v[44:47], v[216:219], v[192:195], v[44:47]\n"
    "s_and_b32 s61, s61, 0xffff\n"
    "v_exp_f32 v161, v161\n"
    "v_mfma_f32_16x16x32_bf16 v[48:51], v[220:223], v[188:191], v[48:51]\n"
    "s_waitcnt lgkmcnt(3)\n"
    "ds_read_b128 v[212:215], v229 offset:1024\n"
    "v_mfma_f32_16x16x32_bf16 v[52:55], v[220:223], v[192:195], v[52:55]\n"
    "v_exp_f32 v162, v162\n"
    "v_mfma_f32_16x16x32_bf16 v[56:59], v[224:227], v[188:191], v[56:59]\n"
    "ds_read_b128 v[216:219], v229 offset:5120\n"
    "v_mfma_f32_16x16x32_bf16 v[60:63], v[224:227], v[192:195], v[60:63]\n"
    "v_exp_f32 v163, v163\n"
    "v_mfma_f32_16x16x32_bf16 v[64:67], v[80:83], v[188:191], v[64:67]\n"
    "v_add_u32 v230, 0x4000, v230\n"
    "v_and_or_b32 v230, v230, v236, v237\n"
    "s_waitcnt vmcnt(12)\n"
    "v_mfma_f32_16x16x32_bf16 v[68:71], v[80:83], v[192:195], v[68:71]\n"
    "s_barrier\n"
    "v_mov_b64 v[116:117], v[148:149]\n"
    "v_mov_b64 v[118:119], v[150:151]\n"
    "v_mov_b64 v[120:121], v[152:153]\n"
    "v_mov_b64 v[122:123], v[154:155]\n"
    "v_mov_b64 v[124:125], v[156:157]\n"
    "v_mov_b64 v[126:127], v[158:159]\n"
    "v_mov_b64 v[128:129], v[160:161]\n"
    "v_mov_b64 v[130:131], v[162:163]\n"
    "v_mov_b64 v[132:133], v[164:165]\n"
    "v_mov_b64 v[134:135], v[166:167]\n"
    "v_mov_b64 v[136:137], v[168:169]\n"
    "v_mov_b64 v[138:139], v[170:171]\n"
    "v_mov_b64 v[140:141], v[172:173]\n"
    "v_mov_b64 v[142:143], v[174:175]\n"
    "v_mov_b64 v[144:145], v[176:177]\n"
    "v_mov_b64 v[146:147], v[178:179]\n"
    "v_swap_b32 v228, v229\n"
    "s_sub_u32 s50, s50, 2\n"
    "s_cmp_le_u32 s50, 3\n"
    "s_cbranch_scc1 LJ_MASKED%=\n"
    "LJ_PAIR%=:\n"
    "v_mfma_f32_16x16x32_bf16 v[148:151], v[196:199], v[84:87], v[72:75]\n"
    "s_waitcnt lgkmcnt(3)\n"
    "ds_read_b128 v[220:223], v228 offset:9216\n"
    "v_cvt_pk_bf16_f32 v180, v116, v117\n"
    "v_mfma_f32_16x16x32_bf16 v[152:155], v[196:199], v[100:103], v[76:79]\n"
    "s_mov_b32 m0, s60\n"
    "v_exp_f32 v132, v132\n"
    "v_mfma_f32_16x16x32_bf16 v[156:159], v[200:203], v[84:87], v[72:75]\n"
    "buffer_load_dwordx4 v231, s[40:43], s56 offen lds\n"
    "ds_read_b128 v[224:227], v228 offset:13312\n"
    "v_mfma_f32_16x16x32_bf16 v[160:163], v[200:203], v[100:103], v[76:79]\n"
    "v_exp_f32 v133, v133\n"
    "v_mfma_f32_16x16x32_bf16 v[164:167], v[204:207], v[84:87], v[72:75]\n"
    "s_waitcnt lgkmcnt(3)\n"
    "ds_read_b128 v[196:199], v228 offset:2048\n"
    "v_cvt_pk_bf16_f32 v181, v118, v119\n"
    "v_mfma_f32_16x16x32_bf16 v[168:171], v[204:207], v[100:103], v[76:79]\n"
    "v_exp_f32 v134, v134\n"
    "v_mfma_f32_16x16x32_bf16 v[172:175], v[208:211], v[84:87], v[72:75]\n"
    "buffer_load_dwordx4 v231, s[40:43], s56 offen offset:1024 lds\n"
    "ds_read_b128 v[200:203], v228 offset:6144\n"
    "v_mfma_f32_16x16x32_bf16 v[176:179], v[208:211], v[100:103], v[76:79]\n"
    "v_exp_f32 v135, v135\n"
    "v_mfma_f32_16x16x32_bf16 v[148:151], v[212:215], v[88:91], v[148:151]\n"
    "s_waitcnt lgkmcnt(3)\n"
    "ds_read_b128 v[204:207], v228 offset:10240\n"
    "v_cvt_pk_bf16_f32 v182, v124, v125\n"
    "v_mfma_f32_16x16x32_bf16 v[152:155], v[212:215], v[104:107], v[152:155]\n"
    "v_exp_f32 v140, v140\n"
    "v_mfma_f32_16x16x32_bf16 v[156:159], v[216:219], v[88:91], v[156:159]\n"
    "buffer_load_dwordx4 v231, s[40:43], s56 offen offset:2048 lds\n"
    "ds_read_b128 v[208:211], v228 offset:14336\n"
    "v_mfma_f32_16x16x32_bf16 v[160:163], v[216:219], v[104:107], v[160:163]\n"
    "v_exp_f32 v141, v141\n"
    "v_mfma_f32_16x16x32_bf16 v[164:167], v[220:223], v[88:91], v[164:167]\n"
    "s_waitcnt lgkmcnt(3)\n"
    "ds_read_b128 v[212:215], v228 offset:3072\n"
    "v_cvt_pk_bf16_f32 v183, v126, v127\n"
    "v_mfma_f32_16x16x32_bf16 v[168:171], v[220:223], v[104:107], v[168:171]\n"
    "v_exp_f32 v142, v142\n"
    "v_mfma_f32_16x16x32_bf16 v[172:175], v[224:227], v[88:91], v[172:175]\n"
    "buffer_load_dwordx4 v231, s[40:43], s56 offen offset:3072 lds\n"
    "ds_read_b128 v[216:219], v228 offset:7168\n"
    "v_mfma_f32_16x16x32_bf16 v[176:179], v[224:227], v[104:107], v[176:179]\n"
    "v_exp_f32 v143, v143\n"
    "v_mfma_f32_16x16x32_bf16 v[148:151], v[196:199], v[92:95], v[148:151]\n"
    "s_waitcnt lgkmcnt(3)\n"
    "ds_read_b128 v[220:223], v228 offset:11264\n"
    "v_cvt_pk_bf16_f32 v184, v120, v121\n"
    "v_mfma_f32_16x16x32_bf16 v[152:155], v[196:199], v[108:111], v[152:155]\n"
    "s_add_u32 s56, s56, 16384\n"
    "v_exp_f32 v136, v136\n"
    "v_mfma_f32_16x16x32_bf16 v[156:159], v[200:203], v[92:95], v[156:159]\n"
    "ds_read_b128 v[224:227], v228 offset:15360\n"
    "v_cvt_pk_bf16_f32 v185, v122, v123\n"
    "v_mfma_f32_16x16x32_bf16 v[160:163], v[200:203], v[108:111], v[160:163]\n"
    "s_cmp_eq_u32 s56, s58\n"
    "v_exp_f32 v137, v137\n"
    "v_mfma_f32_16x16x32_bf16 v[164:167], v[204:207], v[92:95], v[164:167]\n"
    "s_waitcnt lgkmcnt(3)\n"
    "ds_read_b128 v[196:199], v230 offset:0\n"
    "v_cvt_pk_bf16_f32 v186, v128, v129\n"
    "v_mfma_f32_16x16x32_bf16 v[168:171], v[204:207], v[108:111], v[168:171]\n"
    "s_cselect_b32 s56, s51, s56\n"
    "v_exp_f32 v138, v138\n"
    "v_mfma_f32_16x16x32_bf16 v[172:175], v[208:211], v[92:95], v[172:175]\n"
    "ds_read_b128 v[200:203], v230 offset:1024\n"
    "v_cvt_pk_bf16_f32 v187, v130, v131\n"
    "v_mfma_f32_16x16x32_bf16 v[176:179], v[208:211], v[108:111], v[176:179]\n"
    "s_cselect_b32 s42, s48, s42\n"
    "v_exp_f32 v139, v139\n"
    "v_mfma_f32_16x16x32_bf16 v[148:151], v[212:215], v[96:99], v[148:151]\n"
    "s_waitcnt lgkmcnt(3)\n"
    "ds_read_b128 v[204:207], v230 offset:2048\n"
    "v_cvt_pk_bf16_f32 v188, v132, v133\n"
    "v_mfma_f32_16x16x32_bf16 v[152:155], v[212:215], v[112:115], v[152:155]\n"
    "s_add_u32 s60, s60, 16384\n"
    "v_exp_f32 v144, v144\n"
    "v_mfma_f32_16x16x32_bf16 v[156:159], v[216:219], v[96:99], v[156:159]\n"
    "ds_read_b128 v[208:211], v230 offset:3072\n"
    "v_cvt_pk_bf16_f32 v189, v134, v135\n"
    "v_mfma_f32_16x16x32_bf16 v[160:163], v[216:219], v[112:115], v[160:163]\n"
    "s_and_b32 s60, s60, 0xffff\n"
    "v_exp_f32 v145, v145\n"
    "v_mfma_f32_16x16x32_bf16 v[164:167], v[220:223], v[96:99], v[164:167]\n"
    "s_waitcnt lgkmcnt(3)\n"
    "ds_read_b128 v[212:215], v230 offset:4096\n"
    "v_cvt_pk_bf16_f32 v190, v140, v141\n"
    "v_mfma_f32_16x16x32_bf16 v[168:171], v[220:223], v[112:115], v[168:171]\n"
    "v_exp_f32 v146, v146\n"
    "v_mfma_f32_16x16x32_bf16 v[172:175], v[224:227], v[96:99], v[172:175]\n"
    "ds_read_b128 v[216:219], v230 offset:5120\n"
    "v_cvt_pk_bf16_f32 v191, v142, v143\n"
    "v_mfma_f32_16x16x32_bf16 v[176:179], v[224:227], v[112:115], v[176:179]\n"
    "s_or_b32 m0, s61, 0x10000\n"
    "v_exp_f32 v147, v147\n"
    "v_mfma_f32_16x16x32_bf16 v[0:3], v[196:199], v[180:183], v[0:3]\n"
    "s_waitcnt lgkmcnt(3)\n"
    "ds_read_b128 v[220:223], v230 offset:6144\n"
    "v_cvt_pk_bf16_f32 v192, v136, v137\n"
    "v_mfma_f32_16x16x32_bf16 v[4:7], v[196:199], v[184:187], v[4:7]\n"
    "v_exp_f32 v148, v148\n"
    "v_mfma_f32_16x16x32_bf16 v[8:11], v[200:203], v[180:183], v[8:11]\n"
    "buffer_load_dwordx4 v231, s[44:47], s57 offen lds\n"
    "ds_read_b128 v[224:227], v230 offset:7168\n"
    "v_mfma_f32_16x16x32_bf16 v[12:15], v[200:203], v[184:187], v[12:15]\n"
    "v_exp_f32 v149, v149\n"
    "v_mfma_f32_16x16x32_bf16 v[16:19], v[204:207], v[180:183], v[16:19]\n"
    "s_waitcnt lgkmcnt(3)\n"
    "ds_read_b128 v[196:199], v230 offset:8192\n"
    "v_cvt_pk_bf16_f32 v193, v138, v139\n"
    "v_mfma_f32_16x16x32_bf16 v[20:23], v[204:207], v[184:187], v[20:23]\n"
    "v_exp_f32 v150, v150\n"
    "v_mfma_f32_16x16x32_bf16 v[64:67], v[80:83], v[180:183], v[64:67]\n"
    "v_add_u32 v228, 0x4000, v229\n"
    "v_and_b32 v228, 0xffff, v228\n"
    "v_mfma_f32_16x16x32_bf16 v[68:71], v[80:83], v[184:187], v[68:71]\n"
    "v_mfma_f32_16x16x32_bf16 v[24:27], v[208:211], v[180:183], v[24:27]\n"
    "buffer_load_dwordx4 v231, s[44:47], s57 offen offset:1024 lds\n"
    "ds_read_b128 v[200:203], v230 offset:9216\n"
    "v_mfma_f32_16x16x32_bf16 v[28:31], v[208:211], v[184:187], v[28:31]\n"
    "v_exp_f32 v151, v151\n"
    "v_mfma_f32_16x16x32_bf16 v[32:35], v[212:215], v[180:183], v[32:35]\n"
    "s_waitcnt lgkmcnt(3)\n"
    "ds_read_b128 v[204:207], v230 offset:10240\n"
    "v_cvt_pk_bf16_f32 v194, v144, v145\n"
    "v_mfma_f32_16x16x32_bf16 v[36:39], v[212:215], v[184:187], v[36:39]\n"
    "v_exp_f32 v156, v156\n"
    "v_mfma_f32_16x16x32_bf16 v[40:43], v[216:219], v[180:183], v[40:43]\n"
    "buffer_load_dwordx4 v231, s[44:47], s57 offen offset:2048 lds\n"
    "ds_read_b128 v[208:211], v230 offset:11264\n"
    "v_mfma_f32_16x16x32_bf16 v[44:47], v[216:219], v[184:187], v[44:47]\n"
    "v_exp_f32 v157, v157\n"
    "v_mfma_f32_16x16x32_bf16 v[48:51], v[220:223], v[180:183], v[48:51]\n"
    "s_waitcnt lgkmcnt(3)\n"
    "ds_read_b128 v[212:215], v230 offset:12288\n"
    "v_cvt_pk_bf16_f32 v195, v146, v147\n"
    "v_mfma_f32_16x16x32_bf16 v[52:55], v[220:223], v[184:187], v[52:55]\n"
    "v_exp_f32 v158, v158\n"
    "v_mfma_f32_16x16x32_bf16 v[56:59], v[224:227], v[180:183], v[56:59]\n"
    "buffer_load_dwordx4 v231, s[44:47], s57 offen offset:3072 lds\n"
    "ds_read_b128 v[216:219], v230 offset:13312\n"
    "v_mfma_f32_16x16x32_bf16 v[60:63], v[224:227], v[184:187], v[60:63]\n"
    "v_exp_f32 v159, v159\n"
    "v_mfma_f32_16x16x32_bf16 v[0:3], v[196:199], v[188:191], v[0:3]\n"
    "s_waitcnt lgkmcnt(3)\n"
    "ds_read_b128 v[220:223], v230 offset:14336\n"
    "v_mfma_f32_16x16x32_bf16 v[4:7], v[196:199], v[192:195], v[4:7]\n"
    "s_add_u32 s57, s57, 16384\n"
    "v_exp_f32 v152, v152\n"
    "v_mfma_f32_16x16x32_bf16 v[8:11], v[200:203], v[188:191], v[8:11]\n"
    "ds_read_b128 v[224:227], v230 offset:15360\n"
    "v_mfma_f32_16x16x32_bf16 v[12:15], v[200:203], v[192:195], v[12:15]\n"
    "s_cmp_eq_u32 s57, s58\n"
    "v_exp_f32 v153, v153\n"
    "v_mfma_f32_16x16x32_bf16 v[16:19], v[204:207], v[188:191], v[16:19]\n"
    "s_waitcnt lgkmcnt(3)\n"
    "ds_read_b128 v[196:199], v229 offset:0\n"
    "v_mfma_f32_16x16x32_bf16 v[20:23], v[204:207], v[192:195], v[20:23]\n"
    "s_cselect_b32 s57, s51, s57\n"
    "v_exp_f32 v154, v154\n"
    "v_mfma_f32_16x16x32_bf16 v[24:27], v[208:211], v[188:191], v[24:27]\n"
    "ds_read_b128 v[200:203], v229 offset:4096\n"
    "v_mfma_f32_16x16x32_bf16 v[28:31], v[208:211], v[192:195], v[28:31]\n"
    "s_cselect_b32 s46, s48, s46\n"
    "v_exp_f32 v155, v155\n"
    "v_mfma_f32_16x16x32_bf16 v[32:35], v[212:215], v[188:191], v[32:35]\n"
    "s_waitcnt lgkmcnt(3)\n"
    "ds_read_b128 v[204:207], v229 offset:8192\n"
    "v_mfma_f32_16x16x32_bf16 v[36:39], v[212:215], v[192:195], v[36:39]\n"
    "s_add_u32 s61, s61, 16384\n"
    "v_exp_f32 v160, v160\n"
    "v_mfma_f32_16x16x32_bf16 v[40:43], v[216:219], v[188:191], v[40:43]\n"
    "ds_read_b128 v[208:211], v229 offset:12288\n"
    "v_mfma_f32_16x16x32_bf16 v[44:47], v[216:219], v[192:195], v[44:47]\n"
    "s_and_b32 s61, s61, 0xffff\n"
    "v_exp_f32 v161, v161\n"
    "v_mfma_f32_16x16x32_bf16 v[48:51], v[220:223], v[188:191], v[48:51]\n"
    "s_waitcnt lgkmcnt(3)\n"
    "ds_read_b128 v[212:215], v229 offset:1024\n"
    "v_mfma_f32_16x16x32_bf16 v[52:55], v[220:223], v[192:195], v[52:55]\n"
    "v_exp_f32 v162, v162\n"
    "v_mfma_f32_16x16x32_bf16 v[56:59], v[224:227], v[188:191], v[56:59]\n"
    "ds_read_b128 v[216:219], v229 offset:5120\n"
    "v_mfma_f32_16x16x32_bf16 v[60:63], v[224:227], v[192:195], v[60:63]\n"
    "v_exp_f32 v163, v163\n"
    "v_mfma_f32_16x16x32_bf16 v[64:67], v[80:83], v[188:191], v[64:67]\n"
    "v_add_u32 v230, 0x4000, v230\n"
    "v_and_or_b32 v230, v230, v236, v237\n"
    "s_waitcnt vmcnt(12)\n"
    "v_mfma_f32_16x16x32_bf16 v[68:71], v[80:83], v[192:195], v[68:71]\n"
    "s_barrier\n"
    "v_mfma_f32_16x16x32_bf16 v[116:119], v[196:199], v[84:87], v[72:75]\n"
    "s_waitcnt lgkmcnt(3)\n"
    "ds_read_b128 v[220:223], v229 offset:9216\n"
    "v_cvt_pk_bf16_f32 v180, v148, v149\n"
    "v_mfma_f32_16x16x32_bf16 v[120:123], v[196:199], v[100:103], v[76:79]\n"
    "s_mov_b32 m0, s60\n"
    "v_exp_f32 v164, v164\n"
    "v_mfma_f32_16x16x32_bf16 v[124:127], v[200:203], v[84:87], v[72:75]\n"
    "buffer_load_dwordx4 v231, s[40:43], s56 offen lds\n"
    "ds_read_b128 v[224:227], v229 offset:13312\n"
    "v_mfma_f32_16x16x32_bf16 v[128:131], v[200:203], v[100:103], v[76:79]\n"
    "v_exp_f32 v165, v165\n"
    "v_mfma_f32_16x16x32_bf16 v[132:135], v[204:207], v[84:87], v[72:75]\n"
    "s_waitcnt lgkmcnt(3)\n"
    "ds_read_b128 v[196:199], v229 offset:2048\n"
    "v_cvt_pk_bf16_f32 v181, v150, v151\n"
    "v_mfma_f32_16x16x32_bf16 v[136:139], v[204:207], v[100:103], v[76:79]\n"
    "v_exp_f32 v166, v166\n"
    "v_mfma_f32_16x16x32_bf16 v[140:143], v[208:211], v[84:87], v[72:75]\n"
    "buffer_load_dwordx4 v231, s[40:43], s56 offen offset:1024 lds\n"
    "ds_read_b128 v[200:203], v229 offset:6144\n"
    "v_mfma_f32_16x16x32_bf16 v[144:147], v[208:211], v[100:103], v[76:79]\n"
    "v_exp_f32 v167, v167\n"
    "v_mfma_f32_16x16x32_bf16 v[116:119], v[212:215], v[88:91], v[116:119]\n"
    "s_waitcnt lgkmcnt(3)\n"
    "ds_read_b128 v[204:207], v229 offset:10240\n"
    "v_cvt_pk_bf16_f32 v182, v156, v157\n"
    "v_mfma_f32_16x16x32_bf16 v[120:123], v[212:215], v[104:107], v[120:123]\n"
    "v_exp_f32 v172, v172\n"
    "v_mfma_f32_16x16x32_bf16 v[124:127], v[216:219], v[88:91], v[124:127]\n"
    "buffer_load_dwordx4 v231, s[40:43], s56 offen offset:2048 lds\n"
    "ds_read_b128 v[208:211], v229 offset:14336\n"
    "v_mfma_f32_16x16x32_bf16 v[128:131], v[216:219], v[104:107], v[128:131]\n"
    "v_exp_f32 v173, v173\n"
    "v_mfma_f32_16x16x32_bf16 v[132:135], v[220:223], v[88:91], v[132:135]\n"
    "s_waitcnt lgkmcnt(3)\n"
    "ds_read_b128 v[212:215], v229 offset:3072\n"
    "v_cvt_pk_bf16_f32 v183, v158, v159\n"
    "v_mfma_f32_16x16x32_bf16 v[136:139], v[220:223], v[104:107], v[136:139]\n"
    "v_exp_f32 v174, v174\n"
    "v_mfma_f32_16x16x32_bf16 v[140:143], v[224:227], v[88:91], v[140:143]\n"
    "buffer_load_dwordx4 v231, s[40:43], s56 offen offset:3072 lds\n"
    "ds_read_b128 v[216:219], v229 offset:7168\n"
    "v_mfma_f32_16x16x32_bf16 v[144:147], v[224:227], v[104:107], v[144:147]\n"
    "v_exp_f32 v175, v175\n"
    "v_mfma_f32_16x16x32_bf16 v[116:119], v[196:199], v[92:95], v[116:119]\n"
    "s_waitcnt lgkmcnt(3)\n"
    "ds_read_b128 v[220:223], v229 offset:11264\n"
    "v_cvt_pk_bf16_f32 v184, v152, v153\n"
    "v_mfma_f32_16x16x32_bf16 v[120:123], v[196:199], v[108:111], v[120:123]\n"
    "s_add_u32 s56, s56, 16384\n"
    "v_exp_f32 v168, v168\n"
    "v_mfma_f32_16x16x32_bf16 v[124:127], v[200:203], v[92:95], v[124:127]\n"
    "ds_read_b128 v[224:227], v229 offset:15360\n"
    "v_cvt_pk_bf16_f32 v185, v154, v155\n"
    "v_mfma_f32_16x16x32_bf16 v[128:131], v[200:203], v[108:111], v[128:131]\n"
    "s_cmp_eq_u32 s56, s58\n"
    "v_exp_f32 v169, v169\n"
    "v_mfma_f32_16x16x32_bf16 v[132:135], v[204:207], v[92:95], v[132:135]\n"
    "s_waitcnt lgkmcnt(3)\n"
    "ds_read_b128 v[196:199], v230 offset:0\n"
    "v_cvt_pk_bf16_f32 v186, v160, v161\n"
    "v_mfma_f32_16x16x32_bf16 v[136:139], v[204:207], v[108:111], v[136:139]\n"
    "s_cselect_b32 s56, s51, s56\n"
    "v_exp_f32 v170, v170\n"
    "v_mfma_f32_16x16x32_bf16 v[140:143], v[208:211], v[92:95], v[140:143]\n"
    "ds_read_b128 v[200:203], v230 offset:1024\n"
    "v_cvt_pk_bf16_f32 v187, v162, v163\n"
    "v_mfma_f32_16x16x32_bf16 v[144:147], v[208:211], v[108:111], v[144:147]\n"
    "s_cselect_b32 s42, s48, s42\n"
    "v_exp_f32 v171, v171\n"
    "v_mfma_f32_16x16x32_bf16 v[116:119], v[212:215], v[96:99], v[116:119]\n"
    "s_waitcnt lgkmcnt(3)\n"
    "ds_read_b128 v[204:207], v230 offset:2048\n"
    "v_cvt_pk_bf16_f32 v188, v164, v165\n"
    "v_mfma_f32_16x16x32_bf16 v[120:123], v[212:215], v[112:115], v[120:123]\n"
    "s_add_u32 s60, s60, 16384\n"
    "v_exp_f32 v176, v176\n"
    "v_mfma_f32_16x16x32_bf16 v[124:127], v[216:219], v[96:99], v[124:127]\n"
    "ds_read_b128 v[208:211], v230 offset:3072\n"
    "v_cvt_pk_bf16_f32 v189, v166, v167\n"
    "v_mfma_f32_16x16x32_bf16 v[128:131], v[216:219], v[112:115], v[128:131]\n"
    "s_and_b32 s60, s60, 0xffff\n"
    "v_exp_f32 v177, v177\n"
    "v_mfma_f32_16x16x32_bf16 v[132:135], v[220:223], v[96:99], v[132:135]\n"
    "s_waitcnt lgkmcnt(3)\n"
    "ds_read_b128 v[212:215], v230 offset:4096\n"
    "v_cvt_pk_bf16_f32 v190, v172, v173\n"
    "v_mfma_f32_16x16x32_bf16 v[136:139], v[220:223], v[112:115], v[136:139]\n"
    "v_exp_f32 v178, v178\n"
    "v_mfma_f32_16x16x32_bf16 v[140:143], v[224:227], v[96:99], v[140:143]\n"
    "ds_read_b128 v[216:219], v230 offset:5120\n"
    "v_cvt_pk_bf16_f32 v191, v174, v175\n"
    "v_mfma_f32_16x16x32_bf16 v[144:147], v[224:227], v[112:115], v[144:147]\n"
    "s_or_b32 m0, s61, 0x10000\n"
    "v_exp_f32 v179, v179\n"
    "v_mfma_f32_16x16x32_bf16 v[0:3], v[196:199], v[180:183], v[0:3]\n"
    "s_waitcnt lgkmcnt(3)\n"
    "ds_read_b128 v[220:223], v230 offset:6144\n"
    "v_cvt_pk_bf16_f32 v192, v168, v169\n"
    "v_mfma_f32_16x16x32_bf16 v[4:7], v[196:199], v[184:187], v[4:7]\n"
    "v_exp_f32 v116, v116\n"
    "v_mfma_f32_16x16x32_bf16 v[8:11], v[200:203], v[180:183], v[8:11]\n"
    "buffer_load_dwordx4 v231, s[44:47], s57 offen lds\n"
    "ds_read_b128 v[224:227], v230 offset:7168\n"
    "v_mfma_f32_16x16x32_bf16 v[12:15], v[200:203], v[184:187], v[12:15]\n"
    "v_exp_f32 v117, v117\n"
    "v_mfma_f32_16x16x32_bf16 v[16:19], v[204:207], v[180:183], v[16:19]\n"
    "s_waitcnt lgkmcnt(3)\n"
    "ds_read_b128 v[196:199], v230 offset:8192\n"
    "v_cvt_pk_bf16_f32 v193, v170, v171\n"
    "v_mfma_f32_16x16x32_bf16 v[20:23], v[204:207], v[184:187], v[20:23]\n"
    "v_exp_f32 v118, v118\n"
    "v_mfma_f32_16x16x32_bf16 v[64:67], v[80:83], v[180:183], v[64:67]\n"
    "v_add_u32 v229, 0x4000, v228\n"
    "v_and_b32 v229, 0xffff, v229\n"
    "v_mfma_f32_16x16x32_bf16 v[68:71], v[80:83], v[184:187], v[68:71]\n"
    "v_mfma_f32_16x16x32_bf16 v[24:27], v[208:211], v[180:183], v[24:27]\n"
    "buffer_load_dwordx4 v231, s[44:47], s57 offen offset:1024 lds\n"
    "ds_read_b128 v[200:203], v230 offset:9216\n"
    "v_mfma_f32_16x16x32_bf16 v[28:31], v[208:211], v[184:187], v[28:31]\n"
    "v_exp_f32 v119, v119\n"
    "v_mfma_f32_16x16x32_bf16 v[32:35], v[212:215], v[180:183], v[32:35]\n"
    "s_waitcnt lgkmcnt(3)\n"
    "ds_read_b128 v[204:207], v230 offset:10240\n"
    "v_cvt_pk_bf16_f32 v194, v176, v177\n"
    "v_mfma_f32_16x16x32_bf16 v[36:39], v[212:215], v[184:187], v[36:39]\n"
    "v_exp_f32 v124, v124\n"
    "v_mfma_f32_16x16x32_bf16 v[40:43], v[216:219], v[180:183], v[40:43]\n"
    "buffer_load_dwordx4 v231, s[44:47], s57 offen offset:2048 lds\n"
    "ds_read_b128 v[208:211], v230 offset:11264\n"
    "v_mfma_f32_16x16x32_bf16 v[44:47], v[216:219], v[184:187], v[44:47]\n"
    "v_exp_f32 v125, v125\n"
    "v_mfma_f32_16x16x32_bf16 v[48:51], v[220:223], v[180:183], v[48:51]\n"
    "s_waitcnt lgkmcnt(3)\n"
    "ds_read_b128 v[212:215], v230 offset:12288\n"
    "v_cvt_pk_bf16_f32 v195, v178, v179\n"
    "v_mfma_f32_16x16x32_bf16 v[52:55], v[220:223], v[184:187], v[52:55]\n"
    "v_exp_f32 v126, v126\n"
    "v_mfma_f32_16x16x32_bf16 v[56:59], v[224:227], v[180:183], v[56:59]\n"
    "buffer_load_dwordx4 v231, s[44:47], s57 offen offset:3072 lds\n"
    "ds_read_b128 v[216:219], v230 offset:13312\n"
    "v_mfma_f32_16x16x32_bf16 v[60:63], v[224:227], v[184:187], v[60:63]\n"
    "v_exp_f32 v127, v127\n"
    "v_mfma_f32_16x16x32_bf16 v[0:3], v[196:199], v[188:191], v[0:3]\n"
    "s_waitcnt lgkmcnt(3)\n"
    "ds_read_b128 v[220:223], v230 offset:14336\n"
    "v_mfma_f32_16x16x32_bf16 v[4:7], v[196:199], v[192:195], v[4:7]\n"
    "s_add_u32 s57, s57, 16384\n"
    "v_exp_f32 v120, v120\n"
    "v_mfma_f32_16x16x32_bf16 v[8:11], v[200:203], v[188:191], v[8:11]\n"
    "ds_read_b128 v[224:227], v230 offset:15360\n"
    "v_mfma_f32_16x16x32_bf16 v[12:15], v[200:203], v[192:195], v[12:15]\n"
    "s_cmp_eq_u32 s57, s58\n"
    "v_exp_f32 v121, v121\n"
    "v_mfma_f32_16x16x32_bf16 v[16:19], v[204:207], v[188:191], v[16:19]\n"
    "s_waitcnt lgkmcnt(3)\n"
    "ds_read_b128 v[196:199], v228 offset:0\n"
    "v_mfma_f32_16x16x32_bf16 v[20:23], v[204:207], v[192:195], v[20:23]\n"
    "s_cselect_b32 s57, s51, s57\n"
    "v_exp_f32 v122, v122\n"
    "v_mfma_f32_16x16x32_bf16 v[24:27], v[208:211], v[188:191], v[24:27]\n"
    "ds_read_b128 v[200:203], v228 offset:4096\n"
    "v_mfma_f32_16x16x32_bf16 v[28:31], v[208:211], v[192:195], v[28:31]\n"
    "s_cselect_b32 s46, s48, s46\n"
    "v_exp_f32 v123, v123\n"
    "v_mfma_f32_16x16x32_bf16 v[32:35], v[212:215], v[188:191], v[32:35]\n"
    "s_waitcnt lgkmcnt(3)\n"
    "ds_read_b128 v[204:207], v228 offset:8192\n"
    "v_mfma_f32_16x16x32_bf16 v[36:39], v[212:215], v[192:195], v[36:39]\n"
    "s_add_u32 s61, s61, 16384\n"
    "v_exp_f32 v128, v128\n"
    "v_mfma_f32_16x16x32_bf16 v[40:43], v[216:219], v[188:191], v[40:43]\n"
    "ds_read_b128 v[208:211], v228 offset:12288\n"
    "v_mfma_f32_16x16x32_bf16 v[44:47], v[216:219], v[192:195], v[44:47]\n"
    "s_and_b32 s61, s61, 0xffff\n"
    "v_exp_f32 v129, v129\n"
    "v_mfma_f32_16x16x32_bf16 v[48:51], v[220:223], v[188:191], v[48:51]\n"
    "s_waitcnt lgkmcnt(3)\n"
    "ds_read_b128 v[212:215], v228 offset:1024\n"
    "v_mfma_f32_16x16x32_bf16 v[52:55], v[220:223], v[192:195], v[52:55]\n"
    "v_exp_f32 v130, v130\n"
    "v_mfma_f32_16x16x32_bf16 v[56:59], v[224:227], v[188:191], v[56:59]\n"
    "ds_read_b128 v[216:219], v228 offset:5120\n"
    "v_mfma_f32_16x16x32_bf16 v[60:63], v[224:227], v[192:195], v[60:63]\n"
    "v_exp_f32 v131, v131\n"
    "v_mfma_f32_16x16x32_bf16 v[64:67], v[80:83], v[188:191], v[64:67]\n"
    "v_add_u32 v230, 0x4000, v230\n"
    "v_and_or_b32 v230, v230, v236, v237\n"
    "s_waitcnt vmcnt(12)\n"
    "v_mfma_f32_16x16x32_bf16 v[68:71], v[80:83], v[192:195], v[68:71]\n"
    "s_barrier\n"
    "s_sub_u32 s50, s50, 2\n"
    "s_cmp_gt_u32 s50, 3\n"
    "s_cbranch_scc1 LJ_PAIR%=\n"
    "LJ_MASKED%=:\n"
    "v_mfma_f32_16x16x32_bf16 v[148:151], v[196:199], v[84:87], v[72:75]\n"
    "s_waitcnt lgkmcnt(3)\n"
    "ds_read_b128 v[220:223], v228 offset:9216\n"
    "v_cvt_pk_bf16_f32 v180, v116, v117\n"
    "v_mfma_f32_16x16x32_bf16 v[152:155], v[196:199], v[100:103], v[76:79]\n"
    "s_mov_b32 m0, s60\n"
    "v_exp_f32 v132, v132\n"
    "v_mfma_f32_16x16x32_bf16 v[156:159], v[200:203], v[84:87], v[72:75]\n"
    "buffer_load_dwordx4 v231, s[40:43], s56 offen lds\n"
    "ds_read_b128 v[224:227], v228 offset:13312\n"
    "v_mfma_f32_16x16x32_bf16 v[160:163], v[200:203], v[100:103], v[76:79]\n"
    "v_exp_f32 v133, v133\n"
    "v_mfma_f32_16x16x32_bf16 v[164:167], v[204:207], v[84:87], v[72:75]\n"
    "s_waitcnt lgkmcnt(3)\n"
    "ds_read_b128 v[196:199], v228 offset:2048\n"
    "v_cvt_pk_bf16_f32 v181, v118, v119\n"
    "v_mfma_f32_16x16x32_bf16 v[168:171], v[204:207], v[100:103], v[76:79]\n"
    "v_exp_f32 v134, v134\n"
    "v_mfma_f32_16x16x32_bf16 v[172:175], v[208:211], v[84:87], v[72:75]\n"
    "buffer_load_dwordx4 v231, s[40:43], s56 offen offset:1024 lds\n"
    "ds_read_b128 v[200:203], v228 offset:6144\n"
    "v_mfma_f32_16x16x32_bf16 v[176:179], v[208:211], v[100:103], v[76:79]\n"
    "v_exp_f32 v135, v135\n"
    "v_mfma_f32_16x16x32_bf16 v[148:151], v[212:215], v[88:91], v[148:151]\n"
    "s_waitcnt lgkmcnt(3)\n"
    "ds_read_b128 v[204:207], v228 offset:10240\n"
    "v_cvt_pk_bf16_f32 v182, v124, v125\n"
    "v_mfma_f32_16x16x32_bf16 v[152:155], v[212:215], v[104:107], v[152:155]\n"
    "v_exp_f32 v140, v140\n"
    "v_mfma_f32_16x16x32_bf16 v[156:159], v[216:219], v[88:91], v[156:159]\n"
    "buffer_load_dwordx4 v231, s[40:43], s56 offen offset:2048 lds\n"
    "ds_read_b128 v[208:211], v228 offset:14336\n"
    "v_mfma_f32_16x16x32_bf16 v[160:163], v[216:219], v[104:107], v[160:163]\n"
    "v_exp_f32 v141, v141\n"
    "v_mfma_f32_16x16x32_bf16 v[164:167], v[220:223], v[88:91], v[164:167]\n"
    "s_waitcnt lgkmcnt(3)\n"
    "ds_read_b128 v[212:215], v228 offset:3072\n"
    "v_cvt_pk_bf16_f32 v183, v126, v127\n"
    "v_mfma_f32_16x16x32_bf16 v[168:171], v[220:223], v[104:107], v[168:171]\n"
    "v_exp_f32 v142, v142\n"
    "v_mfma_f32_16x16x32_bf16 v[172:175], v[224:227], v[88:91], v[172:175]\n"
    "buffer_load_dwordx4 v231, s[40:43], s56 offen offset:3072 lds\n"
    "ds_read_b128 v[216:219], v228 offset:7168\n"
    "v_mfma_f32_16x16x32_bf16 v[176:179], v[224:227], v[104:107], v[176:179]\n"
    "v_exp_f32 v143, v143\n"
    "v_mfma_f32_16x16x32_bf16 v[148:151], v[196:199], v[92:95], v[148:151]\n"
    "s_waitcnt lgkmcnt(3)\n"
    "ds_read_b128 v[220:223], v228 offset:11264\n"
    "v_cvt_pk_bf16_f32 v184, v120, v121\n"
    "v_mfma_f32_16x16x32_bf16 v[152:155], v[196:199], v[108:111], v[152:155]\n"
    "s_add_u32 s56, s56, 16384\n"
    "v_exp_f32 v136, v136\n"
    "v_mfma_f32_16x16x32_bf16 v[156:159], v[200:203], v[92:95], v[156:159]\n"
    "ds_read_b128 v[224:227], v228 offset:15360\n"
    "v_cvt_pk_bf16_f32 v185, v122, v123\n"
    "v_mfma_f32_16x16x32_bf16 v[160:163], v[200:203], v[108:111], v[160:163]\n"
    "s_cmp_eq_u32 s56, s58\n"
    "v_exp_f32 v137, v137\n"
    "v_mfma_f32_16x16x32_bf16 v[164:167], v[204:207], v[92:95], v[164:167]\n"
    "s_waitcnt lgkmcnt(3)\n"
    "ds_read_b128 v[196:199], v230 offset:0\n"
    "v_cvt_pk_bf16_f32 v186, v128, v129\n"
    "v_mfma_f32_16x16x32_bf16 v[168:171], v[204:207], v[108:111], v[168:171]\n"
    "s_cselect_b32 s56, s51, s56\n"
    "v_exp_f32 v138, v138\n"
    "v_mfma_f32_16x16x32_bf16 v[172:175], v[208:211], v[92:95], v[172:175]\n"
    "ds_read_b128 v[200:203], v230 offset:1024\n"
    "v_cvt_pk_bf16_f32 v187, v130, v131\n"
    "v_mfma_f32_16x16x32_bf16 v[176:179], v[208:211], v[108:111], v[176:179]\n"
    "s_cselect_b32 s42, s48, s42\n"
    "v_exp_f32 v139, v139\n"
    "v_mfma_f32_16x16x32_bf16 v[148:151], v[212:215], v[96:99], v[148:151]\n"
    "s_waitcnt lgkmcnt(3)\n"
    "ds_read_b128 v[204:207], v230 offset:2048\n"
    "v_cvt_pk_bf16_f32 v188, v132, v133\n"
    "v_mfma_f32_16x16x32_bf16 v[152:155], v[212:215], v[112:115], v[152:155]\n"
    "s_add_u32 s60, s60, 16384\n"
    "v_exp_f32 v144, v144\n"
    "v_mfma_f32_16x16x32_bf16 v[156:159], v[216:219], v[96:99], v[156:159]\n"
    "ds_read_b128 v[208:211], v230 offset:3072\n"
    "v_cvt_pk_bf16_f32 v189, v134, v135\n"
    "v_mfma_f32_16x16x32_bf16 v[160:163], v[216:219], v[112:115], v[160:163]\n"
    "s_and_b32 s60, s60, 0xffff\n"
    "v_exp_f32 v145, v145\n"
    "v_mfma_f32_16x16x32_bf16 v[164:167], v[220:223], v[96:99], v[164:167]\n"
    "s_waitcnt lgkmcnt(3)\n"
    "ds_read_b128 v[212:215], v230 offset:4096\n"
    "v_cvt_pk_bf16_f32 v190, v140, v141\n"
    "v_mfma_f32_16x16x32_bf16 v[168:171], v[220:223], v[112:115], v[168:171]\n"
    "v_exp_f32 v146, v146\n"
    "v_mfma_f32_16x16x32_bf16 v[172:175], v[224:227], v[96:99], v[172:175]\n"
    "ds_read_b128 v[216:219], v230 offset:5120\n"
    "v_cvt_pk_bf16_f32 v191, v142, v143\n"
    "v_mfma_f32_16x16x32_bf16 v[176:179], v[224:227], v[112:115], v[176:179]\n"
    "s_or_b32 m0, s61, 0x10000\n"
    "v_exp_f32 v147, v147\n"
    "v_cmp_gt_i32_e64 s[64:65], 0, v232\n"
    "v_cmp_gt_i32_e64 s[66:67], 1, v232\n"
    "v_cmp_gt_i32_e64 s[68:69], 2, v232\n"
    "v_cmp_gt_i32_e64 s[70:71], 3, v232\n"
    "v_cndmask_b32_e64 v148, v148, v233, s[64:65]\n"
    "v_cndmask_b32_e64 v149, v149, v233, s[66:67]\n"
    "v_cndmask_b32_e64 v150, v150, v233, s[68:69]\n"
    "v_cndmask_b32_e64 v151, v151, v233, s[70:71]\n"
    "v_cmp_gt_i32_e64 s[64:65], -16, v232\n"
    "v_cmp_gt_i32_e64 s[66:67], -15, v232\n"
    "v_cmp_gt_i32_e64 s[68:69], -14, v232\n"
    "v_cmp_gt_i32_e64 s[70:71], -13, v232\n"
    "v_cndmask_b32_e64 v152, v152, v233, s[64:65]\n"
    "v_cndmask_b32_e64 v153, v153, v233, s[66:67]\n"
    "v_cndmask_b32_e64 v154, v154, v233, s[68:69]\n"
    "v_cndmask_b32_e64 v155, v155, v233, s[70:71]\n"
    "v_cmp_gt_i32_e64 s[64:65], 16, v232\n"
    "v_cmp_gt_i32_e64 s[66:67], 17, v232\n"
    "v_cmp_gt_i32_e64 s[68:69], 18, v232\n"
    "v_cmp_gt_i32_e64 s[70:71], 19, v232\n"
    "v_cndmask_b32_e64 v156, v156, v233, s[64:65]\n"
    "v_cndmask_b32_e64 v157, v157, v233, s[66:67]\n"
    "v_cndmask_b32_e64 v158, v158, v233, s[68:69]\n"
    "v_cndmask_b32_e64 v159, v159, v233, s[70:71]\n"
    "v_cmp_gt_i32_e64 s[64:65], 0, v232\n"
    "v_cmp_gt_i32_e64 s[66:67], 1, v232\n"
    "v_cmp_gt_i32_e64 s[68:69], 2, v232\n"
    "v_cmp_gt_i32_e64 s[70:71], 3, v232\n"
    "v_cndmask_b32_e64 v160, v160, v233, s[64:65]\n"
    "v_cndmask_b32_e64 v161, v161, v233, s[66:67]\n"
    "v_cndmask_b32_e64 v162, v162, v233, s[68:69]\n"
    "v_cndmask_b32_e64 v163, v163, v233, s[70:71]\n"
    "v_cmp_gt_i32_e64 s[64:65], 32, v232\n"
    "v_cmp_gt_i32_e64 s[66:67], 33, v232\n"
    "v_cmp_gt_i32_e64 s[68:69], 34, v232\n"
    "v_cmp_gt_i32_e64 s[70:71], 35, v232\n"
    "v_cndmask_b32_e64 v164, v164, v233, s[64:65]\n"
    "v_cndmask_b32_e64 v165, v165, v233, s[66:67]\n"
    "v_cndmask_b32_e64 v166, v166, v233, s[68:69]\n"
    "v_cndmask_b32_e64 v167, v167, v233, s[70:71]\n"
    "v_cmp_gt_i32_e64 s[64:65], 16, v232\n"
    "v_cmp_gt_i32_e64 s[66:67], 17, v232\n"
    "v_cmp_gt_i32_e64 s[68:69], 18, v232\n"
    "v_cmp_gt_i32_e64 s[70:71], 19, v232\n"
    "v_cndmask_b32_e64 v168, v168, v233, s[64:65]\n"
    "v_cndmask_b32_e64 v169, v169, v233, s[66:67]\n"
    "v_cndmask_b32_e64 v170, v170, v233, s[68:69]\n"
    "v_cndmask_b32_e64 v171, v171, v233, s[70:71]\n"
    "v_cmp_gt_i32_e64 s[64:65], 48, v232\n"
    "v_cmp_gt_i32_e64 s[66:67], 49, v232\n"
    "v_cmp_gt_i32_e64 s[68:69], 50, v232\n"
    "v_cmp_gt_i32_e64 s[70:71], 51, v232\n"
    "v_cndmask_b32_e64 v172, v172, v233, s[64:65]\n"
    "v_cndmask_b32_e64 v173, v173, v233, s[66:67]\n"
    "v_cndmask_b32_e64 v174, v174, v233, s[68:69]\n"
    "v_cndmask_b32_e64 v175, v175, v233, s[70:71]\n"
    "v_cmp_gt_i32_e64 s[64:65], 32, v232\n"
    "v_cmp_gt_i32_e64 s[66:67], 33, v232\n"
    "v_cmp_gt_i32_e64 s[68:69], 34, v232\n"
    "v_cmp_gt_i32_e64 s[70:71], 35, v232\n"
    "v_cndmask_b32_e64 v176, v176, v233, s[64:65]\n"
    "v_cndmask_b32_e64 v177, v177, v233, s[66:67]\n"
    "v_cndmask_b32_e64 v178, v178, v233, s[68:69]\n"
    "v_cndmask_b32_e64 v179, v179, v233, s[70:71]\n"
    "v_mfma_f32_16x16x32_bf16 v[0:3], v[196:199], v[180:183], v[0:3]\n"
    "s_waitcnt lgkmcnt(3)\n"
    "ds_read_b128 v[220:223], v230 offset:6144\n"
    "v_cvt_pk_bf16_f32 v192, v136, v137\n"
    "v_mfma_f32_16x16x32_bf16 v[4:7], v[196:199], v[184:187], v[4:7]\n"
    "v_exp_f32 v148, v148\n"
    "v_mfma_f32_16x16x32_bf16 v[8:11], v[200:203], v[180:183], v[8:11]\n"
    "buffer_load_dwordx4 v231, s[44:47], s57 offen lds\n"
    "ds_read_b128 v[224:227], v230 offset:7168\n"
    "v_mfma_f32_16x16x32_bf16 v[12:15], v[200:203], v[184:187], v[12:15]\n"
    "v_exp_f32 v149, v149\n"
    "v_mfma_f32_16x16x32_bf16 v[16:19], v[204:207], v[180:183], v[16:19]\n"
    "s_waitcnt lgkmcnt(3)\n"
    "ds_read_b128 v[196:199], v230 offset:8192\n"
    "v_cvt_pk_bf16_f32 v193, v138, v139\n"
    "v_mfma_f32_16x16x32_bf16 v[20:23], v[204:207], v[184:187], v[20:23]\n"
    "v_exp_f32 v150, v150\n"
    "v_mfma_f32_16x16x32_bf16 v[64:67], v[80:83], v[180:183], v[64:67]\n"
    "v_add_u32 v228, 0x4000, v229\n"
    "v_and_b32 v228, 0xffff, v228\n"
    "v_mfma_f32_16x16x32_bf16 v[68:71], v[80:83], v[184:187], v[68:71]\n"
    "v_mfma_f32_16x16x32_bf16 v[24:27], v[208:211], v[180:183], v[24:27]\n"
    "buffer_load_dwordx4 v231, s[44:47], s57 offen offset:1024 lds\n"
    "ds_read_b128 v[200:203], v230 offset:9216\n"
    "v_mfma_f32_16x16x32_bf16 v[28:31], v[208:211], v[184:187], v[28:31]\n"
    "v_exp_f32 v151, v151\n"
    "v_mfma_f32_16x16x32_bf16 v[32:35], v[212:215], v[180:183], v[32:35]\n"
    "s_waitcnt lgkmcnt(3)\n"
    "ds_read_b128 v[204:207], v230 offset:10240\n"
    "v_cvt_pk_bf16_f32 v194, v144, v145\n"
    "v_mfma_f32_16x16x32_bf16 v[36:39], v[212:215], v[184:187], v[36:39]\n"
    "v_exp_f32 v156, v156\n"
    "v_mfma_f32_16x16x32_bf16 v[40:43], v[216:219], v[180:183], v[40:43]\n"
    "buffer_load_dwordx4 v231, s[44:47], s57 offen offset:2048 lds\n"
    "ds_read_b128 v[208:211], v230 offset:11264\n"
    "v_mfma_f32_16x16x32_bf16 v[44:47], v[216:219], v[184:187], v[44:47]\n"
    "v_exp_f32 v157, v157\n"
    "v_mfma_f32_16x16x32_bf16 v[48:51], v[220:223], v[180:183], v[48:51]\n"
    "s_waitcnt lgkmcnt(3)\n"
    "ds_read_b128 v[212:215], v230 offset:12288\n"
    "v_cvt_pk_bf16_f32 v195, v146, v147\n"
    "v_mfma_f32_16x16x32_bf16 v[52:55], v[220:223], v[184:187], v[52:55]\n"
    "v_exp_f32 v158, v158\n"
    "v_mfma_f32_16x16x32_bf16 v[56:59], v[224:227], v[180:183], v[56:59]\n"
    "buffer_load_dwordx4 v231, s[44:47], s57 offen offset:3072 lds\n"
    "ds_read_b128 v[216:219], v230 offset:13312\n"
    "v_mfma_f32_16x16x32_bf16 v[60:63], v[224:227], v[184:187], v[60:63]\n"
    "v_exp_f32 v159, v159\n"
    "v_mfma_f32_16x16x32_bf16 v[0:3], v[196:199], v[188:191], v[0:3]\n"
    "s_waitcnt lgkmcnt(3)\n"
    "ds_read_b128 v[220:223], v230 offset:14336\n"
    "v_mfma_f32_16x16x32_bf16 v[4:7], v[196:199], v[192:195], v[4:7]\n"
    "s_add_u32 s57, s57, 16384\n"
    "v_exp_f32 v152, v152\n"
    "v_mfma_f32_16x16x32_bf16 v[8:11], v[200:203], v[188:191], v[8:11]\n"
    "ds_read_b128 v[224:227], v230 offset:15360\n"
    "v_mfma_f32_16x16x32_bf16 v[12:15], v[200:203], v[192:195], v[12:15]\n"
    "s_cmp_eq_u32 s57, s58\n"
    "v_exp_f32 v153, v153\n"
    "v_mfma_f32_16x16x32_bf16 v[16:19], v[204:207], v[188:191], v[16:19]\n"
    "s_waitcnt lgkmcnt(3)\n"
    "ds_read_b128 v[196:199], v229 offset:0\n"
    "v_mfma_f32_16x16x32_bf16 v[20:23], v[204:207], v[192:195], v[20:23]\n"
    "s_cselect_b32 s57, s51, s57\n"
    "v_exp_f32 v154, v154\n"
    "v_mfma_f32_16x16x32_bf16 v[24:27], v[208:211], v[188:191], v[24:27]\n"
    "ds_read_b128 v[200:203], v229 offset:4096\n"
    "v_mfma_f32_16x16x32_bf16 v[28:31], v[208:211], v[192:195], v[28:31]\n"
    "s_cselect_b32 s46, s48, s46\n"
    "v_exp_f32 v155, v155\n"
    "v_mfma_f32_16x16x32_bf16 v[32:35], v[212:215], v[188:191], v[32:35]\n"
    "s_waitcnt lgkmcnt(3)\n"
    "ds_read_b128 v[204:207], v229 offset:8192\n"
    "v_mfma_f32_16x16x32_bf16 v[36:39], v[212:215], v[192:195], v[36:39]\n"
    "s_add_u32 s61, s61, 16384\n"
    "v_exp_f32 v160, v160\n"
    "v_mfma_f32_16x16x32_bf16 v[40:43], v[216:219], v[188:191], v[40:43]\n"
    "ds_read_b128 v[208:211], v229 offset:12288\n"
    "v_mfma_f32_16x16x32_bf16 v[44:47], v[216:219], v[192:195], v[44:47]\n"
    "s_and_b32 s61, s61, 0xffff\n"
    "v_exp_f32 v161, v161\n"
    "v_mfma_f32_16x16x32_bf16 v[48:51], v[220:223], v[188:191], v[48:51]\n"
    "s_waitcnt lgkmcnt(3)\n"
    "ds_read_b128 v[212:215], v229 offset:1024\n"
    "v_mfma_f32_16x16x32_bf16 v[52:55], v[220:223], v[192:195], v[52:55]\n"
    "v_exp_f32 v162, v162\n"
    "v_mfma_f32_16x16x32_bf16 v[56:59], v[224:227], v[188:191], v[56:59]\n"
    "ds_read_b128 v[216:219], v229 offset:5120\n"
    "v_mfma_f32_16x16x32_bf16 v[60:63], v[224:227], v[192:195], v[60:63]\n"
    "v_exp_f32 v163, v163\n"
    "v_mfma_f32_16x16x32_bf16 v[64:67], v[80:83], v[188:191], v[64:67]\n"
    "v_add_u32 v230, 0x4000, v230\n"
    "v_and_or_b32 v230, v230, v236, v237\n"
    "s_waitcnt vmcnt(12)\n"
    "v_mfma_f32_16x16x32_bf16 v[68:71], v[80:83], v[192:195], v[68:71]\n"
    "s_barrier\n"
    "s_branch LJ_M2%=\n"
    "LJ_SHORT%=:\n"
    "v_mov_b64 v[148:149], v[116:117]\n"
    "v_mov_b64 v[150:151], v[118:119]\n"
    "v_mov_b64 v[152:153], v[120:121]\n"
    "v_mov_b64 v[154:155], v[122:123]\n"
    "v_mov_b64 v[156:157], v[124:125]\n"
    "v_mov_b64 v[158:159], v[126:127]\n"
    "v_mov_b64 v[160:161], v[128:129]\n"
    "v_mov_b64 v[162:163], v[130:131]\n"
    "v_mov_b64 v[164:165], v[132:133]\n"
    "v_mov_b64 v[166:167], v[134:135]\n"
    "v_mov_b64 v[168:169], v[136:137]\n"
    "v_mov_b64 v[170:171], v[138:139]\n"
    "v_mov_b64 v[172:173], v[140:141]\n"
    "v_mov_b64 v[174:175], v[142:143]\n"
    "v_mov_b64 v[176:177], v[144:145]\n"
    "v_mov_b64 v[178:179], v[146:147]\n"
    "v_swap_b32 v228, v229\n"
    "s_nop 7\n"
    "LJ_M2%=:\n"
    "s_cmp_lt_u32 s51, 8192\n"
    "s_cbranch_scc1 LJ_SKIP%=\n"
    "v_mfma_f32_16x16x32_bf16 v[116:119], v[196:199], v[84:87], v[72:75]\n"
    "s_waitcnt lgkmcnt(3)\n"
    "ds_read_b128 v[220:223], v229 offset:9216\n"
    "v_cvt_pk_bf16_f32 v180, v148, v149\n"
    "v_mfma_f32_16x16x32_bf16 v[120:123], v[196:199], v[100:103], v[76:79]\n"
    "s_mov_b32 m0, s60\n"
    "v_exp_f32 v164, v164\n"
    "v_mfma_f32_16x16x32_bf16 v[124:127], v[200:203], v[84:87], v[72:75]\n"
    "buffer_load_dwordx4 v231, s[40:43], s56 offen lds\n"
    "ds_read_b128 v[224:227], v229 offset:13312\n"
    "v_mfma_f32_16x16x32_bf16 v[128:131], v[200:203], v[100:103], v[76:79]\n"
    "v_exp_f32 v165, v165\n"
    "v_mfma_f32_16x16x32_bf16 v[132:135], v[204:207], v[84:87], v[72:75]\n"
    "s_waitcnt lgkmcnt(3)\n"
    "ds_read_b128 v[196:199], v229 offset:2048\n"
    "v_cvt_pk_bf16_f32 v181, v150, v151\n"
    "v_mfma_f32_16x16x32_bf16 v[136:139], v[204:207], v[100:103], v[76:79]\n"
    "v_exp_f32 v166, v166\n"
    "v_mfma_f32_16x16x32_bf16 v[140:143], v[208:211], v[84:87], v[72:75]\n"
    "buffer_load_dwordx4 v231, s[40:43], s56 offen offset:1024 lds\n"
    "ds_read_b128 v[200:203], v229 offset:6144\n"
    "v_mfma_f32_16x16x32_bf16 v[144:147], v[208:211], v[100:103], v[76:79]\n"
    "v_exp_f32 v167, v167\n"
    "v_mfma_f32_16x16x32_bf16 v[116:119], v[212:215], v[88:91], v[116:119]\n"
    "s_waitcnt lgkmcnt(3)\n"
    "ds_read_b128 v[204:207], v229 offset:10240\n"
    "v_cvt_pk_bf16_f32 v182, v156, v157\n"
    "v_mfma_f32_16x16x32_bf16 v[120:123], v[212:215], v[104:107], v[120:123]\n"
    "v_exp_f32 v172, v172\n"
    "v_mfma_f32_16x16x32_bf16 v[124:127], v[216:219], v[88:91], v[124:127]\n"
    "buffer_load_dwordx4 v231, s[40:43], s56 offen offset:2048 lds\n"
    "ds_read_b128 v[208:211], v229 offset:14336\n"
    "v_mfma_f32_16x16x32_bf16 v[128:131], v[216:219], v[104:107], v[128:131]\n"
    "v_exp_f32 v173, v173\n"
    "v_mfma_f32_16x16x32_bf16 v[132:135], v[220:223], v[88:91], v[132:135]\n"
    "s_waitcnt lgkmcnt(3)\n"
    "ds_read_b128 v[212:215], v229 offset:3072\n"
    "v_cvt_pk_bf16_f32 v183, v158, v159\n"
    "v_mfma_f32_16x16x32_bf16 v[136:139], v[220:223], v[104:107], v[136:139]\n"
    "v_exp_f32 v174, v174\n"
    "v_mfma_f32_16x16x32_bf16 v[140:143], v[224:227], v[88:91], v[140:143]\n"
    "buffer_load_dwordx4 v231, s[40:43], s56 offen offset:3072 lds\n"
    "ds_read_b128 v[216:219], v229 offset:7168\n"
    "v_mfma_f32_16x16x32_bf16 v[144:147], v[224:227], v[104:107], v[144:147]\n"
    "v_exp_f32 v175, v175\n"
    "v_mfma_f32_16x16x32_bf16 v[116:119], v[196:199], v[92:95], v[116:119]\n"
    "s_waitcnt lgkmcnt(3)\n"
    "ds_read_b128 v[220:223], v229 offset:11264\n"
    "v_cvt_pk_bf16_f32 v184, v152, v153\n"
    "v_mfma_f32_16x16x32_bf16 v[120:123], v[196:199], v[108:111], v[120:123]\n"
    "s_add_u32 s56, s56, 16384\n"
    "v_exp_f32 v168, v168\n"
    "v_mfma_f32_16x16x32_bf16 v[124:127], v[200:203], v[92:95], v[124:127]\n"
    "ds_read_b128 v[224:227], v229 offset:15360\n"
    "v_cvt_pk_bf16_f32 v185, v154, v155\n"
    "v_mfma_f32_16x16x32_bf16 v[128:131], v[200:203], v[108:111], v[128:131]\n"
    "s_cmp_eq_u32 s56, s58\n"
    "v_exp_f32 v169, v169\n"
    "v_mfma_f32_16x16x32_bf16 v[132:135], v[204:207], v[92:95], v[132:135]\n"
    "s_waitcnt lgkmcnt(3)\n"
    "ds_read_b128 v[196:199], v230 offset:0\n"
    "v_cvt_pk_bf16_f32 v186, v160, v161\n"
    "v_mfma_f32_16x16x32_bf16 v[136:139], v[204:207], v[108:111], v[136:139]\n"
    "s_cselect_b32 s56, s51, s56\n"
    "v_exp_f32 v170, v170\n"
    "v_mfma_f32_16x16x32_bf16 v[140:143], v[208:211], v[92:95], v[140:143]\n"
    "ds_read_b128 v[200:203], v230 offset:1024\n"
    "v_cvt_pk_bf16_f32 v187, v162, v163\n"
    "v_mfma_f32_16x16x32_bf16 v[144:147], v[208:211], v[108:111], v[144:147]\n"
    "s_cselect_b32 s42, s48, s42\n"
    "v_exp_f32 v171, v171\n"
    "v_mfma_f32_16x16x32_bf16 v[116:119], v[212:215], v[96:99], v[116:119]\n"
    "s_waitcnt lgkmcnt(3)\n"
    "ds_read_b128 v[204:207], v230 offset:2048\n"
    "v_cvt_pk_bf16_f32 v188, v164, v165\n"
    "v_mfma_f32_16x16x32_bf16 v[120:123], v[212:215], v[112:115], v[120:123]\n"
    "s_add_u32 s60, s60, 16384\n"
    "v_exp_f32 v176, v176\n"
    "v_mfma_f32_16x16x32_bf16 v[124:127], v[216:219], v[96:99], v[124:127]\n"
    "ds_read_b128 v[208:211], v230 offset:3072\n"
    "v_cvt_pk_bf16_f32 v189, v166, v167\n"
    "v_mfma_f32_16x16x32_bf16 v[128:131], v[216:219], v[112:115], v[128:131]\n"
    "s_and_b32 s60, s60, 0xffff\n"
    "v_exp_f32 v177, v177\n"
    "v_mfma_f32_16x16x32_bf16 v[132:135], v[220:223], v[96:99], v[132:135]\n"
    "s_waitcnt lgkmcnt(3)\n"
    "ds_read_b128 v[212:215], v230 offset:4096\n"
    "v_cvt_pk_bf16_f32 v190, v172, v173\n"
    "v_mfma_f32_16x16x32_bf16 v[136:139], v[220:223], v[112:115], v[136:139]\n"
    "v_exp_f32 v178, v178\n"
    "v_mfma_f32_16x16x32_bf16 v[140:143], v[224:227], v[96:99], v[140:143]\n"
    "ds_read_b128 v[216:219], v230 offset:5120\n"
    "v_cvt_pk_bf16_f32 v191, v174, v175\n"
    "v_mfma_f32_16x16x32_bf16 v[144:147], v[224:227], v[112:115], v[144:147]\n"
    "s_or_b32 m0, s61, 0x10000\n"
    "v_exp_f32 v179, v179\n"
    "v_cmp_gt_i32_e64 s[64:65], 0, v241\n"
    "v_cmp_gt_i32_e64 s[66:67], 1, v241\n"
    "v_cmp_gt_i32_e64 s[68:69], 2, v241\n"
    "v_cmp_gt_i32_e64 s[70:71], 3, v241\n"
    "v_cndmask_b32_e64 v116, v116, v233, s[64:65]\n"
    "v_cndmask_b32_e64 v117, v117, v233, s[66:67]\n"
    "v_cndmask_b32_e64 v118, v118, v233, s[68:69]\n"
    "v_cndmask_b32_e64 v119, v119, v233, s[70:71]\n"
    "v_cmp_gt_i32_e64 s[64:65], -16, v241\n"
    "v_cmp_gt_i32_e64 s[66:67], -15, v241\n"
    "v_cmp_gt_i32_e64 s[68:69], -14, v241\n"
    "v_cmp_gt_i32_e64 s[70:71], -13, v241\n"
    "v_cndmask_b32_e64 v120, v120, v233, s[64:65]\n"
    "v_cndmask_b32_e64 v121, v121, v233, s[66:67]\n"
    "v_cndmask_b32_e64 v122, v122, v233, s[68:69]\n"
    "v_cndmask_b32_e64 v123, v123, v233, s[70:71]\n"
    "v_cmp_gt_i32_e64 s[64:65], 16, v241\n"
    "v_cmp_gt_i32_e64 s[66:67], 17, v241\n"
    "v_cmp_gt_i32_e64 s[68:69], 18, v241\n"
    "v_cmp_gt_i32_e64 s[70:71], 19, v241\n"
    "v_cndmask_b32_e64 v124, v124, v233, s[64:65]\n"
    "v_cndmask_b32_e64 v125, v125, v233, s[66:67]\n"
    "v_cndmask_b32_e64 v126, v126, v233, s[68:69]\n"
    "v_cndmask_b32_e64 v127, v127, v233, s[70:71]\n"
    "v_cmp_gt_i32_e64 s[64:65], 0, v241\n"
    "v_cmp_gt_i32_e64 s[66:67], 1, v241\n"
    "v_cmp_gt_i32_e64 s[68:69], 2, v241\n"
    "v_cmp_gt_i32_e64 s[70:71], 3, v241\n"
    "v_cndmask_b32_e64 v128, v128, v233, s[64:65]\n"
    "v_cndmask_b32_e64 v129, v129, v233, s[66:67]\n"
    "v_cndmask_b32_e64 v130, v130, v233, s[68:69]\n"
    "v_cndmask_b32_e64 v131, v131, v233, s[70:71]\n"
    "v_cmp_gt_i32_e64 s[64:65], 32, v241\n"
    "v_cmp_gt_i32_e64 s[66:67], 33, v241\n"
    "v_cmp_gt_i32_e64 s[68:69], 34, v241\n"
    "v_cmp_gt_i32_e64 s[70:71], 35, v241\n"
    "v_cndmask_b32_e64 v132, v132, v233, s[64:65]\n"
    "v_cndmask_b32_e64 v133, v133, v233, s[66:67]\n"
    "v_cndmask_b32_e64 v134, v134, v233, s[68:69]\n"
    "v_cndmask_b32_e64 v135, v135, v233, s[70:71]\n"
    "v_cmp_gt_i32_e64 s[64:65], 16, v241\n"
    "v_cmp_gt_i32_e64 s[66:67], 17, v241\n"
    "v_cmp_gt_i32_e64 s[68:69], 18, v241\n"
    "v_cmp_gt_i32_e64 s[70:71], 19, v241\n"
    "v_cndmask_b32_e64 v136, v136, v233, s[64:65]\n"
    "v_cndmask_b32_e64 v137, v137, v233, s[66:67]\n"
    "v_cndmask_b32_e64 v138, v138, v233, s[68:69]\n"
    "v_cndmask_b32_e64 v139, v139, v233, s[70:71]\n"
    "v_cmp_gt_i32_e64 s[64:65], 48, v241\n"
    "v_cmp_gt_i32_e64 s[66:67], 49, v241\n"
    "v_cmp_gt_i32_e64 s[68:69], 50, v241\n"
    "v_cmp_gt_i32_e64 s[70:71], 51, v241\n"
    "v_cndmask_b32_e64 v140, v140, v233, s[64:65]\n"
    "v_cndmask_b32_e64 v141, v141, v233, s[66:67]\n"
    "v_cndmask_b32_e64 v142, v142, v233, s[68:69]\n"
    "v_cndmask_b32_e64 v143, v143, v233, s[70:71]\n"
    "v_cmp_gt_i32_e64 s[64:65], 32, v241\n"
    "v_cmp_gt_i32_e64 s[66:67], 33, v241\n"
    "v_cmp_gt_i32_e64 s[68:69], 34, v241\n"
    "v_cmp_gt_i32_e64 s[70:71], 35, v241\n"
    "v_cndmask_b32_e64 v144, v144, v233, s[64:65]\n"
    "v_cndmask_b32_e64 v145, v145, v233, s[66:67]\n"
    "v_cndmask_b32_e64 v146, v146, v233, s[68:69]\n"
    "v_cndmask_b32_e64 v147, v147, v233, s[70:71]\n"
    "v_mfma_f32_16x16x32_bf16 v[0:3], v[196:199], v[180:183], v[0:3]\n"
    "s_waitcnt lgkmcnt(3)\n"
    "ds_read_b128 v[220:223], v230 offset:6144\n"
    "v_cvt_pk_bf16_f32 v192, v168, v169\n"
    "v_mfma_f32_16x16x32_bf16 v[4:7], v[196:199], v[184:187], v[4:7]\n"
    "v_exp_f32 v116, v116\n"
    "v_mfma_f32_16x16x32_bf16 v[8:11], v[200:203], v[180:183], v[8:11]\n"
    "buffer_load_dwordx4 v231, s[44:47], s57 offen lds\n"
    "ds_read_b128 v[224:227], v230 offset:7168\n"
    "v_mfma_f32_16x16x32_bf16 v[12:15], v[200:203], v[184:187], v[12:15]\n"
    "v_exp_f32 v117, v117\n"
    "v_mfma_f32_16x16x32_bf16 v[16:19], v[204:207], v[180:183], v[16:19]\n"
    "s_waitcnt lgkmcnt(3)\n"
    "ds_read_b128 v[196:199], v230 offset:8192\n"
    "v_cvt_pk_bf16_f32 v193, v170, v171\n"
    "v_mfma_f32_16x16x32_bf16 v[20:23], v[204:207], v[184:187], v[20:23]\n"
    "v_exp_f32 v118, v118\n"
    "v_mfma_f32_16x16x32_bf16 v[64:67], v[80:83], v[180:183], v[64:67]\n"
    "v_add_u32 v229, 0x4000, v228\n"
    "v_and_b32 v229, 0xffff, v229\n"
    "v_mfma_f32_16x16x32_bf16 v[68:71], v[80:83], v[184:187], v[68:71]\n"
    "v_mfma_f32_16x16x32_bf16 v[24:27], v[208:211], v[180:183], v[24:27]\n"
    "buffer_load_dwordx4 v231, s[44:47], s57 offen offset:1024 lds\n"
    "ds_read_b128 v[200:203], v230 offset:9216\n"
    "v_mfma_f32_16x16x32_bf16 v[28:31], v[208:211], v[184:187], v[28:31]\n"
    "v_exp_f32 v119, v119\n"
    "v_mfma_f32_16x16x32_bf16 v[32:35], v[212:215], v[180:183], v[32:35]\n"
    "s_waitcnt lgkmcnt(3)\n"
    "ds_read_b128 v[204:207], v230 offset:10240\n"
    "v_cvt_pk_bf16_f32 v194, v176, v177\n"
    "v_mfma_f32_16x16x32_bf16 v[36:39], v[212:215], v[184:187], v[36:39]\n"
    "v_exp_f32 v124, v124\n"
    "v_mfma_f32_16x16x32_bf16 v[40:43], v[216:219], v[180:183], v[40:43]\n"
    "buffer_load_dwordx4 v231, s[44:47], s57 offen offset:2048 lds\n"
    "ds_read_b128 v[208:211], v230 offset:11264\n"
    "v_mfma_f32_16x16x32_bf16 v[44:47], v[216:219], v[184:187], v[44:47]\n"
    "v_exp_f32 v125, v125\n"
    "v_mfma_f32_16x16x32_bf16 v[48:51], v[220:223], v[180:183], v[48:51]\n"
    "s_waitcnt lgkmcnt(3)\n"
    "ds_read_b128 v[212:215], v230 offset:12288\n"
    "v_cvt_pk_bf16_f32 v195, v178, v179\n"
    "v_mfma_f32_16x16x32_bf16 v[52:55], v[220:223], v[184:187], v[52:55]\n"
    "v_exp_f32 v126, v126\n"
    "v_mfma_f32_16x16x32_bf16 v[56:59], v[224:227], v[180:183], v[56:59]\n"
    "buffer_load_dwordx4 v231, s[44:47], s57 offen offset:3072 lds\n"
    "ds_read_b128 v[216:219], v230 offset:13312\n"
    "v_mfma_f32_16x16x32_bf16 v[60:63], v[224:227], v[184:187], v[60:63]\n"
    "v_exp_f32 v127, v127\n"
    "v_mfma_f32_16x16x32_bf16 v[0:3], v[196:199], v[188:191], v[0:3]\n"
    "s_waitcnt lgkmcnt(3)\n"
    "ds_read_b128 v[220:223], v230 offset:14336\n"
    "v_mfma_f32_16x16x32_bf16 v[4:7], v[196:199], v[192:195], v[4:7]\n"
    "s_add_u32 s57, s57, 16384\n"
    "v_exp_f32 v120, v120\n"
    "v_mfma_f32_16x16x32_bf16 v[8:11], v[200:203], v[188:191], v[8:11]\n"
    "ds_read_b128 v[224:227], v230 offset:15360\n"
    "v_mfma_f32_16x16x32_bf16 v[12:15], v[200:203], v[192:195], v[12:15]\n"
    "s_cmp_eq_u32 s57, s58\n"
    "v_exp_f32 v121, v121\n"
    "v_mfma_f32_16x16x32_bf16 v[16:19], v[204:207], v[188:191], v[16:19]\n"
    "s_waitcnt lgkmcnt(3)\n"
    "ds_read_b128 v[196:199], v228 offset:0\n"
    "v_mfma_f32_16x16x32_bf16 v[20:23], v[204:207], v[192:195], v[20:23]\n"
    "s_cselect_b32 s57, s51, s57\n"
    "v_exp_f32 v122, v122\n"
    "v_mfma_f32_16x16x32_bf16 v[24:27], v[208:211], v[188:191], v[24:27]\n"
    "ds_read_b128 v[200:203], v228 offset:4096\n"
    "v_mfma_f32_16x16x32_bf16 v[28:31], v[208:211], v[192:195], v[28:31]\n"
    "s_cselect_b32 s46, s48, s46\n"
    "v_exp_f32 v123, v123\n"
    "v_mfma_f32_16x16x32_bf16 v[32:35], v[212:215], v[188:191], v[32:35]\n"
    "s_waitcnt lgkmcnt(3)\n"
    "ds_read_b128 v[204:207], v228 offset:8192\n"
    "v_mfma_f32_16x16x32_bf16 v[36:39], v[212:215], v[192:195], v[36:39]\n"
    "s_add_u32 s61, s61, 16384\n"
    "v_exp_f32 v128, v128\n"
    "v_mfma_f32_16x16x32_bf16 v[40:43], v[216:219], v[188:191], v[40:43]\n"
    "ds_read_b128 v[208:211], v228 offset:12288\n"
    "v_mfma_f32_16x16x32_bf16 v[44:47], v[216:219], v[192:195], v[44:47]\n"
    "s_and_b32 s61, s61, 0xffff\n"
    "v_exp_f32 v129, v129\n"
    "v_mfma_f32_16x16x32_bf16 v[48:51], v[220:223], v[188:191], v[48:51]\n"
    "s_waitcnt lgkmcnt(3)\n"
    "ds_read_b128 v[212:215], v228 offset:1024\n"
    "v_mfma_f32_16x16x32_bf16 v[52:55], v[220:223], v[192:195], v[52:55]\n"
    "v_exp_f32 v130, v130\n"
    "v_mfma_f32_16x16x32_bf16 v[56:59], v[224:227], v[188:191], v[56:59]\n"
    "ds_read_b128 v[216:219], v228 offset:5120\n"
    "v_mfma_f32_16x16x32_bf16 v[60:63], v[224:227], v[192:195], v[60:63]\n"
    "v_exp_f32 v131, v131\n"
    "v_mfma_f32_16x16x32_bf16 v[64:67], v[80:83], v[188:191], v[64:67]\n"
    "v_add_u32 v230, 0x4000, v230\n"
    "v_and_or_b32 v230, v230, v236, v237\n"
    "s_waitcnt vmcnt(12)\n"
    "v_mfma_f32_16x16x32_bf16 v[68:71], v[80:83], v[192:195], v[68:71]\n"
    "s_barrier\n"
    "s_nop 7\n"
    "s_waitcnt lgkmcnt(0)\n"
    "ds_read_b128 v[196:199], v230 offset:0\n"
    "ds_read_b128 v[200:203], v230 offset:1024\n"
    "ds_read_b128 v[204:207], v230 offset:2048\n"
    "ds_read_b128 v[208:211], v230 offset:3072\n"
    "ds_read_b128 v[212:215], v230 offset:4096\n"
    "ds_read_b128 v[216:219], v230 offset:5120\n"
    "ds_read_b128 v[220:223], v230 offset:6144\n"
    "ds_read_b128 v[224:227], v230 offset:7168\n"
    "v_exp_f32 v132, v132\n"
    "v_exp_f32 v133, v133\n"
    "v_exp_f32 v134, v134\n"
    "v_exp_f32 v135, v135\n"
    "v_exp_f32 v140, v140\n"
    "v_exp_f32 v141, v141\n"
    "v_exp_f32 v142, v142\n"
    "v_exp_f32 v143, v143\n"
    "v_exp_f32 v136, v136\n"
    "v_exp_f32 v137, v137\n"
    "v_exp_f32 v138, v138\n"
    "v_exp_f32 v139, v139\n"
    "v_exp_f32 v144, v144\n"
    "v_exp_f32 v145, v145\n"
    "v_exp_f32 v146, v146\n"
    "v_exp_f32 v147, v147\n"
    "v_cvt_pk_bf16_f32 v180, v116, v117\n"
    "v_cvt_pk_bf16_f32 v181, v118, v119\n"
    "v_cvt_pk_bf16_f32 v182, v124, v125\n"
    "v_cvt_pk_bf16_f32 v183, v126, v127\n"
    "v_cvt_pk_bf16_f32 v184, v120, v121\n"
    "v_cvt_pk_bf16_f32 v185, v122, v123\n"
    "v_cvt_pk_bf16_f32 v186, v128, v129\n"
    "v_cvt_pk_bf16_f32 v187, v130, v131\n"
    "v_cvt_pk_bf16_f32 v188, v132, v133\n"
    "v_cvt_pk_bf16_f32 v189, v134, v135\n"
    "v_cvt_pk_bf16_f32 v190, v140, v141\n"
    "v_cvt_pk_bf16_f32 v191, v142, v143\n"
    "v_cvt_pk_bf16_f32 v192, v136, v137\n"
    "v_cvt_pk_bf16_f32 v193, v138, v139\n"
    "v_cvt_pk_bf16_f32 v194, v144, v145\n"
    "v_cvt_pk_bf16_f32 v195, v146, v147\n"
    "s_waitcnt lgkmcnt(7)\n"
    "v_mfma_f32_16x16x32_bf16 v[0:3], v[196:199], v[180:183], v[0:3]\n"
    "v_mfma_f32_16x16x32_bf16 v[4:7], v[196:199], v[184:187], v[4:7]\n"
    "ds_read_b128 v[196:199], v230 offset:8192\n"
    "s_waitcnt lgkmcnt(7)\n"
    "v_mfma_f32_16x16x32_bf16 v[8:11], v[200:203], v[180:183], v[8:11]\n"
    "v_mfma_f32_16x16x32_bf16 v[12:15], v[200:203], v[184:187], v[12:15]\n"
    "ds_read_b128 v[200:203], v230 offset:9216\n"
    "v_mfma_f32_16x16x32_bf16 v[64:67], v[80:83], v[180:183], v[64:67]\n"
    "v_mfma_f32_16x16x32_bf16 v[68:71], v[80:83], v[184:187], v[68:71]\n"
    "s_waitcnt lgkmcnt(7)\n"
    "v_mfma_f32_16x16x32_bf16 v[16:19], v[204:207], v[180:183], v[16:19]\n"
    "v_mfma_f32_16x16x32_bf16 v[20:23], v[204:207], v[184:187], v[20:23]\n"
    "ds_read_b128 v[204:207], v230 offset:10240\n"
    "s_waitcnt lgkmcnt(7)\n"
    "v_mfma_f32_16x16x32_bf16 v[24:27], v[208:211], v[180:183], v[24:27]\n"
    "v_mfma_f32_16x16x32_bf16 v[28:31], v[208:211], v[184:187], v[28:31]\n"
    "ds_read_b128 v[208:211], v230 offset:11264\n"
    "s_waitcnt lgkmcnt(7)\n"
    "v_mfma_f32_16x16x32_bf16 v[32:35], v[212:215], v[180:183], v[32:35]\n"
    "v_mfma_f32_16x16x32_bf16 v[36:39], v[212:215], v[184:187], v[36:39]\n"
    "ds_read_b128 v[212:215], v230 offset:12288\n"
    "s_waitcnt lgkmcnt(7)\n"
    "v_mfma_f32_16x16x32_bf16 v[40:43], v[216:219], v[180:183], v[40:43]\n"
    "v_mfma_f32_16x16x32_bf16 v[44:47], v[216:219], v[184:187], v[44:47]\n"
    "ds_read_b128 v[216:219], v230 offset:13312\n"
    "s_waitcnt lgkmcnt(7)\n"
    "v_mfma_f32_16x16x32_bf16 v[48:51], v[220:223], v[180:183], v[48:51]\n"
    "v_mfma_f32_16x16x32_bf16 v[52:55], v[220:223], v[184:187], v[52:55]\n"
    "ds_read_b128 v[220:223], v230 offset:14336\n"
    "s_waitcnt lgkmcnt(7)\n"
    "v_mfma_f32_16x16x32_bf16 v[56:59], v[224:227], v[180:183], v[56:59]\n"
    "v_mfma_f32_16x16x32_bf16 v[60:63], v[224:227], v[184:187], v[60:63]\n"
    "ds_read_b128 v[224:227], v230 offset:15360\n"
    "s_waitcnt lgkmcnt(7)\n"
    "v_mfma_f32_16x16x32_bf16 v[0:3], v[196:199], v[188:191], v[0:3]\n"
    "v_mfma_f32_16x16x32_bf16 v[4:7], v[196:199], v[192:195], v[4:7]\n"
    "s_waitcnt lgkmcnt(6)\n"
    "v_mfma_f32_16x16x32_bf16 v[8:11], v[200:203], v[188:191], v[8:11]\n"
    "v_mfma_f32_16x16x32_bf16 v[12:15], v[200:203], v[192:195], v[12:15]\n"
    "v_mfma_f32_16x16x32_bf16 v[64:67], v[80:83], v[188:191], v[64:67]\n"
    "v_mfma_f32_16x16x32_bf16 v[68:71], v[80:83], v[192:195], v[68:71]\n"
    "s_waitcnt lgkmcnt(5)\n"
    "v_mfma_f32_16x16x32_bf16 v[16:19], v[204:207], v[188:191], v[16:19]\n"
    "v_mfma_f32_16x16x32_bf16 v[20:23], v[204:207], v[192:195], v[20:23]\n"
    "s_waitcnt lgkmcnt(4)\n"
    "v_mfma_f32_16x16x32_bf16 v[24:27], v[208:211], v[188:191], v[24:27]\n"
    "v_mfma_f32_16x16x32_bf16 v[28:31], v[208:211], v[192:195], v[28:31]\n"
    "s_waitcnt lgkmcnt(3)\n"
    "v_mfma_f32_16x16x32_bf16 v[32:35], v[212:215], v[188:191], v[32:35]\n"
    "v_mfma_f32_16x16x32_bf16 v[36:39], v[212:215], v[192:195], v[36:39]\n"
    "s_waitcnt lgkmcnt(2)\n"
    "v_mfma_f32_16x16x32_bf16 v[40:43], v[216:219], v[188:191], v[40:43]\n"
    "v_mfma_f32_16x16x32_bf16 v[44:47], v[216:219], v[192:195], v[44:47]\n"
    "s_waitcnt lgkmcnt(1)\n"
    "v_mfma_f32_16x16x32_bf16 v[48:51], v[220:223], v[188:191], v[48:51]\n"
    "v_mfma_f32_16x16x32_bf16 v[52:55], v[220:223], v[192:195], v[52:55]\n"
    "s_waitcnt lgkmcnt(0)\n"
    "v_mfma_f32_16x16x32_bf16 v[56:59], v[224:227], v[188:191], v[56:59]\n"
    "v_mfma_f32_16x16x32_bf16 v[60:63], v[224:227], v[192:195], v[60:63]\n"
    "s_nop 7\n"
    "s_branch LJ_FIN%=\n"
    "LJ_SKIP%=:\n"
    "s_waitcnt lgkmcnt(0)\n"
    "s_mov_b32 m0, s60\n"
    "s_nop 0\n"
    "v_cvt_pk_bf16_f32 v180, v148, v149\n"
    "v_exp_f32 v164, v164\n"
    "buffer_load_dwordx4 v231, s[40:43], s56 offen lds\n"
    "v_exp_f32 v165, v165\n"
    "v_cvt_pk_bf16_f32 v181, v150, v151\n"
    "v_exp_f32 v166, v166\n"
    "buffer_load_dwordx4 v231, s[40:43], s56 offen offset:1024 lds\n"
    "v_exp_f32 v167, v167\n"
    "v_cvt_pk_bf16_f32 v182, v156, v157\n"
    "v_exp_f32 v172, v172\n"
    "buffer_load_dwordx4 v231, s[40:43], s56 offen offset:2048 lds\n"
    "v_exp_f32 v173, v173\n"
    "v_cvt_pk_bf16_f32 v183, v158, v159\n"
    "v_exp_f32 v174, v174\n"
    "buffer_load_dwordx4 v231, s[40:43], s56 offen offset:3072 lds\n"
    "v_exp_f32 v175, v175\n"
    "v_cvt_pk_bf16_f32 v184, v152, v153\n"
    "s_add_u32 s56, s56, 16384\n"
    "v_exp_f32 v168, v168\n"
    "v_cvt_pk_bf16_f32 v185, v154, v155\n"
    "s_cmp_eq_u32 s56, s58\n"
    "v_exp_f32 v169, v169\n"
    "ds_read_b128 v[196:199], v230 offset:0\n"
    "v_cvt_pk_bf16_f32 v186, v160, v161\n"
    "s_cselect_b32 s56, s51, s56\n"
    "v_exp_f32 v170, v170\n"
    "ds_read_b128 v[200:203], v230 offset:1024\n"
    "v_cvt_pk_bf16_f32 v187, v162, v163\n"
    "s_cselect_b32 s42, s48, s42\n"
    "v_exp_f32 v171, v171\n"
    "ds_read_b128 v[204:207], v230 offset:2048\n"
    "v_cvt_pk_bf16_f32 v188, v164, v165\n"
    "s_add_u32 s60, s60, 16384\n"
    "v_exp_f32 v176, v176\n"
    "ds_read_b128 v[208:211], v230 offset:3072\n"
    "v_cvt_pk_bf16_f32 v189, v166, v167\n"
    "s_and_b32 s60, s60, 0xffff\n"
    "v_exp_f32 v177, v177\n"
    "ds_read_b128 v[212:215], v230 offset:4096\n"
    "v_cvt_pk_bf16_f32 v190, v172, v173\n"
    "v_exp_f32 v178, v178\n"
    "ds_read_b128 v[216:219], v230 offset:5120\n"
    "v_cvt_pk_bf16_f32 v191, v174, v175\n"
    "s_or_b32 m0, s61, 0x10000\n"
    "v_exp_f32 v179, v179\n"
    "s_waitcnt lgkmcnt(5)\n"
    "v_mfma_f32_16x16x32_bf16 v[0:3], v[196:199], v[180:183], v[0:3]\n"
    "s_waitcnt lgkmcnt(3)\n"
    "ds_read_b128 v[220:223], v230 offset:6144\n"
    "v_cvt_pk_bf16_f32 v192, v168, v169\n"
    "v_mfma_f32_16x16x32_bf16 v[4:7], v[196:199], v[184:187], v[4:7]\n"
    "v_mfma_f32_16x16x32_bf16 v[8:11], v[200:203], v[180:183], v[8:11]\n"
    "buffer_load_dwordx4 v231, s[44:47], s57 offen lds\n"
    "ds_read_b128 v[224:227], v230 offset:7168\n"
    "v_mfma_f32_16x16x32_bf16 v[12:15], v[200:203], v[184:187], v[12:15]\n"
    "v_mfma_f32_16x16x32_bf16 v[16:19], v[204:207], v[180:183], v[16:19]\n"
    "s_waitcnt lgkmcnt(3)\n"
    "ds_read_b128 v[196:199], v230 offset:8192\n"
    "v_cvt_pk_bf16_f32 v193, v170, v171\n"
    "v_mfma_f32_16x16x32_bf16 v[20:23], v[204:207], v[184:187], v[20:23]\n"
    "v_mfma_f32_16x16x32_bf16 v[64:67], v[80:83], v[180:183], v[64:67]\n"
    "v_add_u32 v229, 0x4000, v228\n"
    "v_and_b32 v229, 0xffff, v229\n"
    "v_mfma_f32_16x16x32_bf16 v[68:71], v[80:83], v[184:187], v[68:71]\n"
    "v_mfma_f32_16x16x32_bf16 v[24:27], v[208:211], v[180:183], v[24:27]\n"
    "buffer_load_dwordx4 v231, s[44:47], s57 offen offset:1024 lds\n"
    "ds_read_b128 v[200:203], v230 offset:9216\n"
    "v_mfma_f32_16x16x32_bf16 v[28:31], v[208:211], v[184:187], v[28:31]\n"
    "v_mfma_f32_16x16x32_bf16 v[32:35], v[212:215], v[180:183], v[32:35]\n"
    "s_waitcnt lgkmcnt(3)\n"
    "ds_read_b128 v[204:207], v230 offset:10240\n"
    "v_cvt_pk_bf16_f32 v194, v176, v177\n"
    "v_mfma_f32_16x16x32_bf16 v[36:39], v[212:215], v[184:187], v[36:39]\n"
    "v_mfma_f32_16x16x32_bf16 v[40:43], v[216:219], v[180:183], v[40:43]\n"
    "buffer_load_dwordx4 v231, s[44:47], s57 offen offset:2048 lds\n"
    "ds_read_b128 v[208:211], v230 offset:11264\n"
    "v_mfma_f32_16x16x32_bf16 v[44:47], v[216:219], v[184:187], v[44:47]\n"
    "v_mfma_f32_16x16x32_bf16 v[48:51], v[220:223], v[180:183], v[48:51]\n"
    "s_waitcnt lgkmcnt(3)\n"
    "ds_read_b128 v[212:215], v230 offset:12288\n"
    "v_cvt_pk_bf16_f32 v195, v178, v179\n"
    "v_mfma_f32_16x16x32_bf16 v[52:55], v[220:223], v[184:187], v[52:55]\n"
    "v_mfma_f32_16x16x32_bf16 v[56:59], v[224:227], v[180:183], v[56:59]\n"
    "buffer_load_dwordx4 v231, s[44:47], s57 offen offset:3072 lds\n"
    "ds_read_b128 v[216:219], v230 offset:13312\n"
    "v_mfma_f32_16x16x32_bf16 v[60:63], v[224:227], v[184:187], v[60:63]\n"
    "v_mfma_f32_16x16x32_bf16 v[0:3], v[196:199], v[188:191], v[0:3]\n"
    "s_waitcnt lgkmcnt(3)\n"
    "ds_read_b128 v[220:223], v230 offset:14336\n"
    "v_mfma_f32_16x16x32_bf16 v[4:7], v[196:199], v[192:195], v[4:7]\n"
    "s_add_u32 s57, s57, 16384\n"
    "v_mfma_f32_16x16x32_bf16 v[8:11], v[200:203], v[188:191], v[8:11]\n"
    "ds_read_b128 v[224:227], v230 offset:15360\n"
    "v_mfma_f32_16x16x32_bf16 v[12:15], v[200:203], v[192:195], v[12:15]\n"
    "s_cmp_eq_u32 s57, s58\n"
    "v_mfma_f32_16x16x32_bf16 v[16:19], v[204:207], v[188:191], v[16:19]\n"
    "s_waitcnt lgkmcnt(3)\n"
    "v_mfma_f32_16x16x32_bf16 v[20:23], v[204:207], v[192:195], v[20:23]\n"
    "s_cselect_b32 s57, s51, s57\n"
    "v_mfma_f32_16x16x32_bf16 v[24:27], v[208:211], v[188:191], v[24:27]\n"
    "v_mfma_f32_16x16x32_bf16 v[28:31], v[208:211], v[192:195], v[28:31]\n"
    "s_cselect_b32 s46, s48, s46\n"
    "v_mfma_f32_16x16x32_bf16 v[32:35], v[212:215], v[188:191], v[32:35]\n"
    "s_waitcnt lgkmcnt(1)\n"
    "v_mfma_f32_16x16x32_bf16 v[36:39], v[212:215], v[192:195], v[36:39]\n"
    "s_add_u32 s61, s61, 16384\n"
    "v_mfma_f32_16x16x32_bf16 v[40:43], v[216:219], v[188:191], v[40:43]\n"
    "v_mfma_f32_16x16x32_bf16 v[44:47], v[216:219], v[192:195], v[44:47]\n"
    "s_and_b32 s61, s61, 0xffff\n"
    "v_mfma_f32_16x16x32_bf16 v[48:51], v[220:223], v[188:191], v[48:51]\n"
    "s_waitcnt lgkmcnt(0)\n"
    "v_mfma_f32_16x16x32_bf16 v[52:55], v[220:223], v[192:195], v[52:55]\n"
    "v_mfma_f32_16x16x32_bf16 v[56:59], v[224:227], v[188:191], v[56:59]\n"
    "v_mfma_f32_16x16x32_bf16 v[60:63], v[224:227], v[192:195], v[60:63]\n"
    "v_mfma_f32_16x16x32_bf16 v[64:67], v[80:83], v[188:191], v[64:67]\n"
    "v_add_u32 v230, 0x4000, v230\n"
    "v_and_or_b32 v230, v230, v236, v237\n"
    "s_waitcnt vmcnt(12)\n"
    "v_mfma_f32_16x16x32_bf16 v[68:71], v[80:83], v[192:195], v[68:71]\n"
    "s_barrier\n"
    "s_nop 6\n"
    "LJ_FIN%=:\n"
    "v_rcp_f32 v180, v64\n"
    "v_rcp_f32 v181, v64\n"
    "v_cmp_nlt_f32 vcc, v64, v240\n"
    "s_nop 0\n"
    "s_cmp_lg_u64 vcc, 0\n"
    "s_cselect_b32 s58, 1, 0\n"
    "s_lshl_b32 s58, s58, s55\n"
    "s_or_b32 s76, s76, s58\n"
    "v_rcp_f32 v182, v68\n"
    "v_rcp_f32 v183, v68\n"
    "v_cmp_nlt_f32 vcc, v68, v240\n"
    "s_nop 0\n"
    "s_cmp_lg_u64 vcc, 0\n"
    "s_cselect_b32 s58, 1, 0\n"
    "s_lshl_b32 s58, s58, s55\n"
    "s_or_b32 s76, s76, s58\n"
    "v_pk_mul_f32 v[0:1], v[0:1], v[180:181]\n"
    "v_pk_mul_f32 v[2:3], v[2:3], v[180:181]\n"
    "v_cvt_pk_bf16_f32 v196, v0, v1\n"
    "v_cvt_pk_bf16_f32 v197, v2, v3\n"
    "v_pk_mul_f32 v[8:9], v[8:9], v[180:181]\n"
    "v_pk_mul_f32 v[10:11], v[10:11], v[180:181]\n"
    "v_cvt_pk_bf16_f32 v198, v8, v9\n"
    "v_cvt_pk_bf16_f32 v199, v10, v11\n"
    "s_add_u32 s63, s62, 0\n"
    "buffer_store_dwordx4 v[196:199], v231, s[72:75], s63 offen offset:0 sc1\n"
    "v_pk_mul_f32 v[16:17], v[16:17], v[180:181]\n"
    "v_pk_mul_f32 v[18:19], v[18:19], v[180:181]\n"
    "v_cvt_pk_bf16_f32 v200, v16, v17\n"
    "v_cvt_pk_bf16_f32 v201, v18, v19\n"
    "v_pk_mul_f32 v[24:25], v[24:25], v[180:181]\n"
    "v_pk_mul_f32 v[26:27], v[26:27], v[180:181]\n"
    "v_cvt_pk_bf16_f32 v202, v24, v25\n"
    "v_cvt_pk_bf16_f32 v203, v26, v27\n"
    "buffer_store_dwordx4 v[200:203], v231, s[72:75], s63 offen offset:1024 sc1\n"
    "v_pk_mul_f32 v[32:33], v[32:33], v[180:181]\n"
    "v_pk_mul_f32 v[34:35], v[34:35], v[180:181]\n"
    "v_cvt_pk_bf16_f32 v204, v32, v33\n"
    "v_cvt_pk_bf16_f32 v205, v34, v35\n"
    "v_pk_mul_f32 v[40:41], v[40:41], v[180:181]\n"
    "v_pk_mul_f32 v[42:43], v[42:43], v[180:181]\n"
    "v_cvt_pk_bf16_f32 v206, v40, v41\n"
    "v_cvt_pk_bf16_f32 v207, v42, v43\n"
    "buffer_store_dwordx4 v[204:207], v231, s[72:75], s63 offen offset:2048 sc1\n"
    "v_pk_mul_f32 v[48:49], v[48:49], v[180:181]\n"
    "v_pk_mul_f32 v[50:51], v[50:51], v[180:181]\n"
    "v_cvt_pk_bf16_f32 v208, v48, v49\n"
    "v_cvt_pk_bf16_f32 v209, v50, v51\n"
    "v_pk_mul_f32 v[56:57], v[56:57], v[180:181]\n"
    "v_pk_mul_f32 v[58:59], v[58:59], v[180:181]\n"
    "v_cvt_pk_bf16_f32 v210, v56, v57\n"
    "v_cvt_pk_bf16_f32 v211, v58, v59\n"
    "buffer_store_dwordx4 v[208:211], v231, s[72:75], s63 offen offset:3072 sc1\n"
    "v_pk_mul_f32 v[4:5], v[4:5], v[182:183]\n"
    "v_pk_mul_f32 v[6:7], v[6:7], v[182:183]\n"
    "v_cvt_pk_bf16_f32 v212, v4, v5\n"
    "v_cvt_pk_bf16_f32 v213, v6, v7\n"
    "v_pk_mul_f32 v[12:13], v[12:13], v[182:183]\n"
    "v_pk_mul_f32 v[14:15], v[14:15], v[182:183]\n"
    "v_cvt_pk_bf16_f32 v214, v12, v13\n"
    "v_cvt_pk_bf16_f32 v215, v14, v15\n"
    "s_add_u32 s63, s62, 131072\n"
    "buffer_store_dwordx4 v[212:215], v231, s[72:75], s63 offen offset:0 sc1\n"
    "v_pk_mul_f32 v[20:21], v[20:21], v[182:183]\n"
    "v_pk_mul_f32 v[22:23], v[22:23], v[182:183]\n"
    "v_cvt_pk_bf16_f32 v216, v20, v21\n"
    "v_cvt_pk_bf16_f32 v217, v22, v23\n"
    "v_pk_mul_f32 v[28:29], v[28:29], v[182:183]\n"
    "v_pk_mul_f32 v[30:31], v[30:31], v[182:183]\n"
    "v_cvt_pk_bf16_f32 v218, v28, v29\n"
    "v_cvt_pk_bf16_f32 v219, v30, v31\n"
    "buffer_store_dwordx4 v[216:219], v231, s[72:75], s63 offen offset:1024 sc1\n"
    "v_pk_mul_f32 v[36:37], v[36:37], v[182:183]\n"
    "v_pk_mul_f32 v[38:39], v[38:39], v[182:183]\n"
    "v_cvt_pk_bf16_f32 v220, v36, v37\n"
    "v_cvt_pk_bf16_f32 v221, v38, v39\n"
    "v_pk_mul_f32 v[44:45], v[44:45], v[182:183]\n"
    "v_pk_mul_f32 v[46:47], v[46:47], v[182:183]\n"
    "v_cvt_pk_bf16_f32 v222, v44, v45\n"
    "v_cvt_pk_bf16_f32 v223, v46, v47\n"
    "buffer_store_dwordx4 v[220:223], v231, s[72:75], s63 offen offset:2048 sc1\n"
    "v_pk_mul_f32 v[52:53], v[52:53], v[182:183]\n"
    "v_pk_mul_f32 v[54:55], v[54:55], v[182:183]\n"
    "v_cvt_pk_bf16_f32 v224, v52, v53\n"
    "v_cvt_pk_bf16_f32 v225, v54, v55\n"
    "v_pk_mul_f32 v[60:61], v[60:61], v[182:183]\n"
    "v_pk_mul_f32 v[62:63], v[62:63], v[182:183]\n"
    "v_cvt_pk_bf16_f32 v226, v60, v61\n"
    "v_cvt_pk_bf16_f32 v227, v62, v63\n"
    "buffer_store_dwordx4 v[224:227], v231, s[72:75], s63 offen offset:3072 sc1\n"
    "s_cmp_eq_u32 s55, 1\n"
    "s_cbranch_scc1 LJ_END%=\n"
    "s_mov_b32 s55, 1\n"
    "s_mov_b32 s50, s77\n"
    "s_mov_b32 s62, s94\n"
    "v_accvgpr_read_b32 v84, a0\n"
    "v_accvgpr_read_b32 v85, a1\n"
    "v_accvgpr_read_b32 v86, a2\n"
    "v_accvgpr_read_b32 v87, a3\n"
    "v_accvgpr_read_b32 v88, a4\n"
    "v_accvgpr_read_b32 v89, a5\n"
    "v_accvgpr_read_b32 v90, a6\n"
    "v_accvgpr_read_b32 v91, a7\n"
    "v_accvgpr_read_b32 v92, a8\n"
    "v_accvgpr_read_b32 v93, a9\n"
    "v_accvgpr_read_b32 v94, a10\n"
    "v_accvgpr_read_b32 v95, a11\n"
    "v_accvgpr_read_b32 v96, a12\n"
    "v_accvgpr_read_b32 v97, a13\n"
    "v_accvgpr_read_b32 v98, a14\n"
    "v_accvgpr_read_b32 v99, a15\n"
    "v_accvgpr_read_b32 v100, a16\n"
    "v_accvgpr_read_b32 v101, a17\n"
    "v_accvgpr_read_b32 v102, a18\n"
    "v_accvgpr_read_b32 v103, a19\n"
    "v_accvgpr_read_b32 v104, a20\n"
    "v_accvgpr_read_b32 v105, a21\n"
    "v_accvgpr_read_b32 v106, a22\n"
    "v_accvgpr_read_b32 v107, a23\n"
    "v_accvgpr_read_b32 v108, a24\n"
    "v_accvgpr_read_b32 v109, a25\n"
    "v_accvgpr_read_b32 v110, a26\n"
    "v_accvgpr_read_b32 v111, a27\n"
    "v_accvgpr_read_b32 v112, a28\n"
    "v_accvgpr_read_b32 v113, a29\n"
    "v_accvgpr_read_b32 v114, a30\n"
    "v_accvgpr_read_b32 v115, a31\n"
    "s_nop 1\n"
    "s_branch LJ_JOB%=\n"
    "LJ_END%=:\n"
    "s_waitcnt vmcnt(0)\n"
      : "={s76}"(flags), "={v242}"(dvec), "+{v[224:231]}"(A), "+{v[232:239]}"(B), "+{s[80:83]}"(sX), "+{s[84:87]}"(sW), "+{s[40:43]}"(sK), "+{s[44:47]}"(sV)
      : "{v[244:247]}"(C), "{s49}"(rfl(2 * (16 - x))), "{s51}"(rfl(wid * 4096)), "{s54}"(rfl(wid * 4096)), "{s[72:75]}"(sO),
        "{s77}"(rfl(2 * (x + 1))), "{s88}"(s88), "{s89}"(s89), "{s90}"(s90), "{s91}"(s91),
        "{s92}"(s92), "{s93}"(rfl(((15 - x) * 8 + wid * 2) * 131072)), "{s94}"(rfl((x * 8 + wid * 2) * 131072)), "{s[96:99]}"(sS),
        "{s95}"(rfl((15 - x) * 128)), "{s[100:101]}"((uint64_t)(ws + WS_CNT))
        , "{s[78:79]}"((uint64_t)(ssd + 2 * blockIdx.x))
      : "v0", "v1", "v2", "v3", "v4", "v5", "v6", "v7", "v8", "v9", "v10", "v11", "v12", "v13", "v14", "v15", "v16", "v17", "v18", "v19", "v20", "v21", "v22", "v23", "v24", "v25", "v26", "v27", "v28", "v29", "v30", "v31", "v32", "v33", "v34", "v35", "v36", "v37", "v38", "v39", "v40", "v41", "v42", "v43", "v44", "v45", "v46", "v47", "v48", "v49", "v50", "v51", "v52", "v53", "v54", "v55", "v56", "v57", "v58", "v59", "v60", "v61", "v62", "v63", "v64", "v65", "v66", "v67", "v68", "v69", "v70", "v71", "v72", "v73", "v74", "v75", "v76", "v77", "v78", "v79", "v80", "v81", "v82", "v83", "v84", "v85", "v86", "v87", "v88", "v89", "v90", "v91", "v92", "v93", "v94", "v95", "v96", "v97", "v98", "v99", "v100", "v101", "v102", "v103", "v104", "v105", "v106", "v107", "v108", "v109", "v110", "v111", "v112", "v113", "v114", "v115", "v116", "v117", "v118", "v119", "v120", "v121", "v122", "v123", "v124", "v125", "v126", "v127", "v128", "v129", "v130", "v131", "v132", "v133", "v134", "v135", "v136", "v137", "v138", "v139", "v140", "v141", "v142", "v143", "v144", "v145", "v146", "v147", "v148", "v149", "v150", "v151", "v152", "v153", "v154", "v155", "v156", "v157", "v158", "v159", "v160", "v161", "v162", "v163", "v164", "v165", "v166", "v167", "v168", "v169", "v170", "v171", "v172", "v173", "v174", "v175", "v176", "v177", "v178", "v179", "v180", "v181", "v182", "v183", "v184", "v185", "v186", "v187", "v188", "v189", "v190", "v191", "v192", "v193", "v194", "v195", "v196", "v197", "v198", "v199", "v200", "v201", "v202", "v203", "v204", "v205", "v206", "v207", "v208", "v209", "v210", "v211", "v212", "v213", "v214", "v215", "v216", "v217", "v218", "v219", "v220", "v221", "v222", "v223", "v240", "v241", "v243", "a0", "a1", "a2", "a3", "a4", "a5", "a6", "a7", "a8", "a9", "a10", "a11", "a12", "a13", "a14", "a15", "a16", "a17", "a18", "a19", "a20", "a21", "a22", "a23", "a24", "a25", "a26", "a27", "a28", "a29", "a30", "a31", "a32", "a33", "a34", "a35", "a36", "a37", "a38", "a39", "a40", "a41", "a42", "a43", "a44", "a45", "a46", "a47", "a48", "a49", "a50", "a51", "a52", "a53", "a54", "a55", "a56", "a57", "a58", "a59", "a60", "a61", "a62", "a63", "s48", "s50", "s52", "s53", "s55", "s56", "s57", "s58", "s59", "s60", "s61", "s62", "s63", "s64", "s65", "s66", "s67", "s68", "s69", "s70", "s71",
        "vcc", "scc", "memory");
    SBAR();
  }
  const unsigned dpair = rfl(dvec);
  gu32* cnt = (gu32*)(ws + WS_CNT);
#pragma unroll 1
  for (int job = 0; job < 2; ++job) {
    const unsigned blk = job ? x : 15 - x;
    const bool bad = (flags >> job) & 1;
    if (bad) attn_redo(xin, wq, ws, h, blk, wid);
    if ((job || bad) && lane_id() == 0) __hip_atomic_fetch_add(cnt + blk * 32, 1u, __ATOMIC_RELAXED, __HIP_MEMORY_SCOPE_AGENT);
  }
  LBAR();
  const unsigned tb64 = xcd * 4 + (j & 3), mb64 = j >> 2;
  WoRing WR;
  WR.srd = srd4(ws + WS_WO + mb64 * 4096, (4u << 20) - mb64 * 4096); WR.off = rfl(wid * 32 * 32768);
  gu32* ccnt = cnt + (tb64 >> 1) * 32;
  unsigned cseen;
  conv2(lds_, lds_ + 131072, dpair, ws, wid, WR, [&] {
    asm volatile("global_load_dword %0, %1, off sc1" : "={v250}"(cseen) : "v"(ccnt) : "memory");
  });
  asm volatile("s_waitcnt vmcnt(26)" : "+{v250}"(cseen) :: "memory");
  if (rfl(cseen) < NH * 4) wait_count(ccnt, NH * 4);
  asm volatile("s_waitcnt lgkmcnt(0)\n\ts_barrier" ::: "memory");
  f32x32 Aa, Ab; u32x8 av; f32x16 gv;
  {
    const unsigned lane = lane_id(), tid = wid * 64 + lane;
    const u32x4 sC = srd4(ws + WS_CF + (size_t)tb64 * 4 * NH * 4096, 4 * NH * 4096);
    const u32x4 sA = srd4(ws + WS_AT + (size_t)mb64 * 64 * 4096 + tb64 * 128, 64 * 4096 - tb64 * 128), sG = srd4(ws + WS_G + (SEQ + tb64 * 64) * 4, 256);
    const u32x4 vc = {lane * 16, lane * 16 + NH * 4096, lane * 16 + 2 * NH * 4096, lane * 16 + 3 * NH * 4096};
    SBAR();
    asm volatile(
    "s_nop 7\n"
    "s_add_u32 s52, s48, 0\n"
    "buffer_load_dwordx4 v[64:67], v232, s[40:43], s52 offen sc1\n"
    "buffer_load_dwordx4 v[68:71], v233, s[40:43], s52 offen sc1\n"
    "buffer_load_dwordx4 v[72:75], v234, s[40:43], s52 offen sc1\n"
    "buffer_load_dwordx4 v[76:79], v235, s[40:43], s52 offen sc1\n"
    "s_add_u32 s52, s48, 1024\n"
    "buffer_load_dwordx4 v[80:83], v232, s[40:43], s52 offen sc1\n"
    "buffer_load_dwordx4 v[84:87], v233, s[40:43], s52 offen sc1\n"
    "buffer_load_dwordx4 v[88:91], v234, s[40:43], s52 offen sc1\n"
    "buffer_load_dwordx4 v[92:95], v235, s[40:43], s52 offen sc1\n"
    "s_add_u32 s52, s48, 2048\n"
    "buffer_load_dwordx4 v[96:99], v232, s[40:43], s52 offen sc1\n"
    "buffer_load_dwordx4 v[100:103], v233, s[40:43], s52 offen sc1\n"
    "buffer_load_dwordx4 v[104:107], v234, s[40:43], s52 offen sc1\n"
    "buffer_load_dwordx4 v[108:111], v235, s[40:43], s52 offen sc1\n"
    "s_add_u32 s52, s48, 3072\n"
    "buffer_load_dwordx4 v[112:115], v232, s[40:43], s52 offen sc1\n"
    "buffer_load_dwordx4 v[116:119], v233, s[40:43], s52 offen sc1\n"
    "buffer_load_dwordx4 v[120:123], v234, s[40:43], s52 offen sc1\n"
    "buffer_load_dwordx4 v[124:127], v235, s[40:43], s52 offen sc1\n"
    "s_add_u32 s52, s48, 4096\n"
    "buffer_load_dwordx4 v[128:131], v232, s[40:43], s52 offen sc1\n"
    "buffer_load_dwordx4 v[132:135], v233, s[40:43], s52 offen sc1\n"
    "buffer_load_dwordx4 v[136:139], v234, s[40:43], s52 offen sc1\n"
    "buffer_load_dwordx4 v[140:143], v235, s[40:43], s52 offen sc1\n"
    "s_add_u32 s52, s48, 5120\n"
    "buffer_load_dwordx4 v[144:147], v232, s[40:43], s52 offen sc1\n"
    "buffer_load_dwordx4 v[148:151], v233, s[40:43], s52 offen sc1\n"
    "buffer_load_dwordx4 v[152:155], v234, s[40:43], s52 offen sc1\n"
    "buffer_load_dwordx4 v[156:159], v235, s[40:43], s52 offen sc1\n"
    "s_add_u32 s50, s49, 491520\n"
    "s_add_u32 s52, s48, 6144\n"
    "s_waitcnt vmcnt(23)\n"
    "v_mfma_f32_16x16x32_bf16 v[0:3], v[64:67], a[0:3], 0\n"
    "s_waitcnt vmcnt(22)\n"
    "v_mfma_f32_16x16x32_bf16 v[16:19], v[68:71], a[0:3], 0\n"
    "s_waitcnt vmcnt(21)\n"
    "v_mfma_f32_16x16x32_bf16 v[32:35], v[72:75], a[0:3], 0\n"
    "s_waitcnt vmcnt(20)\n"
    "v_mfma_f32_16x16x32_bf16 v[48:51], v[76:79], a[0:3], 0\n"
    "buffer_load_dwordx4 a[0:3], v236, s[44:47], s50 offen offset:0\n"
    "v_mfma_f32_16x16x32_bf16 v[4:7], v[64:67], a[4:7], 0\n"
    "v_mfma_f32_16x16x32_bf16 v[20:23], v[68:71], a[4:7], 0\n"
    "v_mfma_f32_16x16x32_bf16 v[36:39], v[72:75], a[4:7], 0\n"
    "v_mfma_f32_16x16x32_bf16 v[52:55], v[76:79], a[4:7], 0\n"
    "buffer_load_dwordx4 a[4:7], v236, s[44:47], s50 offen offset:1024\n"
    "v_mfma_f32_16x16x32_bf16 v[8:11], v[64:67], a[8:11], 0\n"
    "v_mfma_f32_16x16x32_bf16 v[24:27], v[68:71], a[8:11], 0\n"
    "v_mfma_f32_16x16x32_bf16 v[40:43], v[72:75], a[8:11], 0\n"
    "v_mfma_f32_16x16x32_bf16 v[56:59], v[76:79], a[8:11], 0\n"
    "buffer_load_dwordx4 a[8:11], v236, s[44:47], s50 offen offset:2048\n"
    "v_mfma_f32_16x16x32_bf16 v[12:15], v[64:67], a[12:15], 0\n"
    "v_mfma_f32_16x16x32_bf16 v[28:31], v[68:71], a[12:15], 0\n"
    "buffer_load_dwordx4 v[64:67], v232, s[40:43], s52 offen sc1\n"
    "v_mfma_f32_16x16x32_bf16 v[44:47], v[72:75], a[12:15], 0\n"
    "buffer_load_dwordx4 v[68:71], v233, s[40:43], s52 offen sc1\n"
    "v_mfma_f32_16x16x32_bf16 v[60:63], v[76:79], a[12:15], 0\n"
    "buffer_load_dwordx4 a[12:15], v236, s[44:47], s50 offen offset:3072\n"
    "buffer_load_dwordx4 v[72:75], v234, s[40:43], s52 offen sc1\n"
    "buffer_load_dwordx4 v[76:79], v235, s[40:43], s52 offen sc1\n"
    "s_add_u32 s50, s49, 524288\n"
    "s_add_u32 s52, s48, 7168\n"
    "s_mov_b64 exec, 1\n"
    "v_mov_b32 v239, 0\n"
    "v_mov_b32 v240, 1\n"
    "global_atomic_add v239, v240, s[66:67]\n"
    "s_mov_b64 exec, -1\n"
    "s_waitcnt vmcnt(28)\n"
    "v_mfma_f32_16x16x32_bf16 v[0:3], v[80:83], a[16:19], v[0:3]\n"
    "s_waitcnt vmcnt(27)\n"
    "v_mfma_f32_16x16x32_bf16 v[16:19], v[84:87], a[16:19], v[16:19]\n"
    "s_waitcnt vmcnt(26)\n"
    "v_mfma_f32_16x16x32_bf16 v[32:35], v[88:91], a[16:19], v[32:35]\n"
    "s_waitcnt vmcnt(25)\n"
    "v_mfma_f32_16x16x32_bf16 v[48:51], v[92:95], a[16:19], v[48:51]\n"
    "buffer_load_dwordx4 a[16:19], v236, s[44:47], s50 offen offset:0\n"
    "v_mfma_f32_16x16x32_bf16 v[4:7], v[80:83], a[20:23], v[4:7]\n"
    "v_mfma_f32_16x16x32_bf16 v[20:23], v[84:87], a[20:23], v[20:23]\n"
    "v_mfma_f32_16x16x32_bf16 v[36:39], v[88:91], a[20:23], v[36:39]\n"
    "v_mfma_f32_16x16x32_bf16 v[52:55], v[92:95], a[20:23], v[52:55]\n"
    "buffer_load_dwordx4 a[20:23], v236, s[44:47], s50 offen offset:1024\n"
    "v_mfma_f32_16x16x32_bf16 v[8:11], v[80:83], a[24:27], v[8:11]\n"
    "v_mfma_f32_16x16x32_bf16 v[24:27], v[84:87], a[24:27], v[24:27]\n"
    "v_mfma_f32_16x16x32_bf16 v[40:43], v[88:91], a[24:27], v[40:43]\n"
    "v_mfma_f32_16x16x32_bf16 v[56:59], v[92:95], a[24:27], v[56:59]\n"
    "buffer_load_dwordx4 a[24:27], v236, s[44:47], s50 offen offset:2048\n"
    "v_mfma_f32_16x16x32_bf16 v[12:15], v[80:83], a[28:31], v[12:15]\n"
    "v_mfma_f32_16x16x32_bf16 v[28:31], v[84:87], a[28:31], v[28:31]\n"
    "buffer_load_dwordx4 v[80:83], v232, s[40:43], s52 offen sc1\n"
    "v_mfma_f32_16x16x32_bf16 v[44:47], v[88:91], a[28:31], v[44:47]\n"
    "buffer_load_dwordx4 v[84:87], v233, s[40:43], s52 offen sc1\n"
    "v_mfma_f32_16x16x32_bf16 v[60:63], v[92:95], a[28:31], v[60:63]\n"
    "buffer_load_dwordx4 a[28:31], v236, s[44:47], s50 offen offset:3072\n"
    "buffer_load_dwordx4 v[88:91], v234, s[40:43], s52 offen sc1\n"
    "buffer_load_dwordx4 v[92:95], v235, s[40:43], s52 offen sc1\n"
    "s_add_u32 s50, s49, 557056\n"
    "s_add_u32 s52, s48, 8192\n"
    "s_waitcnt vmcnt(32)\n"
    "v_mfma_f32_16x16x32_bf16 v[0:3], v[96:99], a[32:35], v[0:3]\n"
    "s_waitcnt vmcnt(31)\n"
    "v_mfma_f32_16x16x32_bf16 v[16:19], v[100:103], a[32:35], v[16:19]\n"
    "s_waitcnt vmcnt(30)\n"
    "v_mfma_f32_16x16x32_bf16 v[32:35], v[104:107], a[32:35], v[32:35]\n"
    "s_waitcnt vmcnt(29)\n"
    "v_mfma_f32_16x16x32_bf16 v[48:51], v[108:111], a[32:35], v[48:51]\n"
    "buffer_load_dwordx4 a[32:35], v236, s[44:47], s50 offen offset:0\n"
    "v_mfma_f32_16x16x32_bf16 v[4:7], v[96:99], a[36:39], v[4:7]\n"
    "v_mfma_f32_16x16x32_bf16 v[20:23], v[100:103], a[36:39], v[20:23]\n"
    "v_mfma_f32_16x16x32_bf16 v[36:39], v[104:107], a[36:39], v[36:39]\n"
    "v_mfma_f32_16x16x32_bf16 v[52:55], v[108:111], a[36:39], v[52:55]\n"
    "buffer_load_dwordx4 a[36:39], v236, s[44:47], s50 offen offset:1024\n"
    "v_mfma_f32_16x16x32_bf16 v[8:11], v[96:99], a[40:43], v[8:11]\n"
    "v_mfma_f32_16x16x32_bf16 v[24:27], v[100:103], a[40:43], v[24:27]\n"
    "v_mfma_f32_16x16x32_bf16 v[40:43], v[104:107], a[40:43], v[40:43]\n"
    "v_mfma_f32_16x16x32_bf16 v[56:59], v[108:111], a[40:43], v[56:59]\n"
    "buffer_load_dwordx4 a[40:43], v236, s[44:47], s50 offen offset:2048\n"
    "v_mfma_f32_16x16x32_bf16 v[12:15], v[96:99], a[44:47], v[12:15]\n"
    "v_mfma_f32_16x16x32_bf16 v[28:31], v[100:103], a[44:47], v[28:31]\n"
    "buffer_load_dwordx4 v[96:99], v232, s[40:43], s52 offen sc1\n"
    "v_mfma_f32_16x16x32_bf16 v[44:47], v[104:107], a[44:47], v[44:47]\n"
    "buffer_load_dwordx4 v[100:103], v233, s[40:43], s52 offen sc1\n"
    "v_mfma_f32_16x16x32_bf16 v[60:63], v[108:111], a[44:47], v[60:63]\n"
    "buffer_load_dwordx4 a[44:47], v236, s[44:47], s50 offen offset:3072\n"
    "buffer_load_dwordx4 v[104:107], v234, s[40:43], s52 offen sc1\n"
    "buffer_load_dwordx4 v[108:111], v235, s[40:43], s52 offen sc1\n"
    "s_add_u32 s50, s49, 589824\n"
    "s_add_u32 s52, s48, 9216\n"
    "s_waitcnt vmcnt(36)\n"
    "v_mfma_f32_16x16x32_bf16 v[0:3], v[112:115], a[48:51], v[0:3]\n"
    "s_waitcnt vmcnt(35)\n"
    "v_mfma_f32_16x16x32_bf16 v[16:19], v[116:119], a[48:51], v[16:19]\n"
    "s_waitcnt vmcnt(34)\n"
    "v_mfma_f32_16x16x32_bf16 v[32:35], v[120:123], a[48:51], v[32:35]\n"
    "s_waitcnt vmcnt(33)\n"
    "v_mfma_f32_16x16x32_bf16 v[48:51], v[124:127], a[48:51], v[48:51]\n"
    "buffer_load_dwordx4 a[48:51], v236, s[44:47], s50 offen offset:0\n"
    "v_mfma_f32_16x16x32_bf16 v[4:7], v[112:115], a[52:55], v[4:7]\n"
    "v_mfma_f32_16x16x32_bf16 v[20:23], v[116:119], a[52:55], v[20:23]\n"
    "v_mfma_f32_16x16x32_bf16 v[36:39], v[120:123], a[52:55], v[36:39]\n"
    "v_mfma_f32_16x16x32_bf16 v[52:55], v[124:127], a[52:55], v[52:55]\n"
    "buffer_load_dwordx4 a[52:55], v236, s[44:47], s50 offen offset:1024\n"
    "v_mfma_f32_16x16x32_bf16 v[8:11], v[112:115], a[56:59], v[8:11]\n"
    "v_mfma_f32_16x16x32_bf16 v[24:27], v[116:119], a[56:59], v[24:27]\n"
    "v_mfma_f32_16x16x32_bf16 v[40:43], v[120:123], a[56:59], v[40:43]\n"
    "v_mfma_f32_16x16x32_bf16 v[56:59], v[124:127], a[56:59], v[56:59]\n"
    "buffer_load_dwordx4 a[56:59], v236, s[44:47], s50 offen offset:2048\n"
    "v_mfma_f32_16x16x32_bf16 v[12:15], v[112:115], a[60:63], v[12:15]\n"
    "v_mfma_f32_16x16x32_bf16 v[28:31], v[116:119], a[60:63], v[28:31]\n"
    "buffer_load_dwordx4 v[112:115], v232, s[40:43], s52 offen sc1\n"
    "v_mfma_f32_16x16x32_bf16 v[44:47], v[120:123], a[60:63], v[44:47]\n"
    "buffer_load_dwordx4 v[116:119], v233, s[40:43], s52 offen sc1\n"
    "v_mfma_f32_16x16x32_bf16 v[60:63], v[124:127], a[60:63], v[60:63]\n"
    "buffer_load_dwordx4 a[60:63], v236, s[44:47], s50 offen offset:3072\n"
    "buffer_load_dwordx4 v[120:123], v234, s[40:43], s52 offen sc1\n"
    "buffer_load_dwordx4 v[124:127], v235, s[40:43], s52 offen sc1\n"
    "s_add_u32 s50, s49, 622592\n"
    "s_add_u32 s52, s48, 10240\n"
    "s_waitcnt vmcnt(40)\n"
    "v_mfma_f32_16x16x32_bf16 v[0:3], v[128:131], a[64:67], v[0:3]\n"
    "s_waitcnt vmcnt(39)\n"
    "v_mfma_f32_16x16x32_bf16 v[16:19], v[132:135], a[64:67], v[16:19]\n"
    "s_waitcnt vmcnt(38)\n"
    "v_mfma_f32_16x16x32_bf16 v[32:35], v[136:139], a[64:67], v[32:35]\n"
    "s_waitcnt vmcnt(37)\n"
    "v_mfma_f32_16x16x32_bf16 v[48:51], v[140:143], a[64:67], v[48:51]\n"
    "buffer_load_dwordx4 a[64:67], v236, s[44:47], s50 offen offset:0\n"
    "v_mfma_f32_16x16x32_bf16 v[4:7], v[128:131], a[68:71], v[4:7]\n"
    "v_mfma_f32_16x16x32_bf16 v[20:23], v[132:135], a[68:71], v[20:23]\n"
    "v_mfma_f32_16x16x32_bf16 v[36:39], v[136:139], a[68:71], v[36:39]\n"
    "v_mfma_f32_16x16x32_bf16 v[52:55], v[140:143], a[68:71], v[52:55]\n"
    "buffer_load_dwordx4 a[68:71], v236, s[44:47], s50 offen offset:1024\n"
    "v_mfma_f32_16x16x32_bf16 v[8:11], v[128:131], a[72:75], v[8:11]\n"
    "v_mfma_f32_16x16x32_bf16 v[24:27], v[132:135], a[72:75], v[24:27]\n"
    "v_mfma_f32_16x16x32_bf16 v[40:43], v[136:139], a[72:75], v[40:43]\n"
    "v_mfma_f32_16x16x32_bf16 v[56:59], v[140:143], a[72:75], v[56:59]\n"
    "buffer_load_dwordx4 a[72:75], v236, s[44:47], s50 offen offset:2048\n"
    "v_mfma_f32_16x16x32_bf16 v[12:15], v[128:131], a[76:79], v[12:15]\n"
    "v_mfma_f32_16x16x32_bf16 v[28:31], v[132:135], a[76:79], v[28:31]\n"
    "buffer_load_dwordx4 v[128:131], v232, s[40:43], s52 offen sc1\n"
    "v_mfma_f32_16x16x32_bf16 v[44:47], v[136:139], a[76:79], v[44:47]\n"
    "buffer_load_dwordx4 v[132:135], v233, s[40:43], s52 offen sc1\n"
    "v_mfma_f32_16x16x32_bf16 v[60:63], v[140:143], a[76:79], v[60:63]\n"
    "buffer_load_dwordx4 a[76:79], v236, s[44:47], s50 offen offset:3072\n"
    "buffer_load_dwordx4 v[136:139], v234, s[40:43], s52 offen sc1\n"
    "buffer_load_dwordx4 v[140:143], v235, s[40:43], s52 offen sc1\n"
    "s_add_u32 s50, s49, 655360\n"
    "s_add_u32 s52, s48, 11264\n"
    "s_waitcnt vmcnt(44)\n"
    "v_mfma_f32_16x16x32_bf16 v[0:3], v[144:147], a[80:83], v[0:3]\n"
    "s_waitcnt vmcnt(43)\n"
    "v_mfma_f32_16x16x32_bf16 v[16:19], v[148:151], a[80:83], v[16:19]\n"
    "s_waitcnt vmcnt(42)\n"
    "v_mfma_f32_16x16x32_bf16 v[32:35], v[152:155], a[80:83], v[32:35]\n"
    "s_waitcnt vmcnt(41)\n"
    "v_mfma_f32_16x16x32_bf16 v[48:51], v[156:159], a[80:83], v[48:51]\n"
    "buffer_load_dwordx4 a[80:83], v236, s[44:47], s50 offen offset:0\n"
    "v_mfma_f32_16x16x32_bf16 v[4:7], v[144:147], a[84:87], v[4:7]\n"
    "v_mfma_f32_16x16x32_bf16 v[20:23], v[148:151], a[84:87], v[20:23]\n"
    "v_mfma_f32_16x16x32_bf16 v[36:39], v[152:155], a[84:87], v[36:39]\n"
    "v_mfma_f32_16x16x32_bf16 v[52:55], v[156:159], a[84:87], v[52:55]\n"
    "buffer_load_dwordx4 a[84:87], v236, s[44:47], s50 offen offset:1024\n"
    "v_mfma_f32_16x16x32_bf16 v[8:11], v[144:147], a[88:91], v[8:11]\n"
    "v_mfma_f32_16x16x32_bf16 v[24:27], v[148:151], a[88:91], v[24:27]\n"
    "v_mfma_f32_16x16x32_bf16 v[40:43], v[152:155], a[88:91], v[40:43]\n"
    "v_mfma_f32_16x16x32_bf16 v[56:59], v[156:159], a[88:91], v[56:59]\n"
    "buffer_load_dwordx4 a[88:91], v236, s[44:47], s50 offen offset:2048\n"
    "v_mfma_f32_16x16x32_bf16 v[12:15], v[144:147], a[92:95], v[12:15]\n"
    "v_mfma_f32_16x16x32_bf16 v[28:31], v[148:151], a[92:95], v[28:31]\n"
    "buffer_load_dwordx4 v[144:147], v232, s[40:43], s52 offen sc1\n"
    "v_mfma_f32_16x16x32_bf16 v[44:47], v[152:155], a[92:95], v[44:47]\n"
    "buffer_load_dwordx4 v[148:151], v233, s[40:43], s52 offen sc1\n"
    "v_mfma_f32_16x16x32_bf16 v[60:63], v[156:159], a[92:95], v[60:63]\n"
    "buffer_load_dwordx4 a[92:95], v236, s[44:47], s50 offen offset:3072\n"
    "buffer_load_dwordx4 v[152:155], v234, s[40:43], s52 offen sc1\n"
    "buffer_load_dwordx4 v[156:159], v235, s[40:43], s52 offen sc1\n"
    "s_add_u32 s50, s49, 688128\n"
    "s_add_u32 s52, s48, 12288\n"
    "s_waitcnt vmcnt(45)\n"
    "v_mfma_f32_16x16x32_bf16 v[0:3], v[64:67], a[96:99], v[0:3]\n"
    "s_waitcnt vmcnt(44)\n"
    "v_mfma_f32_16x16x32_bf16 v[16:19], v[68:71], a[96:99], v[16:19]\n"
    "s_waitcnt vmcnt(42)\n"
    "v_mfma_f32_16x16x32_bf16 v[32:35], v[72:75], a[96:99], v[32:35]\n"
    "s_waitcnt vmcnt(41)\n"
    "v_mfma_f32_16x16x32_bf16 v[48:51], v[76:79], a[96:99], v[48:51]\n"
    "buffer_load_dwordx4 a[96:99], v236, s[44:47], s50 offen offset:0\n"
    "v_mfma_f32_16x16x32_bf16 v[4:7], v[64:67], a[100:103], v[4:7]\n"
    "v_mfma_f32_16x16x32_bf16 v[20:23], v[68:71], a[100:103], v[20:23]\n"
    "v_mfma_f32_16x16x32_bf16 v[36:39], v[72:75], a[100:103], v[36:39]\n"
    "v_mfma_f32_16x16x32_bf16 v[52:55], v[76:79], a[100:103], v[52:55]\n"
    "buffer_load_dwordx4 a[100:103], v236, s[44:47], s50 offen offset:1024\n"
    "v_mfma_f32_16x16x32_bf16 v[8:11], v[64:67], a[104:107], v[8:11]\n"
    "v_mfma_f32_16x16x32_bf16 v[24:27], v[68:71], a[104:107], v[24:27]\n"
    "v_mfma_f32_16x16x32_bf16 v[40:43], v[72:75], a[104:107], v[40:43]\n"
    "v_mfma_f32_16x16x32_bf16 v[56:59], v[76:79], a[104:107], v[56:59]\n"
    "buffer_load_dwordx4 a[104:107], v236, s[44:47], s50 offen offset:2048\n"
    "v_mfma_f32_16x16x32_bf16 v[12:15], v[64:67], a[108:111], v[12:15]\n"
    "v_mfma_f32_16x16x32_bf16 v[28:31], v[68:71], a[108:111], v[28:31]\n"
    "buffer_load_dwordx4 v[64:67], v232, s[40:43], s52 offen sc1\n"
    "v_mfma_f32_16x16x32_bf16 v[44:47], v[72:75], a[108:111], v[44:47]\n"
    "buffer_load_dwordx4 v[68:71], v233, s[40:43], s52 offen sc1\n"
    "v_mfma_f32_16x16x32_bf16 v[60:63], v[76:79], a[108:111], v[60:63]\n"
    "buffer_load_dwordx4 a[108:111], v236, s[44:47], s50 offen offset:3072\n"
    "buffer_load_dwordx4 v[72:75], v234, s[40:43], s52 offen sc1\n"
    "buffer_load_dwordx4 v[76:79], v235, s[40:43], s52 offen sc1\n"
    "s_add_u32 s50, s49, 720896\n"
    "s_add_u32 s52, s48, 13312\n"
    "s_waitcnt vmcnt(44)\n"
    "v_mfma_f32_16x16x32_bf16 v[0:3], v[80:83], a[112:115], v[0:3]\n"
    "s_waitcnt vmcnt(43)\n"
    "v_mfma_f32_16x16x32_bf16 v[16:19], v[84:87], a[112:115], v[16:19]\n"
    "s_waitcnt vmcnt(41)\n"
    "v_mfma_f32_16x16x32_bf16 v[32:35], v[88:91], a[112:115], v[32:35]\n"
    "s_waitcnt vmcnt(40)\n"
    "v_mfma_f32_16x16x32_bf16 v[48:51], v[92:95], a[112:115], v[48:51]\n"
    "buffer_load_dwordx4 a[112:115], v236, s[44:47], s50 offen offset:0\n"
    "v_mfma_f32_16x16x32_bf16 v[4:7], v[80:83], a[116:119], v[4:7]\n"
    "v_mfma_f32_16x16x32_bf16 v[20:23], v[84:87], a[116:119], v[20:23]\n"
    "v_mfma_f32_16x16x32_bf16 v[36:39], v[88:91], a[116:119], v[36:39]\n"
    "v_mfma_f32_16x16x32_bf16 v[52:55], v[92:95], a[116:119], v[52:55]\n"
    "buffer_load_dwordx4 a[116:119], v236, s[44:47], s50 offen offset:1024\n"
    "v_mfma_f32_16x16x32_bf16 v[8:11], v[80:83], a[120:123], v[8:11]\n"
    "v_mfma_f32_16x16x32_bf16 v[24:27], v[84:87], a[120:123], v[24:27]\n"
    "v_mfma_f32_16x16x32_bf16 v[40:43], v[88:91], a[120:123], v[40:43]\n"
    "v_mfma_f32_16x16x32_bf16 v[56:59], v[92:95], a[120:123], v[56:59]\n"
    "buffer_load_dwordx4 a[120:123], v236, s[44:47], s50 offen offset:2048\n"
    "v_mfma_f32_16x16x32_bf16 v[12:15], v[80:83], a[124:127], v[12:15]\n"
    "v_mfma_f32_16x16x32_bf16 v[28:31], v[84:87], a[124:127], v[28:31]\n"
    "buffer_load_dwordx4 v[80:83], v232, s[40:43], s52 offen sc1\n"
    "v_mfma_f32_16x16x32_bf16 v[44:47], v[88:91], a[124:127], v[44:47]\n"
    "buffer_load_dwordx4 v[84:87], v233, s[40:43], s52 offen sc1\n"
    "v_mfma_f32_16x16x32_bf16 v[60:63], v[92:95], a[124:127], v[60:63]\n"
    "buffer_load_dwordx4 a[124:127], v236, s[44:47], s50 offen offset:3072\n"
    "buffer_load_dwordx4 v[88:91], v234, s[40:43], s52 offen sc1\n"
    "buffer_load_dwordx4 v[92:95], v235, s[40:43], s52 offen sc1\n"
    "s_add_u32 s50, s49, 753664\n"
    "s_add_u32 s52, s48, 14336\n"
    "s_waitcnt vmcnt(44)\n"
    "v_mfma_f32_16x16x32_bf16 v[0:3], v[96:99], a[128:131], v[0:3]\n"
    "s_waitcnt vmcnt(43)\n"
    "v_mfma_f32_16x16x32_bf16 v[16:19], v[100:103], a[128:131], v[16:19]\n"
    "s_waitcnt vmcnt(41)\n"
    "v_mfma_f32_16x16x32_bf16 v[32:35], v[104:107], a[128:131], v[32:35]\n"
    "s_waitcnt vmcnt(40)\n"
    "v_mfma_f32_16x16x32_bf16 v[48:51], v[108:111], a[128:131], v[48:51]\n"
    "buffer_load_dwordx4 a[128:131], v236, s[44:47], s50 offen offset:0\n"
    "v_mfma_f32_16x16x32_bf16 v[4:7], v[96:99], a[132:135], v[4:7]\n"
    "v_mfma_f32_16x16x32_bf16 v[20:23], v[100:103], a[132:135], v[20:23]\n"
    "v_mfma_f32_16x16x32_bf16 v[36:39], v[104:107], a[132:135], v[36:39]\n"
    "v_mfma_f32_16x16x32_bf16 v[52:55], v[108:111], a[132:135], v[52:55]\n"
    "buffer_load_dwordx4 a[132:135], v236, s[44:47], s50 offen offset:1024\n"
    "v_mfma_f32_16x16x32_bf16 v[8:11], v[96:99], a[136:139], v[8:11]\n"
    "v_mfma_f32_16x16x32_bf16 v[24:27], v[100:103], a[136:139], v[24:27]\n"
    "v_mfma_f32_16x16x32_bf16 v[40:43], v[104:107], a[136:139], v[40:43]\n"
    "v_mfma_f32_16x16x32_bf16 v[56:59], v[108:111], a[136:139], v[56:59]\n"
    "buffer_load_dwordx4 a[136:139], v236, s[44:47], s50 offen offset:2048\n"
    "v_mfma_f32_16x16x32_bf16 v[12:15], v[96:99], a[140:143], v[12:15]\n"
    "v_mfma_f32_16x16x32_bf16 v[28:31], v[100:103], a[140:143], v[28:31]\n"
    "buffer_load_dwordx4 v[96:99], v232, s[40:43], s52 offen sc1\n"
    "v_mfma_f32_16x16x32_bf16 v[44:47], v[104:107], a[140:143], v[44:47]\n"
    "buffer_load_dwordx4 v[100:103], v233, s[40:43], s52 offen sc1\n"
    "v_mfma_f32_16x16x32_bf16 v[60:63], v[108:111], a[140:143], v[60:63]\n"
    "buffer_load_dwordx4 a[140:143], v236, s[44:47], s50 offen offset:3072\n"
    "buffer_load_dwordx4 v[104:107], v234, s[40:43], s52 offen sc1\n"
    "buffer_load_dwordx4 v[108:111], v235, s[40:43], s52 offen sc1\n"
    "s_add_u32 s50, s49, 786432\n"
    "s_add_u32 s52, s48, 15360\n"
    "s_waitcnt vmcnt(44)\n"
    "v_mfma_f32_16x16x32_bf16 v[0:3], v[112:115], a[144:147], v[0:3]\n"
    "s_waitcnt vmcnt(43)\n"
    "v_mfma_f32_16x16x32_bf16 v[16:19], v[116:119], a[144:147], v[16:19]\n"
    "s_waitcnt vmcnt(41)\n"
    "v_mfma_f32_16x16x32_bf16 v[32:35], v[120:123], a[144:147], v[32:35]\n"
    "s_waitcnt vmcnt(40)\n"
    "v_mfma_f32_16x16x32_bf16 v[48:51], v[124:127], a[144:147], v[48:51]\n"
    "buffer_load_dwordx4 a[144:147], v236, s[44:47], s50 offen offset:0\n"
    "v_mfma_f32_16x16x32_bf16 v[4:7], v[112:115], a[148:151], v[4:7]\n"
    "v_mfma_f32_16x16x32_bf16 v[20:23], v[116:119], a[148:151], v[20:23]\n"
    "v_mfma_f32_16x16x32_bf16 v[36:39], v[120:123], a[148:151], v[36:39]\n"
    "v_mfma_f32_16x16x32_bf16 v[52:55], v[124:127], a[148:151], v[52:55]\n"
    "buffer_load_dwordx4 a[148:151], v236, s[44:47], s50 offen offset:1024\n"
    "v_mfma_f32_16x16x32_bf16 v[8:11], v[112:115], a[152:155], v[8:11]\n"
    "v_mfma_f32_16x16x32_bf16 v[24:27], v[116:119], a[152:155], v[24:27]\n"
    "v_mfma_f32_16x16x32_bf16 v[40:43], v[120:123], a[152:155], v[40:43]\n"
    "v_mfma_f32_16x16x32_bf16 v[56:59], v[124:127], a[152:155], v[56:59]\n"
    "buffer_load_dwordx4 a[152:155], v236, s[44:47], s50 offen offset:2048\n"
    "v_mfma_f32_16x16x32_bf16 v[12:15], v[112:115], a[156:159], v[12:15]\n"
    "v_mfma_f32_16x16x32_bf16 v[28:31], v[116:119], a[156:159], v[28:31]\n"
    "buffer_load_dwordx4 v[112:115], v232, s[40:43], s52 offen sc1\n"
    "v_mfma_f32_16x16x32_bf16 v[44:47], v[120:123], a[156:159], v[44:47]\n"
    "buffer_load_dwordx4 v[116:119], v233, s[40:43], s52 offen sc1\n"
    "v_mfma_f32_16x16x32_bf16 v[60:63], v[124:127], a[156:159], v[60:63]\n"
    "buffer_load_dwordx4 a[156:159], v236, s[44:47], s50 offen offset:3072\n"
    "buffer_load_dwordx4 v[120:123], v234, s[40:43], s52 offen sc1\n"
    "buffer_load_dwordx4 v[124:127], v235, s[40:43], s52 offen sc1\n"
    "s_add_u32 s50, s49, 819200\n"
    "s_add_u32 s52, s48, 16384\n"
    "s_waitcnt vmcnt(44)\n"
    "v_mfma_f32_16x16x32_bf16 v[0:3], v[128:131], a[160:163], v[0:3]\n"
    "s_waitcnt vmcnt(43)\n"
    "v_mfma_f32_16x16x32_bf16 v[16:19], v[132:135], a[160:163], v[16:19]\n"
    "s_waitcnt vmcnt(41)\n"
    "v_mfma_f32_16x16x32_bf16 v[32:35], v[136:139], a[160:163], v[32:35]\n"
    "s_waitcnt vmcnt(40)\n"
    "v_mfma_f32_16x16x32_bf16 v[48:51], v[140:143], a[160:163], v[48:51]\n"
    "buffer_load_dwordx4 a[160:163], v236, s[44:47], s50 offen offset:0\n"
    "v_mfma_f32_16x16x32_bf16 v[4:7], v[128:131], a[164:167], v[4:7]\n"
    "v_mfma_f32_16x16x32_bf16 v[20:23], v[132:135], a[164:167], v[20:23]\n"
    "v_mfma_f32_16x16x32_bf16 v[36:39], v[136:139], a[164:167], v[36:39]\n"
    "v_mfma_f32_16x16x32_bf16 v[52:55], v[140:143], a[164:167], v[52:55]\n"
    "buffer_load_dwordx4 a[164:167], v236, s[44:47], s50 offen offset:1024\n"
    "v_mfma_f32_16x16x32_bf16 v[8:11], v[128:131], a[168:171], v[8:11]\n"
    "v_mfma_f32_16x16x32_bf16 v[24:27], v[132:135], a[168:171], v[24:27]\n"
    "v_mfma_f32_16x16x32_bf16 v[40:43], v[136:139], a[168:171], v[40:43]\n"
    "v_mfma_f32_16x16x32_bf16 v[56:59], v[140:143], a[168:171], v[56:59]\n"
    "buffer_load_dwordx4 a[168:171], v236, s[44:47], s50 offen offset:2048\n"
    "v_mfma_f32_16x16x32_bf16 v[12:15], v[128:131], a[172:175], v[12:15]\n"
    "v_mfma_f32_16x16x32_bf16 v[28:31], v[132:135], a[172:175], v[28:31]\n"
    "buffer_load_dwordx4 v[128:131], v232, s[40:43], s52 offen sc1\n"
    "v_mfma_f32_16x16x32_bf16 v[44:47], v[136:139], a[172:175], v[44:47]\n"
    "buffer_load_dwordx4 v[132:135], v233, s[40:43], s52 offen sc1\n"
    "v_mfma_f32_16x16x32_bf16 v[60:63], v[140:143], a[172:175], v[60:63]\n"
    "buffer_load_dwordx4 a[172:175], v236, s[44:47], s50 offen offset:3072\n"
    "buffer_load_dwordx4 v[136:139], v234, s[40:43], s52 offen sc1\n"
    "buffer_load_dwordx4 v[140:143], v235, s[40:43], s52 offen sc1\n"
    "s_add_u32 s50, s49, 851968\n"
    "s_add_u32 s52, s48, 17408\n"
    "s_waitcnt vmcnt(44)\n"
    "v_mfma_f32_16x16x32_bf16 v[0:3], v[144:147], a[176:179], v[0:3]\n"
    "s_waitcnt vmcnt(43)\n"
    "v_mfma_f32_16x16x32_bf16 v[16:19], v[148:151], a[176:179], v[16:19]\n"
    "s_waitcnt vmcnt(41)\n"
    "v_mfma_f32_16x16x32_bf16 v[32:35], v[152:155], a[176:179], v[32:35]\n"
    "s_waitcnt vmcnt(40)\n"
    "v_mfma_f32_16x16x32_bf16 v[48:51], v[156:159], a[176:179], v[48:51]\n"
    "buffer_load_dwordx4 a[176:179], v236, s[44:47], s50 offen offset:0\n"
    "v_mfma_f32_16x16x32_bf16 v[4:7], v[144:147], a[180:183], v[4:7]\n"
    "v_mfma_f32_16x16x32_bf16 v[20:23], v[148:151], a[180:183], v[20:23]\n"
    "v_mfma_f32_16x16x32_bf16 v[36:39], v[152:155], a[180:183], v[36:39]\n"
    "v_mfma_f32_16x16x32_bf16 v[52:55], v[156:159], a[180:183], v[52:55]\n"
    "buffer_load_dwordx4 a[180:183], v236, s[44:47], s50 offen offset:1024\n"
    "v_mfma_f32_16x16x32_bf16 v[8:11], v[144:147], a[184:187], v[8:11]\n"
    "v_mfma_f32_16x16x32_bf16 v[24:27], v[148:151], a[184:187], v[24:27]\n"
    "v_mfma_f32_16x16x32_bf16 v[40:43], v[152:155], a[184:187], v[40:43]\n"
    "v_mfma_f32_16x16x32_bf16 v[56:59], v[156:159], a[184:187], v[56:59]\n"
    "buffer_load_dwordx4 a[184:187], v236, s[44:47], s50 offen offset:2048\n"
    "v_mfma_f32_16x16x32_bf16 v[12:15], v[144:147], a[188:191], v[12:15]\n"
    "v_mfma_f32_16x16x32_bf16 v[28:31], v[148:151], a[188:191], v[28:31]\n"
    "buffer_load_dwordx4 v[144:147], v232, s[40:43], s52 offen sc1\n"
    "v_mfma_f32_16x16x32_bf16 v[44:47], v[152:155], a[188:191], v[44:47]\n"
    "buffer_load_dwordx4 v[148:151], v233, s[40:43], s52 offen sc1\n"
    "v_mfma_f32_16x16x32_bf16 v[60:63], v[156:159], a[188:191], v[60:63]\n"
    "buffer_load_dwordx4 a[188:191], v236, s[44:47], s50 offen offset:3072\n"
    "buffer_load_dwordx4 v[152:155], v234, s[40:43], s52 offen sc1\n"
    "buffer_load_dwordx4 v[156:159], v235, s[40:43], s52 offen sc1\n"
    "s_add_u32 s50, s49, 884736\n"
    "s_add_u32 s52, s48, 18432\n"
    "s_waitcnt vmcnt(44)\n"
    "v_mfma_f32_16x16x32_bf16 v[0:3], v[64:67], a[192:195], v[0:3]\n"
    "s_waitcnt vmcnt(43)\n"
    "v_mfma_f32_16x16x32_bf16 v[16:19], v[68:71], a[192:195], v[16:19]\n"
    "s_waitcnt vmcnt(41)\n"
    "v_mfma_f32_16x16x32_bf16 v[32:35], v[72:75], a[192:195], v[32:35]\n"
    "s_waitcnt vmcnt(40)\n"
    "v_mfma_f32_16x16x32_bf16 v[48:51], v[76:79], a[192:195], v[48:51]\n"
    "buffer_load_dwordx4 a[192:195], v236, s[44:47], s50 offen offset:0\n"
    "v_mfma_f32_16x16x32_bf16 v[4:7], v[64:67], a[196:199], v[4:7]\n"
    "v_mfma_f32_16x16x32_bf16 v[20:23], v[68:71], a[196:199], v[20:23]\n"
    "v_mfma_f32_16x16x32_bf16 v[36:39], v[72:75], a[196:199], v[36:39]\n"
    "v_mfma_f32_16x16x32_bf16 v[52:55], v[76:79], a[196:199], v[52:55]\n"
    "buffer_load_dwordx4 a[196:199], v236, s[44:47], s50 offen offset:1024\n"
    "v_mfma_f32_16x16x32_bf16 v[8:11], v[64:67], a[200:203], v[8:11]\n"
    "v_mfma_f32_16x16x32_bf16 v[24:27], v[68:71], a[200:203], v[24:27]\n"
    "v_mfma_f32_16x16x32_bf16 v[40:43], v[72:75], a[200:203], v[40:43]\n"
    "v_mfma_f32_16x16x32_bf16 v[56:59], v[76:79], a[200:203], v[56:59]\n"
    "buffer_load_dwordx4 a[200:203], v236, s[44:47], s50 offen offset:2048\n"
    "v_mfma_f32_16x16x32_bf16 v[12:15], v[64:67], a[204:207], v[12:15]\n"
    "v_mfma_f32_16x16x32_bf16 v[28:31], v[68:71], a[204:207], v[28:31]\n"
    "buffer_load_dwordx4 v[64:67], v232, s[40:43], s52 offen sc1\n"
    "v_mfma_f32_16x16x32_bf16 v[44:47], v[72:75], a[204:207], v[44:47]\n"
    "buffer_load_dwordx4 v[68:71], v233, s[40:43], s52 offen sc1\n"
    "v_mfma_f32_16x16x32_bf16 v[60:63], v[76:79], a[204:207], v[60:63]\n"
    "buffer_load_dwordx4 a[204:207], v236, s[44:47], s50 offen offset:3072\n"
    "buffer_load_dwordx4 v[72:75], v234, s[40:43], s52 offen sc1\n"
    "buffer_load_dwordx4 v[76:79], v235, s[40:43], s52 offen sc1\n"
    "s_add_u32 s50, s49, 917504\n"
    "s_add_u32 s52, s48, 19456\n"
    "s_waitcnt vmcnt(44)\n"
    "v_mfma_f32_16x16x32_bf16 v[0:3], v[80:83], a[208:211], v[0:3]\n"
    "s_waitcnt vmcnt(43)\n"
    "v_mfma_f32_16x16x32_bf16 v[16:19], v[84:87], a[208:211], v[16:19]\n"
    "s_waitcnt vmcnt(41)\n"
    "v_mfma_f32_16x16x32_bf16 v[32:35], v[88:91], a[208:211], v[32:35]\n"
    "s_waitcnt vmcnt(40)\n"
    "v_mfma_f32_16x16x32_bf16 v[48:51], v[92:95], a[208:211], v[48:51]\n"
    "buffer_load_dwordx4 a[208:211], v236, s[44:47], s50 offen offset:0\n"
    "v_mfma_f32_16x16x32_bf16 v[4:7], v[80:83], a[212:215], v[4:7]\n"
    "v_mfma_f32_16x16x32_bf16 v[20:23], v[84:87], a[212:215], v[20:23]\n"
    "v_mfma_f32_16x16x32_bf16 v[36:39], v[88:91], a[212:215], v[36:39]\n"
    "v_mfma_f32_16x16x32_bf16 v[52:55], v[92:95], a[212:215], v[52:55]\n"
    "buffer_load_dwordx4 a[212:215], v236, s[44:47], s50 offen offset:1024\n"
    "v_mfma_f32_16x16x32_bf16 v[8:11], v[80:83], a[216:219], v[8:11]\n"
    "v_mfma_f32_16x16x32_bf16 v[24:27], v[84:87], a[216:219], v[24:27]\n"
    "v_mfma_f32_16x16x32_bf16 v[40:43], v[88:91], a[216:219], v[40:43]\n"
    "v_mfma_f32_16x16x32_bf16 v[56:59], v[92:95], a[216:219], v[56:59]\n"
    "buffer_load_dwordx4 a[216:219], v236, s[44:47], s50 offen offset:2048\n"
    "v_mfma_f32_16x16x32_bf16 v[12:15], v[80:83], a[220:223], v[12:15]\n"
    "v_mfma_f32_16x16x32_bf16 v[28:31], v[84:87], a[220:223], v[28:31]\n"
    "buffer_load_dwordx4 v[80:83], v232, s[40:43], s52 offen sc1\n"
    "v_mfma_f32_16x16x32_bf16 v[44:47], v[88:91], a[220:223], v[44:47]\n"
    "buffer_load_dwordx4 v[84:87], v233, s[40:43], s52 offen sc1\n"
    "v_mfma_f32_16x16x32_bf16 v[60:63], v[92:95], a[220:223], v[60:63]\n"
    "buffer_load_dwordx4 a[220:223], v236, s[44:47], s50 offen offset:3072\n"
    "buffer_load_dwordx4 v[88:91], v234, s[40:43], s52 offen sc1\n"
    "buffer_load_dwordx4 v[92:95], v235, s[40:43], s52 offen sc1\n"
    "s_add_u32 s50, s49, 950272\n"
    "s_add_u32 s52, s48, 20480\n"
    "s_waitcnt vmcnt(44)\n"
    "v_mfma_f32_16x16x32_bf16 v[0:3], v[96:99], a[224:227], v[0:3]\n"
    "s_waitcnt vmcnt(43)\n"
    "v_mfma_f32_16x16x32_bf16 v[16:19], v[100:103], a[224:227], v[16:19]\n"
    "s_waitcnt vmcnt(41)\n"
    "v_mfma_f32_16x16x32_bf16 v[32:35], v[104:107], a[224:227], v[32:35]\n"
    "s_waitcnt vmcnt(40)\n"
    "v_mfma_f32_16x16x32_bf16 v[48:51], v[108:111], a[224:227], v[48:51]\n"
    "buffer_load_dwordx4 a[224:227], v236, s[44:47], s50 offen offset:0\n"
    "v_mfma_f32_16x16x32_bf16 v[4:7], v[96:99], a[228:231], v[4:7]\n"
    "v_mfma_f32_16x16x32_bf16 v[20:23], v[100:103], a[228:231], v[20:23]\n"
    "v_mfma_f32_16x16x32_bf16 v[36:39], v[104:107], a[228:231], v[36:39]\n"
    "v_mfma_f32_16x16x32_bf16 v[52:55], v[108:111], a[228:231], v[52:55]\n"
    "buffer_load_dwordx4 a[228:231], v236, s[44:47], s50 offen offset:1024\n"
    "v_mfma_f32_16x16x32_bf16 v[8:11], v[96:99], a[232:235], v[8:11]\n"
    "v_mfma_f32_16x16x32_bf16 v[24:27], v[100:103], a[232:235], v[24:27]\n"
    "v_mfma_f32_16x16x32_bf16 v[40:43], v[104:107], a[232:235], v[40:43]\n"
    "v_mfma_f32_16x16x32_bf16 v[56:59], v[108:111], a[232:235], v[56:59]\n"
    "buffer_load_dwordx4 a[232:235], v236, s[44:47], s50 offen offset:2048\n"
    "v_mfma_f32_16x16x32_bf16 v[12:15], v[96:99], a[236:239], v[12:15]\n"
    "v_mfma_f32_16x16x32_bf16 v[28:31], v[100:103], a[236:239], v[28:31]\n"
    "buffer_load_dwordx4 v[96:99], v232, s[40:43], s52 offen sc1\n"
    "v_mfma_f32_16x16x32_bf16 v[44:47], v[104:107], a[236:239], v[44:47]\n"
    "buffer_load_dwordx4 v[100:103], v233, s[40:43], s52 offen sc1\n"
    "v_mfma_f32_16x16x32_bf16 v[60:63], v[108:111], a[236:239], v[60:63]\n"
    "buffer_load_dwordx4 a[236:239], v236, s[44:47], s50 offen offset:3072\n"
    "buffer_load_dwordx4 v[104:107], v234, s[40:43], s52 offen sc1\n"
    "buffer_load_dwordx4 v[108:111], v235, s[40:43], s52 offen sc1\n"
    "s_add_u32 s50, s49, 983040\n"
    "s_add_u32 s52, s48, 21504\n"
    "s_waitcnt vmcnt(44)\n"
    "v_mfma_f32_16x16x32_bf16 v[0:3], v[112:115], a[0:3], v[0:3]\n"
    "s_waitcnt vmcnt(43)\n"
    "v_mfma_f32_16x16x32_bf16 v[16:19], v[116:119], a[0:3], v[16:19]\n"
    "s_waitcnt vmcnt(41)\n"
    "v_mfma_f32_16x16x32_bf16 v[32:35], v[120:123], a[0:3], v[32:35]\n"
    "s_waitcnt vmcnt(40)\n"
    "v_mfma_f32_16x16x32_bf16 v[48:51], v[124:127], a[0:3], v[48:51]\n"
    "buffer_load_dwordx4 a[0:3], v236, s[44:47], s50 offen offset:0\n"
    "v_mfma_f32_16x16x32_bf16 v[4:7], v[112:115], a[4:7], v[4:7]\n"
    "v_mfma_f32_16x16x32_bf16 v[20:23], v[116:119], a[4:7], v[20:23]\n"
    "v_mfma_f32_16x16x32_bf16 v[36:39], v[120:123], a[4:7], v[36:39]\n"
    "v_mfma_f32_16x16x32_bf16 v[52:55], v[124:127], a[4:7], v[52:55]\n"
    "buffer_load_dwordx4 a[4:7], v236, s[44:47], s50 offen offset:1024\n"
    "v_mfma_f32_16x16x32_bf16 v[8:11], v[112:115], a[8:11], v[8:11]\n"
    "v_mfma_f32_16x16x32_bf16 v[24:27], v[116:119], a[8:11], v[24:27]\n"
    "v_mfma_f32_16x16x32_bf16 v[40:43], v[120:123], a[8:11], v[40:43]\n"
    "v_mfma_f32_16x16x32_bf16 v[56:59], v[124:127], a[8:11], v[56:59]\n"
    "buffer_load_dwordx4 a[8:11], v236, s[44:47], s50 offen offset:2048\n"
    "v_mfma_f32_16x16x32_bf16 v[12:15], v[112:115], a[12:15], v[12:15]\n"
    "v_mfma_f32_16x16x32_bf16 v[28:31], v[116:119], a[12:15], v[28:31]\n"
    "buffer_load_dwordx4 v[112:115], v232, s[40:43], s52 offen sc1\n"
    "v_mfma_f32_16x16x32_bf16 v[44:47], v[120:123], a[12:15], v[44:47]\n"
    "buffer_load_dwordx4 v[116:119], v233, s[40:43], s52 offen sc1\n"
    "v_mfma_f32_16x16x32_bf16 v[60:63], v[124:127], a[12:15], v[60:63]\n"
    "buffer_load_dwordx4 a[12:15], v236, s[44:47], s50 offen offset:3072\n"
    "buffer_load_dwordx4 v[120:123], v234, s[40:43], s52 offen sc1\n"
    "buffer_load_dwordx4 v[124:127], v235, s[40:43], s52 offen sc1\n"
    "s_add_u32 s50, s49, 1015808\n"
    "s_add_u32 s52, s48, 22528\n"
    "v_mov_b32 v239, 0\n"
    "global_load_dword v240, v239, s[64:65] sc1\n"
    "s_waitcnt vmcnt(45)\n"
    "v_mfma_f32_16x16x32_bf16 v[0:3], v[128:131], a[16:19], v[0:3]\n"
    "s_waitcnt vmcnt(44)\n"
    "v_mfma_f32_16x16x32_bf16 v[16:19], v[132:135], a[16:19], v[16:19]\n"
    "s_waitcnt vmcnt(42)\n"
    "v_mfma_f32_16x16x32_bf16 v[32:35], v[136:139], a[16:19], v[32:35]\n"
    "s_waitcnt vmcnt(41)\n"
    "v_mfma_f32_16x16x32_bf16 v[48:51], v[140:143], a[16:19], v[48:51]\n"
    "buffer_load_dwordx4 a[16:19], v236, s[44:47], s50 offen offset:0\n"
    "v_mfma_f32_16x16x32_bf16 v[4:7], v[128:131], a[20:23], v[4:7]\n"
    "v_mfma_f32_16x16x32_bf16 v[20:23], v[132:135], a[20:23], v[20:23]\n"
    "v_mfma_f32_16x16x32_bf16 v[36:39], v[136:139], a[20:23], v[36:39]\n"
    "v_mfma_f32_16x16x32_bf16 v[52:55], v[140:143], a[20:23], v[52:55]\n"
    "buffer_load_dwordx4 a[20:23], v236, s[44:47], s50 offen offset:1024\n"
    "v_mfma_f32_16x16x32_bf16 v[8:11], v[128:131], a[24:27], v[8:11]\n"
    "v_mfma_f32_16x16x32_bf16 v[24:27], v[132:135], a[24:27], v[24:27]\n"
    "v_mfma_f32_16x16x32_bf16 v[40:43], v[136:139], a[24:27], v[40:43]\n"
    "v_mfma_f32_16x16x32_bf16 v[56:59], v[140:143], a[24:27], v[56:59]\n"
    "buffer_load_dwordx4 a[24:27], v236, s[44:47], s50 offen offset:2048\n"
    "v_mfma_f32_16x16x32_bf16 v[12:15], v[128:131], a[28:31], v[12:15]\n"
    "v_mfma_f32_16x16x32_bf16 v[28:31], v[132:135], a[28:31], v[28:31]\n"
    "buffer_load_dwordx4 v[128:131], v232, s[40:43], s52 offen sc1\n"
    "v_mfma_f32_16x16x32_bf16 v[44:47], v[136:139], a[28:31], v[44:47]\n"
    "buffer_load_dwordx4 v[132:135], v233, s[40:43], s52 offen sc1\n"
    "v_mfma_f32_16x16x32_bf16 v[60:63], v[140:143], a[28:31], v[60:63]\n"
    "buffer_load_dwordx4 a[28:31], v236, s[44:47], s50 offen offset:3072\n"
    "buffer_load_dwordx4 v[136:139], v234, s[40:43], s52 offen sc1\n"
    "buffer_load_dwordx4 v[140:143], v235, s[40:43], s52 offen sc1\n"
    "s_add_u32 s52, s48, 23552\n"
    "s_waitcnt vmcnt(45)\n"
    "v_mfma_f32_16x16x32_bf16 v[0:3], v[144:147], a[32:35], v[0:3]\n"
    "s_waitcnt vmcnt(44)\n"
    "v_mfma_f32_16x16x32_bf16 v[16:19], v[148:151], a[32:35], v[16:19]\n"
    "s_waitcnt vmcnt(42)\n"
    "v_mfma_f32_16x16x32_bf16 v[32:35], v[152:155], a[32:35], v[32:35]\n"
    "s_waitcnt vmcnt(41)\n"
    "v_mfma_f32_16x16x32_bf16 v[48:51], v[156:159], a[32:35], v[48:51]\n"
    "v_mfma_f32_16x16x32_bf16 v[4:7], v[144:147], a[36:39], v[4:7]\n"
    "v_mfma_f32_16x16x32_bf16 v[20:23], v[148:151], a[36:39], v[20:23]\n"
    "v_mfma_f32_16x16x32_bf16 v[36:39], v[152:155], a[36:39], v[36:39]\n"
    "v_mfma_f32_16x16x32_bf16 v[52:55], v[156:159], a[36:39], v[52:55]\n"
    "v_mfma_f32_16x16x32_bf16 v[8:11], v[144:147], a[40:43], v[8:11]\n"
    "v_mfma_f32_16x16x32_bf16 v[24:27], v[148:151], a[40:43], v[24:27]\n"
    "v_mfma_f32_16x16x32_bf16 v[40:43], v[152:155], a[40:43], v[40:43]\n"
    "v_mfma_f32_16x16x32_bf16 v[56:59], v[156:159], a[40:43], v[56:59]\n"
    "v_mfma_f32_16x16x32_bf16 v[12:15], v[144:147], a[44:47], v[12:15]\n"
    "v_mfma_f32_16x16x32_bf16 v[28:31], v[148:151], a[44:47], v[28:31]\n"
    "buffer_load_dwordx4 v[144:147], v232, s[40:43], s52 offen sc1\n"
    "v_mfma_f32_16x16x32_bf16 v[44:47], v[152:155], a[44:47], v[44:47]\n"
    "buffer_load_dwordx4 v[148:151], v233, s[40:43], s52 offen sc1\n"
    "v_mfma_f32_16x16x32_bf16 v[60:63], v[156:159], a[44:47], v[60:63]\n"
    "buffer_load_dwordx4 v[152:155], v234, s[40:43], s52 offen sc1\n"
    "buffer_load_dwordx4 v[156:159], v235, s[40:43], s52 offen sc1\n"
    "s_add_u32 s52, s48, 24576\n"
    "s_waitcnt vmcnt(41)\n"
    "v_mfma_f32_16x16x32_bf16 v[0:3], v[64:67], a[48:51], v[0:3]\n"
    "s_waitcnt vmcnt(40)\n"
    "v_mfma_f32_16x16x32_bf16 v[16:19], v[68:71], a[48:51], v[16:19]\n"
    "s_waitcnt vmcnt(38)\n"
    "v_mfma_f32_16x16x32_bf16 v[32:35], v[72:75], a[48:51], v[32:35]\n"
    "s_waitcnt vmcnt(37)\n"
    "v_mfma_f32_16x16x32_bf16 v[48:51], v[76:79], a[48:51], v[48:51]\n"
    "v_mfma_f32_16x16x32_bf16 v[4:7], v[64:67], a[52:55], v[4:7]\n"
    "v_mfma_f32_16x16x32_bf16 v[20:23], v[68:71], a[52:55], v[20:23]\n"
    "v_mfma_f32_16x16x32_bf16 v[36:39], v[72:75], a[52:55], v[36:39]\n"
    "v_mfma_f32_16x16x32_bf16 v[52:55], v[76:79], a[52:55], v[52:55]\n"
    "v_mfma_f32_16x16x32_bf16 v[8:11], v[64:67], a[56:59], v[8:11]\n"
    "v_mfma_f32_16x16x32_bf16 v[24:27], v[68:71], a[56:59], v[24:27]\n"
    "v_mfma_f32_16x16x32_bf16 v[40:43], v[72:75], a[56:59], v[40:43]\n"
    "v_mfma_f32_16x16x32_bf16 v[56:59], v[76:79], a[56:59], v[56:59]\n"
    "v_mfma_f32_16x16x32_bf16 v[12:15], v[64:67], a[60:63], v[12:15]\n"
    "v_mfma_f32_16x16x32_bf16 v[28:31], v[68:71], a[60:63], v[28:31]\n"
    "buffer_load_dwordx4 v[64:67], v232, s[40:43], s52 offen sc1\n"
    "v_mfma_f32_16x16x32_bf16 v[44:47], v[72:75], a[60:63], v[44:47]\n"
    "buffer_load_dwordx4 v[68:71], v233, s[40:43], s52 offen sc1\n"
    "v_mfma_f32_16x16x32_bf16 v[60:63], v[76:79], a[60:63], v[60:63]\n"
    "buffer_load_dwordx4 v[72:75], v234, s[40:43], s52 offen sc1\n"
    "buffer_load_dwordx4 v[76:79], v235, s[40:43], s52 offen sc1\n"
    "s_add_u32 s52, s48, 25600\n"
    "s_waitcnt vmcnt(37)\n"
    "v_mfma_f32_16x16x32_bf16 v[0:3], v[80:83], a[64:67], v[0:3]\n"
    "s_waitcnt vmcnt(36)\n"
    "v_mfma_f32_16x16x32_bf16 v[16:19], v[84:87], a[64:67], v[16:19]\n"
    "s_waitcnt vmcnt(34)\n"
    "v_mfma_f32_16x16x32_bf16 v[32:35], v[88:91], a[64:67], v[32:35]\n"
    "s_waitcnt vmcnt(33)\n"
    "v_mfma_f32_16x16x32_bf16 v[48:51], v[92:95], a[64:67], v[48:51]\n"
    "v_mfma_f32_16x16x32_bf16 v[4:7], v[80:83], a[68:71], v[4:7]\n"
    "v_mfma_f32_16x16x32_bf16 v[20:23], v[84:87], a[68:71], v[20:23]\n"
    "v_mfma_f32_16x16x32_bf16 v[36:39], v[88:91], a[68:71], v[36:39]\n"
    "v_mfma_f32_16x16x32_bf16 v[52:55], v[92:95], a[68:71], v[52:55]\n"
    "v_mfma_f32_16x16x32_bf16 v[8:11], v[80:83], a[72:75], v[8:11]\n"
    "v_mfma_f32_16x16x32_bf16 v[24:27], v[84:87], a[72:75], v[24:27]\n"
    "v_mfma_f32_16x16x32_bf16 v[40:43], v[88:91], a[72:75], v[40:43]\n"
    "v_mfma_f32_16x16x32_bf16 v[56:59], v[92:95], a[72:75], v[56:59]\n"
    "v_mfma_f32_16x16x32_bf16 v[12:15], v[80:83], a[76:79], v[12:15]\n"
    "v_mfma_f32_16x16x32_bf16 v[28:31], v[84:87], a[76:79], v[28:31]\n"
    "buffer_load_dwordx4 v[80:83], v232, s[40:43], s52 offen sc1\n"
    "v_mfma_f32_16x16x32_bf16 v[44:47], v[88:91], a[76:79], v[44:47]\n"
    "buffer_load_dwordx4 v[84:87], v233, s[40:43], s52 offen sc1\n"
    "v_mfma_f32_16x16x32_bf16 v[60:63], v[92:95], a[76:79], v[60:63]\n"
    "buffer_load_dwordx4 v[88:91], v234, s[40:43], s52 offen sc1\n"
    "buffer_load_dwordx4 v[92:95], v235, s[40:43], s52 offen sc1\n"
    "s_add_u32 s52, s48, 26624\n"
    "s_waitcnt vmcnt(33)\n"
    "v_mfma_f32_16x16x32_bf16 v[0:3], v[96:99], a[80:83], v[0:3]\n"
    "s_waitcnt vmcnt(32)\n"
    "v_mfma_f32_16x16x32_bf16 v[16:19], v[100:103], a[80:83], v[16:19]\n"
    "s_waitcnt vmcnt(30)\n"
    "v_mfma_f32_16x16x32_bf16 v[32:35], v[104:107], a[80:83], v[32:35]\n"
    "s_waitcnt vmcnt(29)\n"
    "v_mfma_f32_16x16x32_bf16 v[48:51], v[108:111], a[80:83], v[48:51]\n"
    "v_mfma_f32_16x16x32_bf16 v[4:7], v[96:99], a[84:87], v[4:7]\n"
    "v_mfma_f32_16x16x32_bf16 v[20:23], v[100:103], a[84:87], v[20:23]\n"
    "v_mfma_f32_16x16x32_bf16 v[36:39], v[104:107], a[84:87], v[36:39]\n"
    "v_mfma_f32_16x16x32_bf16 v[52:55], v[108:111], a[84:87], v[52:55]\n"
    "v_mfma_f32_16x16x32_bf16 v[8:11], v[96:99], a[88:91], v[8:11]\n"
    "v_mfma_f32_16x16x32_bf16 v[24:27], v[100:103], a[88:91], v[24:27]\n"
    "v_mfma_f32_16x16x32_bf16 v[40:43], v[104:107], a[88:91], v[40:43]\n"
    "v_mfma_f32_16x16x32_bf16 v[56:59], v[108:111], a[88:91], v[56:59]\n"
    "v_mfma_f32_16x16x32_bf16 v[12:15], v[96:99], a[92:95], v[12:15]\n"
    "v_mfma_f32_16x16x32_bf16 v[28:31], v[100:103], a[92:95], v[28:31]\n"
    "buffer_load_dwordx4 v[96:99], v232, s[40:43], s52 offen sc1\n"
    "v_mfma_f32_16x16x32_bf16 v[44:47], v[104:107], a[92:95], v[44:47]\n"
    "buffer_load_dwordx4 v[100:103], v233, s[40:43], s52 offen sc1\n"
    "v_mfma_f32_16x16x32_bf16 v[60:63], v[108:111], a[92:95], v[60:63]\n"
    "buffer_load_dwordx4 v[104:107], v234, s[40:43], s52 offen sc1\n"
    "buffer_load_dwordx4 v[108:111], v235, s[40:43], s52 offen sc1\n"
    "s_add_u32 s52, s48, 27648\n"
    "s_waitcnt vmcnt(29)\n"
    "v_mfma_f32_16x16x32_bf16 v[0:3], v[112:115], a[96:99], v[0:3]\n"
    "s_waitcnt vmcnt(28)\n"
    "v_mfma_f32_16x16x32_bf16 v[16:19], v[116:119], a[96:99], v[16:19]\n"
    "s_waitcnt vmcnt(26)\n"
    "v_mfma_f32_16x16x32_bf16 v[32:35], v[120:123], a[96:99], v[32:35]\n"
    "s_waitcnt vmcnt(25)\n"
    "v_mfma_f32_16x16x32_bf16 v[48:51], v[124:127], a[96:99], v[48:51]\n"
    "v_mfma_f32_16x16x32_bf16 v[4:7], v[112:115], a[100:103], v[4:7]\n"
    "v_mfma_f32_16x16x32_bf16 v[20:23], v[116:119], a[100:103], v[20:23]\n"
    "v_mfma_f32_16x16x32_bf16 v[36:39], v[120:123], a[100:103], v[36:39]\n"
    "v_mfma_f32_16x16x32_bf16 v[52:55], v[124:127], a[100:103], v[52:55]\n"
    "v_mfma_f32_16x16x32_bf16 v[8:11], v[112:115], a[104:107], v[8:11]\n"
    "v_mfma_f32_16x16x32_bf16 v[24:27], v[116:119], a[104:107], v[24:27]\n"
    "v_mfma_f32_16x16x32_bf16 v[40:43], v[120:123], a[104:107], v[40:43]\n"
    "v_mfma_f32_16x16x32_bf16 v[56:59], v[124:127], a[104:107], v[56:59]\n"
    "v_mfma_f32_16x16x32_bf16 v[12:15], v[112:115], a[108:111], v[12:15]\n"
    "v_mfma_f32_16x16x32_bf16 v[28:31], v[116:119], a[108:111], v[28:31]\n"
    "buffer_load_dwordx4 v[112:115], v232, s[40:43], s52 offen sc1\n"
    "v_mfma_f32_16x16x32_bf16 v[44:47], v[120:123], a[108:111], v[44:47]\n"
    "buffer_load_dwordx4 v[116:119], v233, s[40:43], s52 offen sc1\n"
    "v_mfma_f32_16x16x32_bf16 v[60:63], v[124:127], a[108:111], v[60:63]\n"
    "buffer_load_dwordx4 v[120:123], v234, s[40:43], s52 offen sc1\n"
    "buffer_load_dwordx4 v[124:127], v235, s[40:43], s52 offen sc1\n"
    "s_add_u32 s52, s48, 28672\n"
    "s_waitcnt vmcnt(24)\n"
    "v_mfma_f32_16x16x32_bf16 v[0:3], v[128:131], a[112:115], v[0:3]\n"
    "s_waitcnt vmcnt(23)\n"
    "v_mfma_f32_16x16x32_bf16 v[16:19], v[132:135], a[112:115], v[16:19]\n"
    "s_waitcnt vmcnt(21)\n"
    "v_mfma_f32_16x16x32_bf16 v[32:35], v[136:139], a[112:115], v[32:35]\n"
    "s_waitcnt vmcnt(20)\n"
    "v_mfma_f32_16x16x32_bf16 v[48:51], v[140:143], a[112:115], v[48:51]\n"
    "v_mfma_f32_16x16x32_bf16 v[4:7], v[128:131], a[116:119], v[4:7]\n"
    "v_mfma_f32_16x16x32_bf16 v[20:23], v[132:135], a[116:119], v[20:23]\n"
    "v_mfma_f32_16x16x32_bf16 v[36:39], v[136:139], a[116:119], v[36:39]\n"
    "v_mfma_f32_16x16x32_bf16 v[52:55], v[140:143], a[116:119], v[52:55]\n"
    "v_mfma_f32_16x16x32_bf16 v[8:11], v[128:131], a[120:123], v[8:11]\n"
    "v_mfma_f32_16x16x32_bf16 v[24:27], v[132:135], a[120:123], v[24:27]\n"
    "v_mfma_f32_16x16x32_bf16 v[40:43], v[136:139], a[120:123], v[40:43]\n"
    "v_mfma_f32_16x16x32_bf16 v[56:59], v[140:143], a[120:123], v[56:59]\n"
    "v_mfma_f32_16x16x32_bf16 v[12:15], v[128:131], a[124:127], v[12:15]\n"
    "v_mfma_f32_16x16x32_bf16 v[28:31], v[132:135], a[124:127], v[28:31]\n"
    "buffer_load_dwordx4 v[128:131], v232, s[40:43], s52 offen sc1\n"
    "v_mfma_f32_16x16x32_bf16 v[44:47], v[136:139], a[124:127], v[44:47]\n"
    "buffer_load_dwordx4 v[132:135], v233, s[40:43], s52 offen sc1\n"
    "v_mfma_f32_16x16x32_bf16 v[60:63], v[140:143], a[124:127], v[60:63]\n"
    "buffer_load_dwordx4 v[136:139], v234, s[40:43], s52 offen sc1\n"
    "buffer_load_dwordx4 v[140:143], v235, s[40:43], s52 offen sc1\n"
    "s_add_u32 s52, s48, 29696\n"
    "s_waitcnt vmcnt(23)\n"
    "v_mfma_f32_16x16x32_bf16 v[0:3], v[144:147], a[128:131], v[0:3]\n"
    "s_waitcnt vmcnt(22)\n"
    "v_mfma_f32_16x16x32_bf16 v[16:19], v[148:151], a[128:131], v[16:19]\n"
    "s_waitcnt vmcnt(21)\n"
    "v_mfma_f32_16x16x32_bf16 v[32:35], v[152:155], a[128:131], v[32:35]\n"
    "s_waitcnt vmcnt(20)\n"
    "v_mfma_f32_16x16x32_bf16 v[48:51], v[156:159], a[128:131], v[48:51]\n"
    "v_mfma_f32_16x16x32_bf16 v[4:7], v[144:147], a[132:135], v[4:7]\n"
    "v_mfma_f32_16x16x32_bf16 v[20:23], v[148:151], a[132:135], v[20:23]\n"
    "v_mfma_f32_16x16x32_bf16 v[36:39], v[152:155], a[132:135], v[36:39]\n"
    "v_mfma_f32_16x16x32_bf16 v[52:55], v[156:159], a[132:135], v[52:55]\n"
    "v_mfma_f32_16x16x32_bf16 v[8:11], v[144:147], a[136:139], v[8:11]\n"
    "v_mfma_f32_16x16x32_bf16 v[24:27], v[148:151], a[136:139], v[24:27]\n"
    "v_mfma_f32_16x16x32_bf16 v[40:43], v[152:155], a[136:139], v[40:43]\n"
    "v_mfma_f32_16x16x32_bf16 v[56:59], v[156:159], a[136:139], v[56:59]\n"
    "v_mfma_f32_16x16x32_bf16 v[12:15], v[144:147], a[140:143], v[12:15]\n"
    "v_mfma_f32_16x16x32_bf16 v[28:31], v[148:151], a[140:143], v[28:31]\n"
    "buffer_load_dwordx4 v[144:147], v232, s[40:43], s52 offen sc1\n"
    "v_mfma_f32_16x16x32_bf16 v[44:47], v[152:155], a[140:143], v[44:47]\n"
    "buffer_load_dwordx4 v[148:151], v233, s[40:43], s52 offen sc1\n"
    "v_mfma_f32_16x16x32_bf16 v[60:63], v[156:159], a[140:143], v[60:63]\n"
    "buffer_load_dwordx4 v[152:155], v234, s[40:43], s52 offen sc1\n"
    "buffer_load_dwordx4 v[156:159], v235, s[40:43], s52 offen sc1\n"
    "s_add_u32 s52, s48, 30720\n"
    "s_waitcnt vmcnt(23)\n"
    "v_mfma_f32_16x16x32_bf16 v[0:3], v[64:67], a[144:147], v[0:3]\n"
    "s_waitcnt vmcnt(22)\n"
    "v_mfma_f32_16x16x32_bf16 v[16:19], v[68:71], a[144:147], v[16:19]\n"
    "s_waitcnt vmcnt(21)\n"
    "v_mfma_f32_16x16x32_bf16 v[32:35], v[72:75], a[144:147], v[32:35]\n"
    "s_waitcnt vmcnt(20)\n"
    "v_mfma_f32_16x16x32_bf16 v[48:51], v[76:79], a[144:147], v[48:51]\n"
    "v_mfma_f32_16x16x32_bf16 v[4:7], v[64:67], a[148:151], v[4:7]\n"
    "v_mfma_f32_16x16x32_bf16 v[20:23], v[68:71], a[148:151], v[20:23]\n"
    "v_mfma_f32_16x16x32_bf16 v[36:39], v[72:75], a[148:151], v[36:39]\n"
    "v_mfma_f32_16x16x32_bf16 v[52:55], v[76:79], a[148:151], v[52:55]\n"
    "v_mfma_f32_16x16x32_bf16 v[8:11], v[64:67], a[152:155], v[8:11]\n"
    "v_mfma_f32_16x16x32_bf16 v[24:27], v[68:71], a[152:155], v[24:27]\n"
    "v_mfma_f32_16x16x32_bf16 v[40:43], v[72:75], a[152:155], v[40:43]\n"
    "v_mfma_f32_16x16x32_bf16 v[56:59], v[76:79], a[152:155], v[56:59]\n"
    "v_mfma_f32_16x16x32_bf16 v[12:15], v[64:67], a[156:159], v[12:15]\n"
    "v_mfma_f32_16x16x32_bf16 v[28:31], v[68:71], a[156:159], v[28:31]\n"
    "buffer_load_dwordx4 v[64:67], v232, s[40:43], s52 offen sc1\n"
    "v_mfma_f32_16x16x32_bf16 v[44:47], v[72:75], a[156:159], v[44:47]\n"
    "buffer_load_dwordx4 v[68:71], v233, s[40:43], s52 offen sc1\n"
    "v_mfma_f32_16x16x32_bf16 v[60:63], v[76:79], a[156:159], v[60:63]\n"
    "buffer_load_dwordx4 v[72:75], v234, s[40:43], s52 offen sc1\n"
    "buffer_load_dwordx4 v[76:79], v235, s[40:43], s52 offen sc1\n"
    "s_add_u32 s52, s48, 31744\n"
    "s_waitcnt vmcnt(23)\n"
    "v_mfma_f32_16x16x32_bf16 v[0:3], v[80:83], a[160:163], v[0:3]\n"
    "s_waitcnt vmcnt(22)\n"
    "v_mfma_f32_16x16x32_bf16 v[16:19], v[84:87], a[160:163], v[16:19]\n"
    "s_waitcnt vmcnt(21)\n"
    "v_mfma_f32_16x16x32_bf16 v[32:35], v[88:91], a[160:163], v[32:35]\n"
    "s_waitcnt vmcnt(20)\n"
    "v_mfma_f32_16x16x32_bf16 v[48:51], v[92:95], a[160:163], v[48:51]\n"
    "v_mfma_f32_16x16x32_bf16 v[4:7], v[80:83], a[164:167], v[4:7]\n"
    "v_mfma_f32_16x16x32_bf16 v[20:23], v[84:87], a[164:167], v[20:23]\n"
    "v_mfma_f32_16x16x32_bf16 v[36:39], v[88:91], a[164:167], v[36:39]\n"
    "v_mfma_f32_16x16x32_bf16 v[52:55], v[92:95], a[164:167], v[52:55]\n"
    "v_mfma_f32_16x16x32_bf16 v[8:11], v[80:83], a[168:171], v[8:11]\n"
    "v_mfma_f32_16x16x32_bf16 v[24:27], v[84:87], a[168:171], v[24:27]\n"
    "v_mfma_f32_16x16x32_bf16 v[40:43], v[88:91], a[168:171], v[40:43]\n"
    "v_mfma_f32_16x16x32_bf16 v[56:59], v[92:95], a[168:171], v[56:59]\n"
    "v_mfma_f32_16x16x32_bf16 v[12:15], v[80:83], a[172:175], v[12:15]\n"
    "v_mfma_f32_16x16x32_bf16 v[28:31], v[84:87], a[172:175], v[28:31]\n"
    "buffer_load_dwordx4 v[80:83], v232, s[40:43], s52 offen sc1\n"
    "v_mfma_f32_16x16x32_bf16 v[44:47], v[88:91], a[172:175], v[44:47]\n"
    "buffer_load_dwordx4 v[84:87], v233, s[40:43], s52 offen sc1\n"
    "v_mfma_f32_16x16x32_bf16 v[60:63], v[92:95], a[172:175], v[60:63]\n"
    "buffer_load_dwordx4 v[88:91], v234, s[40:43], s52 offen sc1\n"
    "buffer_load_dwordx4 v[92:95], v235, s[40:43], s52 offen sc1\n"
    "v_readfirstlane_b32 s53, v240\n"
    "s_movk_i32 s51, 0x4000\n"
    "s_cmp_ge_u32 s53, 128\n"
    "s_cbranch_scc1 LO_GO%=\n"
    "LO_SPIN%=:\n"
    "s_sleep 1\n"
    "global_load_dword v240, v239, s[64:65] sc1\n"
    "s_waitcnt vmcnt(0)\n"
    "v_readfirstlane_b32 s53, v240\n"
    "s_sub_u32 s51, s51, 1\n"
    "s_cmp_eq_u32 s51, 0\n"
    "s_cbranch_scc1 LO_GO%=\n"
    "s_cmp_lt_u32 s53, 128\n"
    "s_cbranch_scc1 LO_SPIN%=\n"
    "LO_GO%=:\n"
    "s_barrier\n"
    "buffer_load_dwordx4 v[208:211], v237, s[56:59], 0 offen sc1\n"
    "buffer_load_dwordx4 v[212:215], v237, s[56:59], 0 offen offset:16 sc1\n"
    "buffer_load_dwordx4 v[216:219], v238, s[60:63], 0 offen offset:0\n"
    "buffer_load_dwordx4 v[220:223], v238, s[60:63], 0 offen offset:16\n"
    "buffer_load_dwordx4 v[224:227], v238, s[60:63], 0 offen offset:32\n"
    "buffer_load_dwordx4 v[228:231], v238, s[60:63], 0 offen offset:48\n"
    "s_waitcnt vmcnt(29)\n"
    "v_mfma_f32_16x16x32_bf16 v[0:3], v[96:99], a[176:179], v[0:3]\n"
    "s_waitcnt vmcnt(28)\n"
    "v_mfma_f32_16x16x32_bf16 v[16:19], v[100:103], a[176:179], v[16:19]\n"
    "s_waitcnt vmcnt(27)\n"
    "v_mfma_f32_16x16x32_bf16 v[32:35], v[104:107], a[176:179], v[32:35]\n"
    "s_waitcnt vmcnt(26)\n"
    "v_mfma_f32_16x16x32_bf16 v[48:51], v[108:111], a[176:179], v[48:51]\n"
    "v_mfma_f32_16x16x32_bf16 v[4:7], v[96:99], a[180:183], v[4:7]\n"
    "v_mfma_f32_16x16x32_bf16 v[20:23], v[100:103], a[180:183], v[20:23]\n"
    "v_mfma_f32_16x16x32_bf16 v[36:39], v[104:107], a[180:183], v[36:39]\n"
    "v_mfma_f32_16x16x32_bf16 v[52:55], v[108:111], a[180:183], v[52:55]\n"
    "v_mfma_f32_16x16x32_bf16 v[8:11], v[96:99], a[184:187], v[8:11]\n"
    "v_mfma_f32_16x16x32_bf16 v[24:27], v[100:103], a[184:187], v[24:27]\n"
    "v_mfma_f32_16x16x32_bf16 v[40:43], v[104:107], a[184:187], v[40:43]\n"
    "v_mfma_f32_16x16x32_bf16 v[56:59], v[108:111], a[184:187], v[56:59]\n"
    "v_mfma_f32_16x16x32_bf16 v[12:15], v[96:99], a[188:191], v[12:15]\n"
    "v_mfma_f32_16x16x32_bf16 v[28:31], v[100:103], a[188:191], v[28:31]\n"
    "v_mfma_f32_16x16x32_bf16 v[44:47], v[104:107], a[188:191], v[44:47]\n"
    "v_mfma_f32_16x16x32_bf16 v[60:63], v[108:111], a[188:191], v[60:63]\n"
    "s_waitcnt vmcnt(25)\n"
    "v_mfma_f32_16x16x32_bf16 v[0:3], v[112:115], a[192:195], v[0:3]\n"
    "s_waitcnt vmcnt(24)\n"
    "v_mfma_f32_16x16x32_bf16 v[16:19], v[116:119], a[192:195], v[16:19]\n"
    "s_waitcnt vmcnt(23)\n"
    "v_mfma_f32_16x16x32_bf16 v[32:35], v[120:123], a[192:195], v[32:35]\n"
    "s_waitcnt vmcnt(22)\n"
    "v_mfma_f32_16x16x32_bf16 v[48:51], v[124:127], a[192:195], v[48:51]\n"
    "v_mfma_f32_16x16x32_bf16 v[4:7], v[112:115], a[196:199], v[4:7]\n"
    "v_mfma_f32_16x16x32_bf16 v[20:23], v[116:119], a[196:199], v[20:23]\n"
    "v_mfma_f32_16x16x32_bf16 v[36:39], v[120:123], a[196:199], v[36:39]\n"
    "v_mfma_f32_16x16x32_bf16 v[52:55], v[124:127], a[196:199], v[52:55]\n"
    "v_mfma_f32_16x16x32_bf16 v[8:11], v[112:115], a[200:203], v[8:11]\n"
    "v_mfma_f32_16x16x32_bf16 v[24:27], v[116:119], a[200:203], v[24:27]\n"
    "v_mfma_f32_16x16x32_bf16 v[40:43], v[120:123], a[200:203], v[40:43]\n"
    "v_mfma_f32_16x16x32_bf16 v[56:59], v[124:127], a[200:203], v[56:59]\n"
    "v_mfma_f32_16x16x32_bf16 v[12:15], v[112:115], a[204:207], v[12:15]\n"
    "v_mfma_f32_16x16x32_bf16 v[28:31], v[116:119], a[204:207], v[28:31]\n"
    "v_mfma_f32_16x16x32_bf16 v[44:47], v[120:123], a[204:207], v[44:47]\n"
    "v_mfma_f32_16x16x32_bf16 v[60:63], v[124:127], a[204:207], v[60:63]\n"
    "s_waitcnt vmcnt(21)\n"
    "v_mfma_f32_16x16x32_bf16 v[0:3], v[128:131], a[208:211], v[0:3]\n"
    "s_waitcnt vmcnt(20)\n"
    "v_mfma_f32_16x16x32_bf16 v[16:19], v[132:135], a[208:211], v[16:19]\n"
    "s_waitcnt vmcnt(19)\n"
    "v_mfma_f32_16x16x32_bf16 v[32:35], v[136:139], a[208:211], v[32:35]\n"
    "s_waitcnt vmcnt(18)\n"
    "v_mfma_f32_16x16x32_bf16 v[48:51], v[140:143], a[208:211], v[48:51]\n"
    "v_mfma_f32_16x16x32_bf16 v[4:7], v[128:131], a[212:215], v[4:7]\n"
    "v_mfma_f32_16x16x32_bf16 v[20:23], v[132:135], a[212:215], v[20:23]\n"
    "v_mfma_f32_16x16x32_bf16 v[36:39], v[136:139], a[212:215], v[36:39]\n"
    "v_mfma_f32_16x16x32_bf16 v[52:55], v[140:143], a[212:215], v[52:55]\n"
    "v_mfma_f32_16x16x32_bf16 v[8:11], v[128:131], a[216:219], v[8:11]\n"
    "v_mfma_f32_16x16x32_bf16 v[24:27], v[132:135], a[216:219], v[24:27]\n"
    "v_mfma_f32_16x16x32_bf16 v[40:43], v[136:139], a[216:219], v[40:43]\n"
    "v_mfma_f32_16x16x32_bf16 v[56:59], v[140:143], a[216:219], v[56:59]\n"
    "v_mfma_f32_16x16x32_bf16 v[12:15], v[128:131], a[220:223], v[12:15]\n"
    "v_mfma_f32_16x16x32_bf16 v[28:31], v[132:135], a[220:223], v[28:31]\n"
    "v_mfma_f32_16x16x32_bf16 v[44:47], v[136:139], a[220:223], v[44:47]\n"
    "v_mfma_f32_16x16x32_bf16 v[60:63], v[140:143], a[220:223], v[60:63]\n"
    "s_waitcnt vmcnt(17)\n"
    "v_mfma_f32_16x16x32_bf16 v[0:3], v[144:147], a[224:227], v[0:3]\n"
    "s_waitcnt vmcnt(16)\n"
    "v_mfma_f32_16x16x32_bf16 v[16:19], v[148:151], a[224:227], v[16:19]\n"
    "s_waitcnt vmcnt(15)\n"
    "v_mfma_f32_16x16x32_bf16 v[32:35], v[152:155], a[224:227], v[32:35]\n"
    "s_waitcnt vmcnt(14)\n"
    "v_mfma_f32_16x16x32_bf16 v[48:51], v[156:159], a[224:227], v[48:51]\n"
    "v_mfma_f32_16x16x32_bf16 v[4:7], v[144:147], a[228:231], v[4:7]\n"
    "v_mfma_f32_16x16x32_bf16 v[20:23], v[148:151], a[228:231], v[20:23]\n"
    "v_mfma_f32_16x16x32_bf16 v[36:39], v[152:155], a[228:231], v[36:39]\n"
    "v_mfma_f32_16x16x32_bf16 v[52:55], v[156:159], a[228:231], v[52:55]\n"
    "v_mfma_f32_16x16x32_bf16 v[8:11], v[144:147], a[232:235], v[8:11]\n"
    "v_mfma_f32_16x16x32_bf16 v[24:27], v[148:151], a[232:235], v[24:27]\n"
    "v_mfma_f32_16x16x32_bf16 v[40:43], v[152:155], a[232:235], v[40:43]\n"
    "v_mfma_f32_16x16x32_bf16 v[56:59], v[156:159], a[232:235], v[56:59]\n"
    "v_mfma_f32_16x16x32_bf16 v[12:15], v[144:147], a[236:239], v[12:15]\n"
    "v_mfma_f32_16x16x32_bf16 v[28:31], v[148:151], a[236:239], v[28:31]\n"
    "v_mfma_f32_16x16x32_bf16 v[44:47], v[152:155], a[236:239], v[44:47]\n"
    "v_mfma_f32_16x16x32_bf16 v[60:63], v[156:159], a[236:239], v[60:63]\n"
    "s_waitcnt vmcnt(13)\n"
    "v_mfma_f32_16x16x32_bf16 v[0:3], v[64:67], a[0:3], v[0:3]\n"
    "s_waitcnt vmcnt(12)\n"
    "v_mfma_f32_16x16x32_bf16 v[16:19], v[68:71], a[0:3], v[16:19]\n"
    "s_waitcnt vmcnt(11)\n"
    "v_mfma_f32_16x16x32_bf16 v[32:35], v[72:75], a[0:3], v[32:35]\n"
    "s_waitcnt vmcnt(10)\n"
    "v_mfma_f32_16x16x32_bf16 v[48:51], v[76:79], a[0:3], v[48:51]\n"
    "v_mfma_f32_16x16x32_bf16 v[4:7], v[64:67], a[4:7], v[4:7]\n"
    "v_mfma_f32_16x16x32_bf16 v[20:23], v[68:71], a[4:7], v[20:23]\n"
    "v_mfma_f32_16x16x32_bf16 v[36:39], v[72:75], a[4:7], v[36:39]\n"
    "v_mfma_f32_16x16x32_bf16 v[52:55], v[76:79], a[4:7], v[52:55]\n"
    "v_mfma_f32_16x16x32_bf16 v[8:11], v[64:67], a[8:11], v[8:11]\n"
    "v_mfma_f32_16x16x32_bf16 v[24:27], v[68:71], a[8:11], v[24:27]\n"
    "v_mfma_f32_16x16x32_bf16 v[40:43], v[72:75], a[8:11], v[40:43]\n"
    "v_mfma_f32_16x16x32_bf16 v[56:59], v[76:79], a[8:11], v[56:59]\n"
    "v_mfma_f32_16x16x32_bf16 v[12:15], v[64:67], a[12:15], v[12:15]\n"
    "v_mfma_f32_16x16x32_bf16 v[28:31], v[68:71], a[12:15], v[28:31]\n"
    "v_mfma_f32_16x16x32_bf16 v[44:47], v[72:75], a[12:15], v[44:47]\n"
    "v_mfma_f32_16x16x32_bf16 v[60:63], v[76:79], a[12:15], v[60:63]\n"
    "s_waitcnt vmcnt(9)\n"
    "v_mfma_f32_16x16x32_bf16 v[0:3], v[80:83], a[16:19], v[0:3]\n"
    "s_waitcnt vmcnt(8)\n"
    "v_mfma_f32_16x16x32_bf16 v[16:19], v[84:87], a[16:19], v[16:19]\n"
    "s_waitcnt vmcnt(7)\n"
    "v_mfma_f32_16x16x32_bf16 v[32:35], v[88:91], a[16:19], v[32:35]\n"
    "s_waitcnt vmcnt(6)\n"
    "v_mfma_f32_16x16x32_bf16 v[48:51], v[92:95], a[16:19], v[48:51]\n"
    "v_mfma_f32_16x16x32_bf16 v[4:7], v[80:83], a[20:23], v[4:7]\n"
    "v_mfma_f32_16x16x32_bf16 v[20:23], v[84:87], a[20:23], v[20:23]\n"
    "v_mfma_f32_16x16x32_bf16 v[36:39], v[88:91], a[20:23], v[36:39]\n"
    "v_mfma_f32_16x16x32_bf16 v[52:55], v[92:95], a[20:23], v[52:55]\n"
    "v_mfma_f32_16x16x32_bf16 v[8:11], v[80:83], a[24:27], v[8:11]\n"
    "v_mfma_f32_16x16x32_bf16 v[24:27], v[84:87], a[24:27], v[24:27]\n"
    "v_mfma_f32_16x16x32_bf16 v[40:43], v[88:91], a[24:27], v[40:43]\n"
    "v_mfma_f32_16x16x32_bf16 v[56:59], v[92:95], a[24:27], v[56:59]\n"
    "v_mfma_f32_16x16x32_bf16 v[12:15], v[80:83], a[28:31], v[12:15]\n"
    "v_mfma_f32_16x16x32_bf16 v[28:31], v[84:87], a[28:31], v[28:31]\n"
    "v_mfma_f32_16x16x32_bf16 v[44:47], v[88:91], a[28:31], v[44:47]\n"
    "v_mfma_f32_16x16x32_bf16 v[60:63], v[92:95], a[28:31], v[60:63]\n"
    "s_waitcnt vmcnt(0)\n"
    "s_nop 6\n"
      : "={v[0:31]}"(Aa), "={v[32:63]}"(Ab), "={v[208:215]}"(av), "={v[216:231]}"(gv), "+{a[0:15]}"(WR.w[0]), "+{a[16:31]}"(WR.w[1]), "+{a[32:47]}"(WR.w[2]), "+{a[48:63]}"(WR.w[3]), "+{a[64:79]}"(WR.w[4]),
        "+{a[80:95]}"(WR.w[5]), "+{a[96:111]}"(WR.w[6]), "+{a[112:127]}"(WR.w[7]), "+{a[128:143]}"(WR.w[8]), "+{a[144:159]}"(WR.w[9]), "+{a[160:175]}"(WR.w[10]),
        "+{a[176:191]}"(WR.w[11]), "+{a[192:207]}"(WR.w[12]), "+{a[208:223]}"(WR.w[13]), "+{a[224:239]}"(WR.w[14])
      : "{v[232:235]}"(vc), "{v236}"(lane * 16), "{v237}"((tid >> 2) * 4096 + (tid & 3) * 32), "{v238}"((tid & 3) * 64), "{s[40:43]}"(sC), "{s[44:47]}"(WR.srd),
        "{s[56:59]}"(sA), "{s[60:63]}"(sG), "{s[64:65]}"((uint64_t)(ws + WS_CNT + (16 + mb64) * 128)), "{s[66:67]}"((uint64_t)(ws + WS_CNT + (16 + (blockIdx.x >> 5)) * 128)), "{s48}"(rfl(wid * 8 * 4096)), "{s49}"(WR.off)
      : "v64", "v65", "v66", "v67", "v68", "v69", "v70", "v71", "v72", "v73", "v74", "v75", "v76", "v77", "v78", "v79", "v80", "v81", "v82", "v83", "v84", "v85", "v86", "v87", "v88", "v89", "v90", "v91", "v92", "v93", "v94", "v95", "v96", "v97", "v98", "v99", "v100", "v101", "v102", "v103", "v104", "v105", "v106", "v107", "v108", "v109", "v110", "v111", "v112", "v113", "v114", "v115", "v116", "v117", "v118", "v119", "v120", "v121", "v122", "v123", "v124", "v125", "v126", "v127", "v128", "v129", "v130", "v131", "v132", "v133", "v134", "v135", "v136", "v137", "v138", "v139", "v140", "v141", "v142", "v143", "v144", "v145", "v146", "v147", "v148", "v149", "v150", "v151", "v152", "v153", "v154", "v155", "v156", "v157", "v158", "v159", "v160", "v161", "v162", "v163", "v164", "v165", "v166", "v167", "v168", "v169", "v170", "v171", "v172", "v173", "v174", "v175", "v176", "v177", "v178", "v179", "v180", "v181", "v182", "v183", "v184", "v185", "v186", "v187", "v188", "v189", "v190", "v191", "v192", "v193", "v194", "v195", "v196", "v197", "v198", "v199", "v200", "v201", "v202", "v203", "v204", "v205", "v206", "v207", "v239", "v240", "s50", "s51", "s52", "s53", "vcc", "scc", "memory");
    SBAR();
  }
  {
    const unsigned lane = lane_id(), tid = wid * 64 + lane;
    const unsigned m = lane & 15, g = lane >> 4;
#pragma unroll
    for (int tb = 0; tb < 4; ++tb)
#pragma unroll
      for (int mb = 0; mb < 4; ++mb) {
        const int b = (16 * tb + 4 * mb) & 31;
        const f32x32& src = tb < 2 ? Aa : Ab;
        *(f32x4*)(lds_ + wid * (64 * 272) + (16 * mb + m) * 272 + (16 * tb + 4 * g) * 4) = f32x4{src[b], src[b + 1], src[b + 2], src[b + 3]};
      }
    LBAR();
    const unsigned m2 = tid >> 2, e2 = (tid & 3) * 16;
    f32x4 b[4];
#pragma unroll
    for (int q = 0; q < 4; ++q) {
      const char* rp = lds_ + m2 * 272 + (e2 + 4 * q) * 4;
      b[q] = (*(const f32x4*)rp + *(const f32x4*)(rp + 64 * 272)) + (*(const f32x4*)(rp + 2 * 64 * 272) + *(const f32x4*)(rp + 3 * 64 * 272));
    }
    LBAR();
    unsigned short* tile = (unsigned short*)lds_;
#pragma unroll
    for (int e = 0; e < 16; ++e) {
      const unsigned w = av[e >> 1];
      const float a = (e & 1) ? bfhi(w) : bflo(w);
      const float o = a + bfr(gv[e] * bfr(b[e >> 2][e & 3]));
      tile[(e2 + e) * 72 + m2] = (unsigned short)(cvtpk(o, 0.f) & 0xffffu);
    }
    LBAR();
#pragma unroll
    for (int i = 0; i < 2; ++i) {
      const unsigned r = i * 32 + (tid >> 3), c = (tid & 7) * 8;
      *(u32x4*)(out + (size_t)(tb64 * 64 + r) * DM + mb64 * 64 + c) = *(const u32x4*)(tile + r * 72 + c);
    }
  }
}

extern "C" void kernel_launch(void* const* d_in, const int* in_sizes, int n_in, void* d_out, int out_size, void* d_ws, size_t ws_size, hipStream_t stream) {
  (void)in_sizes; (void)n_in; (void)out_size;
  const unsigned short* aq = (const unsigned short*)d_in[0];
  const unsigned short* ak = (const unsigned short*)d_in[1];
  const unsigned short* av = (const unsigned short*)d_in[2];
  const unsigned short* shk = (const unsigned short*)d_in[3];
  const unsigned short* ssk = (const unsigned short*)d_in[4];
  const unsigned short* ssd = (const unsigned short*)d_in[5];
  const char* ckv = (const char*)d_in[7];
  const char* wq = (const char*)d_in[8];
  const char* wuk = (const char*)d_in[9];
  const char* wuv = (const char*)d_in[10];
  const unsigned short* wo = (const unsigned short*)d_in[11];
  const unsigned short* gw = (const unsigned short*)d_in[12];
  char* ws = (char*)d_ws + WS_OFFSET(ws_size);
  (void)hipFuncSetAttribute((const void*)k1_kvproj, hipFuncAttributeMaxDynamicSharedMemorySize, K1_LDS);
  (void)hipFuncSetAttribute((const void*)k2_attn, hipFuncAttributeMaxDynamicSharedMemorySize, K2_LDS);
  k1_kvproj<<<256, 512, K1_LDS, stream>>>(ckv, wuk, wuv, (const char*)aq, (const char*)ak, (const char*)av, (const char*)shk, (const char*)gw, (const char*)wo, wq, (const char*)ssk, ws);
  k2_attn<<<256, 256, K2_LDS, stream>>>((const char*)aq, wq, ssk, ssd, ws, (unsigned short*)d_out);
}
```
